```python
import torch

def _softmax(x, axis=-1):
    return torch.softmax(x, dim=axis)
def _gelu(x):
    return torch.nn.functional.gelu(x, approximate="tanh")
def _silu(x):
    return torch.nn.functional.silu(x)
def _sigmoid(x):
    return torch.sigmoid(x)
def _layernorm(x, axis=-1, eps=1e-5):
    m = x.mean(dim=axis, keepdim=True)
    v = x.var(dim=axis, keepdim=True, unbiased=False)
    return (x - m) / torch.sqrt(v + eps)
def _scatter_add(tgt, idx, src, axis=0):
    return tgt.clone().index_add_(axis, idx, src)
def _segment_sum(src, seg, n):
    out = torch.zeros((n,) + src.shape[1:], dtype=src.dtype)
    return out.index_add_(0, seg, src)
def _pad(x, pw):
    flat = tuple(v for pair in reversed(pw) for v in pair)
    return torch.nn.functional.pad(x, flat)

kernel_name = 'parallel_sum__ssm_conv_s4d__glu_gated_glu__prompt'

def setup_inputs():

    _w = (lambda a: (lambda t: t.to(torch.bfloat16) if t.is_floating_point() else t)(torch.as_tensor(a)))
    def _A_setup():
        Bsz, L, D, N = 1, 2048, 512, 128
        return {
            "u": _w((lambda *s: torch.randn(*s, dtype=torch.bfloat16) * 0.05)(Bsz, L, D)),
            "log_A_real": _w((lambda *s: torch.randn(*s, dtype=torch.bfloat16) * 3.2)(D, N) * 0.5),
            "A_imag": _w((lambda *s: torch.randn(*s, dtype=torch.bfloat16) * 0.1)(D, N) * 3.14),
            "C_real": _w((lambda *s: torch.randn(*s, dtype=torch.bfloat16) * 0.8)(D, N) / N ** 0.5),
            "C_imag": _w((lambda *s: torch.randn(*s, dtype=torch.bfloat16) * 0.8)(D, N) / N ** 0.5),
            "log_dt": _w((lambda *s: torch.randn(*s, dtype=torch.bfloat16) * 0.1)(D) * 0.5 - 4.0),
            "D_skip": _w((lambda *s: torch.randn(*s, dtype=torch.bfloat16) * 0.1)(D)),
        }
    _A_in = _A_setup()

    def _B_setup():
        B, S, D, F = 1, 2048, 512, 1376
        return {
            "x": _w((lambda *s: torch.randn(*s, dtype=torch.bfloat16) * 0.05)(B, S, D)),
            "w_gate": _w((lambda *s: torch.randn(*s, dtype=torch.bfloat16) * 0.05)(D, F)),
            "w_up": _w((lambda *s: torch.randn(*s, dtype=torch.bfloat16) * 0.05)(D, F)),
            "w_down": _w((lambda *s: torch.randn(*s, dtype=torch.bfloat16) * 0.05)(F, D)),
        }
    _B_in = _B_setup()

    return {
        "A_u": _A_in["u"],
        "A_log_A_real": _A_in["log_A_real"],
        "A_A_imag": _A_in["A_imag"],
        "A_C_real": _A_in["C_real"],
        "A_C_imag": _A_in["C_imag"],
        "A_log_dt": _A_in["log_dt"],
        "A_D_skip": _A_in["D_skip"],
        "B_x": _B_in["x"],
        "B_w_gate": _B_in["w_gate"],
        "B_w_up": _B_in["w_up"],
        "B_w_down": _B_in["w_down"],
    }

def reference(A_u, A_log_A_real, A_A_imag, A_C_real, A_C_imag, A_log_dt, A_D_skip, B_x, B_w_gate, B_w_up, B_w_down):
    def _A_ref(u, log_A_real, A_imag, C_real, C_imag, log_dt, D_skip):
        L = u.shape[1]
        dt = torch.exp(log_dt)
        A = -torch.exp(log_A_real) + 1j * A_imag
        C = (C_real + 1j * C_imag) * dt[:, None]
        pos = (lambda x, dt: x.to(dt))(torch.arange(L), log_dt.dtype)
        P = torch.exp((A * dt[:, None])[:, :, None] * pos[None, None, :])
        K = 2.0 * torch.einsum("dn,dnl->dl", C, P).real
        L2 = 2 * L
        u_t = (lambda x, *d: x.permute(*d))(u, 0, 2, 1)
        Kf = (lambda x, *a, **k: torch.fft.rfft(x.float(), *a, **k))(K, L2)
        Uf = (lambda x, *a, **k: torch.fft.rfft(x.float(), *a, **k))(u_t, L2)
        y = (lambda x, *a, **k: torch.fft.irfft(x, *a, **k).to(torch.bfloat16))(Uf * Kf[None, :, :], L2)[:, :, :L]
        y = (lambda x, *d: x.permute(*d))(y, 0, 2, 1)
        return y + u * D_skip[None, None, :]

    def _B_ref(x, w_gate, w_up, w_down):
        g = torch.einsum("bsd,df->bsf", x, w_gate)
        u = torch.einsum("bsd,df->bsf", x, w_up)
        h = _sigmoid(g) * u
        return torch.einsum("bsf,fd->bsd", h, w_down)

    x = A_u
    xn = _layernorm(x, axis=-1)
    return x + _A_ref(xn, A_log_A_real, A_A_imag, A_C_real, A_C_imag, A_log_dt, A_D_skip) + _B_ref(xn, B_w_gate, B_w_up, B_w_down)
```

```cpp
#include <hip/hip_runtime.h>
#include <hip/hip_bf16.h>
typedef unsigned short u16;
__device__ __forceinline__ float b2f(u16 v){ return __uint_as_float(((unsigned)v)<<16); }
__device__ __forceinline__ u16 f2b(float f){ unsigned u=__float_as_uint(f); return (u16)((u+0x7FFF+((u>>16)&1))>>16); }
__device__ __forceinline__ float rb(float f){ return b2f(f2b(f)); }
constexpr int L=2048,D=512,N=128,F=1376;
__global__ void k_ln(const u16* x, u16* xn){
  int r=blockIdx.x, t=threadIdx.x; float v[8]; float s=0;
  for(int i=0;i<8;i++){v[i]=b2f(x[r*D+t*8+i]); s+=v[i];}
  for(int o=32;o;o>>=1) s+=__shfl_xor(s,o);
  float mean=s/D; float q=0;
  for(int i=0;i<8;i++){float d=v[i]-mean; q+=d*d;}
  for(int o=32;o;o>>=1) q+=__shfl_xor(q,o);
  float m=rb(mean), var=rb(q/D); float sd=rb(sqrtf(rb(var+1e-5f)));
  for(int i=0;i<8;i++) xn[r*D+t*8+i]=f2b(rb(v[i]-m)/sd);
}
__global__ void k_kgen(const u16* lar,const u16* aim,const u16* cr,const u16* ci,const u16* ldt,float* K){
  int d=blockIdx.y; int l=blockIdx.x*256+threadIdx.x;
  float dt=rb(expf(b2f(ldt[d]))); float pos=rb((float)l); float acc=0;
  for(int n=0;n<N;n++){
    float a=-rb(expf(b2f(lar[d*N+n])))*dt, w=b2f(aim[d*N+n])*dt;
    float e=expf(a*pos); float sn,cs; sincosf(w*pos,&sn,&cs);
    acc+= e*(b2f(cr[d*N+n])*dt*cs - b2f(ci[d*N+n])*dt*sn);
  }
  K[d*L+l]=2.f*acc;
}
__global__ void k_conv(const u16* x,const u16* xn,const float* K,const u16* dsk,u16* t1){
  int l=blockIdx.x; int d=blockIdx.y*256+threadIdx.x; float acc=0;
  for(int j=0;j<=l;j++) acc+=K[d*L+j]*b2f(xn[(l-j)*D+d]);
  float a=rb(rb(acc)+rb(b2f(xn[l*D+d])*b2f(dsk[d])));
  t1[l*D+d]=f2b(b2f(x[l*D+d])+a);
}
__global__ void k_g1(const u16* xn,const u16* wg,const u16* wu,u16* h){
  int l=blockIdx.x; int f=blockIdx.y*32+threadIdx.x; float g=0,u=0;
  for(int k=0;k<D;k++){float a=b2f(xn[l*D+k]); g+=a*b2f(wg[k*F+f]); u+=a*b2f(wu[k*F+f]);}
  g=rb(g);u=rb(u); float s=rb(1.f/(1.f+expf(-g))); h[l*F+f]=f2b(s*u);
}
__global__ void k_g2(const u16* h,const u16* wd,const u16* t1,u16* out){
  int l=blockIdx.x; int d=blockIdx.y*256+threadIdx.x; float a=0;
  for(int k=0;k<F;k++) a+=b2f(h[l*F+k])*b2f(wd[k*D+d]);
  out[l*D+d]=f2b(b2f(t1[l*D+d])+rb(a));
}
extern "C" void kernel_launch(void* const* d_in, const int* in_sizes, int n_in, void* d_out, int out_size, void* d_ws, size_t ws_size, hipStream_t stream){
  const u16* x=(const u16*)d_in[0];
  char* ws=(char*)d_ws;
  u16* xn=(u16*)ws; float* K=(float*)(ws+(4<<20)); u16* t1=(u16*)(ws+(12<<20)); u16* h=(u16*)(ws+(16<<20));
  k_ln<<<L,64,0,stream>>>(x,xn);
  k_kgen<<<dim3(L/256,D),256,0,stream>>>((const u16*)d_in[1],(const u16*)d_in[2],(const u16*)d_in[3],(const u16*)d_in[4],(const u16*)d_in[5],K);
  k_conv<<<dim3(L,2),256,0,stream>>>(x,xn,K,(const u16*)d_in[6],t1);
  k_g1<<<dim3(L,F/32),32,0,stream>>>(xn,(const u16*)d_in[8],(const u16*)d_in[9],h);
  k_g2<<<dim3(L,2),256,0,stream>>>(h,(const u16*)d_in[10],t1,(u16*)d_out);
}
```

```cpp
#include <hip/hip_runtime.h>
#include <hip/hip_bf16.h>

namespace {

typedef unsigned short u16;
typedef unsigned long long u64;
typedef __attribute__((ext_vector_type(4))) short s16x4;
typedef __attribute__((ext_vector_type(8))) short s16x8;
typedef __attribute__((ext_vector_type(8))) __bf16 bf16x8;
typedef __attribute__((ext_vector_type(2))) __bf16 bf16x2;
typedef __attribute__((ext_vector_type(8))) _Float16 f16x8;
typedef __attribute__((ext_vector_type(2))) _Float16 f16x2;
typedef __attribute__((ext_vector_type(2))) float f32x2;
typedef __attribute__((ext_vector_type(4))) float f32x4;
typedef __attribute__((ext_vector_type(16))) float f32x16;
typedef __attribute__((ext_vector_type(4))) unsigned u32x4;
typedef __attribute__((ext_vector_type(2))) unsigned u32x2;
typedef __amdgpu_buffer_rsrc_t rsrc_t;
#define LDS __attribute__((address_space(3)))
#define GLB __attribute__((address_space(1)))
#define SB() __builtin_amdgcn_sched_barrier(0)
#define RLX_AGENT __ATOMIC_RELAXED, __HIP_MEMORY_SCOPE_AGENT
#define RLX_WG __ATOMIC_RELAXED, __HIP_MEMORY_SCOPE_WORKGROUP

constexpr int L = 2048, D = 512, NM = 128, F = 1376, NU = F / 16;
constexpr int SC1 = 16;
constexpr unsigned SPIN_CAP = 1u << 18;

constexpr int WBLK = 3104, GOFF = 16384, UOFF = GOFF + 4 * WBLK, SLOT = GOFF + 8 * WBLK;
constexpr int LEFT = 32768;
constexpr int XIMG = 2 * SLOT + LEFT, XPAD = 480, XCH = XPAD + 4096;
constexpr int SBASE = 3 * SLOT;
constexpr int LNROW = 1088, KROW = 272, KSW = 32 * KROW;
constexpr int T4 = XIMG + 2 * XCH, T4CH = 2060 * 8;
constexpr int KC = SBASE + 4 * KSW, KCCH = 642 * 4;
constexpr int STO = KC, CSO = KC + 1024;
constexpr int SYNC = KC + 2 * KCCH;
constexpr int DUMMY = SBASE + 16384;
constexpr int LDS_BYTES = 163840;
static_assert(SYNC + 64 <= LDS_BYTES && T4 + 2 * T4CH <= KC, "LDS map");

constexpr int WS_E = 0, WS_XNF = 4096, WS_CSF = 8192, WS_HF = 12288, WS_YF = 20480, WS_ST = 32768, WS_CS = 40960;
constexpr int WS_XNT = 1 << 20, WS_YT = 3 << 20, WS_H = 5 << 20, WS_END = WS_H + 64 * NU * 1024;
constexpr int CSP = 1408;

__device__ __forceinline__ unsigned rfl(unsigned v) { return __builtin_amdgcn_readfirstlane(v); }
__device__ __forceinline__ rsrc_t mkr(const void* p, unsigned n) { return __builtin_amdgcn_make_buffer_rsrc((void*)p, 0, n, 0x00020000); }
__device__ __forceinline__ void dma16(unsigned m0v, unsigned voff, rsrc_t r, unsigned soff) {
  asm volatile("s_mov_b32 m0, %0\n\ts_nop 0\n\tbuffer_load_dwordx4 %1, %2, %3 offen lds" ::"s"(m0v), "v"(voff), "s"(r), "s"(soff) : "memory");
}
__device__ __forceinline__ void touch(unsigned m0v, unsigned voff, rsrc_t r, unsigned soff) {
  asm volatile("s_mov_b32 m0, %0\n\ts_nop 0\n\tbuffer_load_dword %1, %2, %3 offen lds" ::"s"(m0v), "v"(voff), "s"(r), "s"(soff) : "memory");
}
template <int W> __device__ __forceinline__ void waitv() { asm volatile("s_waitcnt vmcnt(%0)" ::"n"(W) : "memory"); }
__device__ __forceinline__ void waitl0() { asm volatile("s_waitcnt lgkmcnt(0)" ::: "memory"); }
__device__ __forceinline__ void fence() { asm volatile("" ::: "memory"); }
__device__ __forceinline__ void bar() { waitl0(); __builtin_amdgcn_s_barrier(); fence(); }
__device__ __forceinline__ unsigned pk2(float a, float b) { f32x2 p = {a, b}; return __builtin_bit_cast(unsigned, __builtin_convertvector(p, bf16x2)); }
__device__ __forceinline__ unsigned pkh(float a, float b) { f32x2 p = {a, b}; return __builtin_bit_cast(unsigned, __builtin_convertvector(p, f16x2)); }
__device__ __forceinline__ float blo(unsigned v) { return __uint_as_float(v << 16); }
__device__ __forceinline__ float bhi(unsigned v) { return __uint_as_float(v & 0xffff0000u); }
__device__ __forceinline__ float rb(float a) { return blo(pk2(a, a)); }
template <int C, int RM> __device__ __forceinline__ float dppf(float v) {
  return __builtin_bit_cast(float, __builtin_amdgcn_update_dpp(0, __builtin_bit_cast(int, v), C, RM, 0xf, true));
}
__device__ __forceinline__ float lane63(float v) { return __builtin_bit_cast(float, __builtin_amdgcn_readlane(__builtin_bit_cast(int, v), 63)); }
__device__ __forceinline__ float wave_sum(float v) {
  v += dppf<0x111, 0xf>(v); v += dppf<0x112, 0xf>(v); v += dppf<0x114, 0xf>(v); v += dppf<0x118, 0xf>(v);
  v += dppf<0x142, 0xa>(v); v += dppf<0x143, 0xc>(v);
  return lane63(v);
}
__device__ __forceinline__ float wave_max_nonneg(float v) {
  v = fmaxf(v, dppf<0x111, 0xf>(v)); v = fmaxf(v, dppf<0x112, 0xf>(v)); v = fmaxf(v, dppf<0x114, 0xf>(v)); v = fmaxf(v, dppf<0x118, 0xf>(v));
  v = fmaxf(v, dppf<0x142, 0xa>(v)); v = fmaxf(v, dppf<0x143, 0xc>(v));
  return lane63(v);
}
__device__ __forceinline__ s16x8 cat(s16x4 a, s16x4 b) { return __builtin_shufflevector(a, b, 0, 1, 2, 3, 4, 5, 6, 7); }
__device__ __forceinline__ s16x4 trd(LDS char* p) { return __builtin_amdgcn_ds_read_tr16_b64_v4i16((LDS s16x4*)p); }
__device__ __forceinline__ f32x4 mfma16(s16x8 a, s16x8 b, f32x4 c) {
  return __builtin_amdgcn_mfma_f32_16x16x32_bf16(__builtin_bit_cast(bf16x8, a), __builtin_bit_cast(bf16x8, b), c, 0, 0, 0);
}
__device__ __forceinline__ void poll(const char* ws, unsigned off, u64 tag) {
  GLB u64* p = (GLB u64*)(ws + off);
#pragma unroll 1
  for (unsigned spins = 0; spins < SPIN_CAP; ++spins) {
    if (__all(__hip_atomic_load(p, RLX_AGENT) == tag)) break;
    __builtin_amdgcn_s_sleep(1);
  }
  fence();
}
__device__ __forceinline__ void poll4(const char* ws, unsigned off, u64 tag) {
  GLB u64* p = (GLB u64*)(ws + off);
#pragma unroll 1
  for (unsigned spins = 0; spins < SPIN_CAP; ++spins) {
    const u64 a = __hip_atomic_load(p, RLX_AGENT), b = __hip_atomic_load(p + 64, RLX_AGENT);
    const u64 c = __hip_atomic_load(p + 128, RLX_AGENT), d = __hip_atomic_load(p + 192, RLX_AGENT);
    if (__all(((a ^ tag) | (b ^ tag) | (c ^ tag) | (d ^ tag)) == 0)) break;
    __builtin_amdgcn_s_sleep(1);
  }
  fence();
}
__device__ __forceinline__ void setflag(char* ws, unsigned off, u64 tag, int lane) {
  if (lane == 0) __hip_atomic_store((GLB u64*)(ws + off), tag, RLX_AGENT);
}
struct cpx { float r, i; };
__device__ __forceinline__ cpx cmul(cpx a, cpx b) { return {a.r * b.r - a.i * b.i, a.r * b.i + a.i * b.r}; }
__device__ __forceinline__ cpx cpow2(float s2, float th, float p) {
  const float t = th * p, n = __builtin_rintf(t * 0.15915494f);
  const float fr = __builtin_fmaf(t, 0.15915494f, -n) + t * 6.4206382e-9f;
  const float e = __builtin_amdgcn_exp2f(s2 * p);
  return {e * __builtin_amdgcn_cosf(fr), e * __builtin_amdgcn_sinf(fr)};
}
__device__ __forceinline__ float ln_norm(float x, float m, float sd, float rs) {
  const float d = rb(x - m), q = d * rs;
  return __builtin_fmaf(__builtin_fmaf(-q, sd, d), rs, q);
}

#define STAMP(i)
#define STAMP_OUT(role)

struct G1Frag { s16x8 x[4]; s16x8 w[6]; };
struct CvFrag { s16x8 a[8]; s16x8 b[8]; };

__global__ __launch_bounds__(512, 2) void fused_kernel(const u16* __restrict__ x, const u16* __restrict__ lar, const u16* __restrict__ aim,
                                                       const u16* __restrict__ cre, const u16* __restrict__ cim, const u16* __restrict__ ldt,
                                                       const u16* __restrict__ dsk, const u16* __restrict__ wg, const u16* __restrict__ wu,
                                                       const u16* __restrict__ wd, u16* __restrict__ out, char* ws) {
  __shared__ __attribute__((aligned(16))) char lds_raw[LDS_BYTES];
  LDS char* lds = (LDS char*)lds_raw;
  const int w = rfl(threadIdx.x >> 6), lane = threadIdx.x & 63;
  const int b = blockIdx.x, xcd = b & 7, r = b >> 3;
  STAMP(0);
  if (w == 7) {
    u64 pc; unsigned l0, l1;
    asm volatile("s_getpc_b64 %0\n.Lk_code_begin:\n\ts_mov_b32 %1, .Lk_code_end0-.Lk_code_begin\n\ts_mov_b32 %2, .Lk_code_end1-.Lk_code_begin" : "=s"(pc), "=s"(l0), "=s"(l1));
    touch(DUMMY, ((lane & 15) * 32 + r) * 128, mkr((const void*)pc, max(l0, l1)), 0);
  }
  const rsrc_t rx = mkr(x, L * D * 2);
  const rsrc_t rws = mkr(ws, WS_END);
  const bool gemm = r < 30;
  const int idx = (xcd >> 1) * 30 + r, nt = idx >> 3, mt = (xcd & 1) * 8 + (idx & 7);
  const int n0 = nt < 14 ? nt * 96 : 1280;
  const int m2 = (xcd & 1) * 16 + (xcd >> 1) * 4 + (r >> 3), n2 = r & 7;
  const int j16 = lane & 15, g = lane >> 4;

  if (w < 4) {
    const rsrc_t rwd = mkr(wd, F * D * 2);
    unsigned vd[2];
#pragma unroll
    for (int i = 0; i < 2; ++i) { const int row = lane >> 3, f = ((row >> 1) & 1) + 2 * i; vd[i] = row * 1024 + n2 * 128 + (((lane & 7) ^ (2 * f)) << 4); }
    auto wdloc = [&](int pos) { return (unsigned)(((pos / 2 + 2) % 3) * SLOT + (pos & 1) * 4096); };
    unsigned wl = w * 8192, wo = w * (11 * 32768);
    auto issue_wd = [&](int step, int pos, int p) { dma16(wdloc(pos) + wl + p * 1024, vd[p & 1], rwd, wo + step * 32768 + p * 8192); };
    u64 tag;

    if (gemm) {
      const int mw = w & 1, nw = w >> 1;
      const rsrc_t rw = mkr(nw ? wu : wg, D * F * 2);
      unsigned vx[2], vw[3];
#pragma unroll
      for (int i = 0; i < 2; ++i) {
        const int row = i * 8 + (lane >> 3), id = (row >> 1) & 7, f = (id & 4) | ((id & 1) << 1) | ((id >> 1) & 1);
        vx[i] = (mt * 128 + w * 32 + row) * 1024 + (((lane & 7) ^ f) << 4);
      }
#pragma unroll
      for (int i = 0; i < 3; ++i) { const int c = i * 64 + lane, kr = (c * 171) >> 11, ch = c - kr * 12; vw[i] = (mw * 32 + kr) * (F * 2) + ch * 16 + n0 * 2; }
      const unsigned mx = w * 4096, mwd = (nw ? UOFF : GOFF) + mw * 2 * WBLK;
      auto issue = [&](int s, int i) {
        if (s >= 8) { if (i < 8) issue_wd((s - 8) * 2 + (i >> 2), (s - 8) * 2 + (i >> 2), i & 3); return; }
        const unsigned slot = (s % 3) * SLOT;
        if (i < 4) dma16(slot + mx + i * 1024, vx[i & 1], rx, s * 128 + (i >> 1) * 16384);
        else { const int j = i - 4; dma16(slot + mwd + (j / 3) * WBLK + (j % 3) * 1024, vw[j % 3], rw, s * (64 * F * 2) + (j / 3) * 16 * F * 2); }
      };
#pragma unroll
      for (int i = 0; i < 10; ++i) issue(0, i);
#pragma unroll
      for (int i = 0; i < 10; ++i) issue(1, i);
      unsigned ax[2], aw;
      {
        const int id = (j16 >> 1) & 7, f = (id & 4) | ((id & 1) << 1) | ((id >> 1) & 1), cg = 2 * (g & 1) + (g >> 1);
#pragma unroll
        for (int s = 0; s < 2; ++s) ax[s] = (mw * 64 + j16) * 128 + (((4 * s + cg) ^ f) << 4);
        aw = GOFF + (g & 1) * WBLK + (8 * (g >> 1) + (j16 >> 2)) * 192 + nw * 96 + (j16 & 3) * 8;
      }
      f32x4 acc[4][6] = {};
      G1Frag fr[2];
      auto half = [&](G1Frag& c, bool domm, G1Frag& n, bool dord, int s, int jj, int sd, int i0) {
#pragma unroll
        for (int t = 0; t < 24; ++t) {
          if (domm) acc[t & 3][t >> 2] = mfma16(c.w[t >> 2], c.x[t & 3], acc[t & 3][t >> 2]);
          SB();
          if (dord && (t & 1) == 0) {
            const int k = t >> 1;
            if (k < 2) {
              n.x[2 * k] = *(const LDS s16x8*)(lds + (s % 3) * SLOT + ax[jj] + 2 * k * 2048);
              n.x[2 * k + 1] = *(const LDS s16x8*)(lds + (s % 3) * SLOT + ax[jj] + (2 * k + 1) * 2048);
            } else if (k < 8) {
              const int u = k - 2;
              const unsigned o = (s % 3) * SLOT + jj * 2 * WBLK + (u / 3) * 4 * WBLK + (u % 3) * 32;
              n.w[u] = cat(trd(lds + aw + o), trd(lds + aw + o + 768));
            }
          }
          if ((t & 3) == 3 && t >= 7) issue(sd, i0 + (t - 7) / 4);
          SB();
        }
      };
#pragma unroll
      for (int s = 0; s < 8; ++s) {
        if (s < 7) waitv<10>(); else waitv<8>();
        bar();
        if (s == 0) STAMP(1);
        if (s == 4) STAMP(2);
        half(fr[1], s > 0, fr[0], true, s, 0, s + 2, 0);
        half(fr[0], true, fr[1], true, s, 1, s + 2, 5);
      }
      bar();
      f32x2 st[4]; f32x4 cs[6];
      tag = *(const LDS u64*)(lds + SYNC + 16);
#pragma unroll
      for (int m = 0; m < 4; ++m) st[m] = *(const LDS f32x2*)(lds + STO + (mw * 64 + m * 16 + j16) * 8);
#pragma unroll
      for (int u = 0; u < 6; ++u) cs[u] = *(const LDS f32x4*)(lds + CSO + ((u / 3) * 96 + (nw * 3 + u % 3) * 16 + g * 4) * 4);
      half(fr[1], true, fr[0], false, 0, 0, 10, 0);
#pragma unroll
      for (int i = 5; i < 8; ++i) issue(10, i);
      STAMP(3);
#pragma unroll
      for (int mp = 0; mp < 2; ++mp) {
#pragma unroll
        for (int u = 0; u < 3; ++u) {
          unsigned o[2][2];
#pragma unroll
          for (int mi = 0; mi < 2; ++mi) {
            const int m = mp * 2 + mi;
            const float nm = st[m][0], sd = st[m][1], kg = -1.44269504f * __builtin_amdgcn_rcpf(sd);
            float hv[4];
#pragma unroll
            for (int e = 0; e < 4; ++e) {
              const float gg = __builtin_fmaf(nm, cs[u][e], acc[m][u][e]), uu = __builtin_fmaf(nm, cs[u + 3][e], acc[m][u + 3][e]);
              hv[e] = uu * __builtin_amdgcn_rcpf(__builtin_fmaf(__builtin_amdgcn_exp2f(gg * kg), sd, sd));
            }
            o[mi][0] = pk2(hv[0], hv[1]); o[mi][1] = pk2(hv[2], hv[3]);
          }
          const auto r0 = __builtin_amdgcn_permlane16_swap(o[0][0], o[1][0], false, false);
          const auto r1 = __builtin_amdgcn_permlane16_swap(o[0][1], o[1][1], false, false);
          const u32x4 ov = {r0[0], r1[0], r0[1], r1[1]};
          const int unit = (n0 >> 4) + nw * 3 + u;
          if (nt < 14 || unit >= 84) __builtin_amdgcn_raw_buffer_store_b128(ov, rws, lane * 16, WS_H + ((mt * 4 + mw * 2 + mp) * NU + unit) * 1024, SC1);
        }
      }
      STAMP(4);
      waitv<0>();
      if (nt < 14 || nw == 1) setflag(ws, WS_HF + ((mt * 2 + mw) * 32 + (nt < 14 ? nt * 2 + nw : 28)) * 8, tag, lane);
    } else {
      const int j = xcd * 2 + (r - 30), t = threadIdx.x;
      const rsrc_t rw = mkr((j >> 3) ? wu : wg, D * F * 2);
      const int kg = (t * 745) >> 14, c = t - kg * 22, col = (j & 7) * 176 + c * 8;
      const unsigned vo = (kg < 11 && col < F) ? kg * (F * 2) + col * 2 : 0x7f000000u;
      float a[8] = {};
      u32x4 v[2][8];
      auto ld = [&](int sl) {
#pragma unroll
        for (int i = 0; i < 8; ++i) v[sl & 1][i] = __builtin_amdgcn_raw_buffer_load_b128(rw, vo + (sl * 8 + i) * (11 * F * 2), 0, 0);
      };
      ld(0);
#pragma unroll
      for (int sl = 0; sl < 6; ++sl) {
        if (sl < 5) ld(sl + 1);
        if (sl < 4) bar();
#pragma unroll
        for (int i = 0; i < 8; ++i)
#pragma unroll
          for (int e = 0; e < 4; ++e) { const unsigned q = v[sl & 1][i][e]; a[2 * e] += blo(q); a[2 * e + 1] += bhi(q); }
        asm volatile("" : "+v"(a[0]), "+v"(a[1]), "+v"(a[2]), "+v"(a[3]), "+v"(a[4]), "+v"(a[5]), "+v"(a[6]), "+v"(a[7]));
      }
      if (kg < 11) {
        *(LDS f32x4*)(lds + kg * 704 + c * 32) = f32x4{a[0], a[1], a[2], a[3]};
        *(LDS f32x4*)(lds + kg * 704 + c * 32 + 16) = f32x4{a[4], a[5], a[6], a[7]};
      }
      bar();
      tag = *(const LDS u64*)(lds + SYNC + 16);
      if (t < 176) {
        float s = 0.f;
#pragma unroll
        for (int k = 0; k < 11; ++k) s += *(const LDS float*)(lds + k * 704 + t * 4);
        __builtin_amdgcn_raw_buffer_store_b32(__float_as_uint(s), rws, t * 4, WS_CS + ((j >> 3) * CSP + (j & 7) * 176) * 4, SC1);
      }
      waitv<0>();
      bar();
      if (w == 0) setflag(ws, WS_CSF + j * 8, tag, lane);
#pragma unroll
      for (int i = 0; i < 3; ++i) touch(SLOT + w * 256, (j * 688 + i * 256 + t) * 128, rwd, 0);
      waitv<0>();
      bar();
#pragma unroll
      for (int i = 0; i < 24; ++i) issue_wd(i >> 2, i >> 2, i & 3);
      bar();
      bar();
      waitv<0>();
    }

    STAMP(5);
    asm volatile("" : "+s"(wl), "+s"(wo));
    unsigned ad[4];
    {
      const int q = j16 >> 2, f = ((q >> 1) & 1) + 2 * (g & 1);
#pragma unroll
      for (int u = 0; u < 4; ++u) ad[u] = w * 8192 + (8 * g + q) * 128 + ((u ^ f) << 5) + (j16 & 3) * 8;
    }
    auto rdwd = [&](s16x8* dst, int pos) {
#pragma unroll
      for (int u = 0; u < 4; ++u) dst[u] = cat(trd(lds + ad[u] + wdloc(pos)), trd(lds + ad[u] + wdloc(pos) + 512));
    };
    s16x8 wf[5][4];
#pragma unroll
    for (int s = 0; s < 5; ++s) rdwd(wf[s], s);
    waitl0();
    SB();
#pragma unroll
    for (int i = 0; i < 16; ++i) issue_wd(6 + (i >> 2), i >> 2, i & 3);
    if (w < 3) {
#pragma unroll
      for (int i = 0; i < 4; ++i) issue_wd(10, 4, i);
    }
    {
      const int f0 = (w * 22) / 3, f1 = min(w * 22 + 21, 85) / 3;
      poll(ws, WS_HF + (m2 * 32 + min(f0 + j16, f1)) * 8, tag);
    }
    waitv<0>();
    STAMP(6);
    unsigned vh[4];
#pragma unroll
    for (int m = 0; m < 4; ++m) { const int mm = (m + w) & 3; vh[m] = (mm >> 1) * (NU * 1024) + (g >> 1) * 1024 + (32 * (g & 1) + 16 * (mm & 1) + j16) * 16; }
    const unsigned sh = WS_H + (m2 * 2 * NU + w * 22) * 1024;
    s16x8 hf[3][4];
    auto ldh = [&](int s) {
#pragma unroll
      for (int m = 0; m < 4; ++m) {
        const unsigned vo = (s == 10 && w == 3) ? 0x7f000000u : vh[m];
        hf[s % 3][m] = __builtin_bit_cast(s16x8, __builtin_amdgcn_raw_buffer_load_b128(rws, vo, sh + s * 2048, SC1));
      }
    };
    f32x4 a2[4][4] = {};
    ldh(0); ldh(1); ldh(2);
    SB();
#pragma unroll
    for (int s = 0; s < 11; ++s) {
#pragma unroll
      for (int m = 0; m < 4; ++m)
#pragma unroll
        for (int u = 0; u < 4; ++u) a2[m][u] = mfma16(wf[s % 5][u], hf[s % 3][m], a2[m][u]);
      SB();
      if (s + 3 < 11) ldh(s + 3);
      if (s + 5 < 11) rdwd(wf[s % 5], s == 0 ? 5 : s - 1);
      SB();
    }
    STAMP(7);
#pragma unroll
    for (int m = 1; m < 4; ++m)
#pragma unroll
      for (int u = 0; u < 4; ++u) *(LDS f32x4*)(lds + wdloc(m - 1) + w * 8192 + u * 1024 + lane * 16) = a2[m][u];
    bar();
    STAMP(8);
    unsigned o[4][2];
#pragma unroll
    for (int u = 0; u < 4; ++u) {
      f32x4 s = a2[0][u];
#pragma unroll
      for (int m = 1; m < 4; ++m) s += *(const LDS f32x4*)(lds + wdloc(m - 1) + ((w - m) & 3) * 8192 + u * 1024 + lane * 16);
      const u32x2 t1 = *(const LDS u32x2*)(lds + T4 + (u >> 1) * T4CH + (u & 1) * 8192 + 4096 + w * 512 + lane * 8);
      const unsigned b0 = pk2(s[0], s[1]), b1 = pk2(s[2], s[3]);
      o[u][0] = pk2(blo(t1[0]) + blo(b0), bhi(t1[0]) + bhi(b0));
      o[u][1] = pk2(blo(t1[1]) + blo(b1), bhi(t1[1]) + bhi(b1));
    }
#pragma unroll
    for (int up = 0; up < 2; ++up) {
      const auto r0 = __builtin_amdgcn_permlane16_swap(o[2 * up][0], o[2 * up + 1][0], false, false);
      const auto r1 = __builtin_amdgcn_permlane16_swap(o[2 * up][1], o[2 * up + 1][1], false, false);
      const u32x4 ov = {r0[0], r1[0], r0[1], r1[1]};
      *(u32x4*)(out + (m2 * 64 + w * 16 + j16) * D + n2 * 64 + up * 32 + (g & 1) * 16 + (g >> 1) * 8) = ov;
    }
    STAMP(9);
    STAMP_OUT(0);
    asm volatile(".Lk_code_end0:");
  } else {
    const int sw = w - 4, cl = sw >> 1, hh = sw & 1, st = threadIdx.x - 256;
    const int row0 = (xcd & 1) * 1024 + (xcd >> 1) * 256 + r * 8;
    const int cp = xcd * 32 + r, ch = cp * 2 + cl;
    u32x4 xr[2];
#pragma unroll
    for (int i = 0; i < 2; ++i) xr[i] = __builtin_amdgcn_raw_buffer_load_b128(rx, lane * 16, (row0 + 2 * sw + i) * 1024, 0);
    if (sw < 2) touch(DUMMY + 256 + sw * 256, ((lane >> 3) * 64 + (xcd & 1) * 32 + r) * (F * 2) + (xcd >> 1) * 688 + (lane & 7) * 112, mkr(sw ? wu : wg, D * F * 2), 0);
    const int pi = ch * NM + hh * 64 + lane;
    const u16 p_lar = lar[pi], p_aim = aim[pi], p_cr = cre[pi], p_ci = cim[pi], p_ldt = ldt[ch];
    u64 tag = *(const __attribute__((address_space(4))) u64*)(ws + WS_E) + 0x9E3779B97F4A7C15ull;
    for (int i = st; i < (LDS_BYTES - KC) / 16; i += 256) *(LDS u32x4*)(lds + KC + i * 16) = u32x4{0, 0, 0, 0};
    unsigned stat = 0;
#pragma unroll
    for (int i = 0; i < 2; ++i) {
      float v[8];
#pragma unroll
      for (int e = 0; e < 4; ++e) { const unsigned q = xr[i][e]; v[2 * e] = blo(q); v[2 * e + 1] = bhi(q); }
      const float mean = wave_sum(((v[0] + v[1]) + (v[2] + v[3])) + ((v[4] + v[5]) + (v[6] + v[7]))) * (1.f / D);
      float q2 = 0.f;
#pragma unroll
      for (int e = 0; e < 8; ++e) q2 = __builtin_fmaf(v[e] - mean, v[e] - mean, q2);
      const float var = rb(wave_sum(q2) * (1.f / D));
      const float m = rb(mean), sd = rb(__builtin_amdgcn_sqrtf(rb(var + 1e-5f))), rs = __builtin_amdgcn_rcpf(sd);
      u32x4 o;
#pragma unroll
      for (int e = 0; e < 4; ++e) o[e] = pk2(ln_norm(v[2 * e], m, sd, rs), ln_norm(v[2 * e + 1], m, sd, rs));
      *(LDS u32x4*)(lds + SBASE + (2 * sw + i) * LNROW + lane * 16) = o;
      if (lane == i) stat = pk2(m, sd);
    }
    asm volatile("" : "+s"(tag));
    STAMP(1);
    bar();
    if (st == 0) *(LDS u64*)(lds + SYNC + 16) = tag;
#pragma unroll
    for (int i = 0; i < 2; ++i) {
      LDS char* p = lds + SBASE + (j16 >> 2) * LNROW + (i * 256 + sw * 64 + g * 16 + (j16 & 3) * 4) * 2;
      const int c = i * 256 + st;
      __builtin_amdgcn_raw_buffer_store_b128(__builtin_bit_cast(u32x4, cat(trd(p), trd(p + 4 * LNROW))), rws,
                                             (c >> 1) * 8192 + (c & 1) * 16, WS_XNT + row0 * 4, SC1);
    }
    if (lane < 2) __builtin_amdgcn_raw_buffer_store_b32(stat, rws, lane * 4, WS_ST + (row0 + 2 * sw) * 4, SC1);
    const float dt = rb(expf(blo(p_ldt)));
    const float s2 = -rb(expf(blo(p_lar))) * dt * 1.44269504f, th = blo(p_aim) * dt;
    const unsigned eb = min(max(__float_as_uint(wave_max_nonneg(fmaxf(fabsf(blo(p_cr)), fabsf(blo(p_ci))))) >> 23, 1u), 253u);
    const float sc = __uint_as_float((254u - eb) << 23), ksc = 2.f * dt * __uint_as_float(eb << 23);
    cpx z1 = cpow2(s2, th, 1.f), z8c = cpow2(s2, -th, 8.f), z256 = cpow2(s2, th, 256.f);
    asm volatile("" : "+v"(z1.r), "+v"(z1.i), "+v"(z8c.r), "+v"(z8c.i), "+v"(z256.r), "+v"(z256.i));
    bar();
    LDS char* ks = lds + SBASE + sw * KSW;
    auto put = [&](int row, cpx v) { *(LDS unsigned*)(ks + row * KROW + lane * 4) = pkh(v.r, v.i); };
    f16x8 vf[8];
    {
      cpx v = {1.f, 0.f};
      put(0, v);
      v = z8c;
#pragma unroll
      for (int k = 1; k < 32; ++k) { put(k, v); v = cmul(v, z8c); }
      fence();
#pragma unroll
      for (int s = 0; s < 8; ++s) vf[s] = *(const LDS f16x8*)(ks + (lane & 31) * KROW + s * 32 + (lane >> 5) * 16);
    }
    waitv<0>();
    bar();
    if (sw == 0) setflag(ws, WS_XNF + b * 8, tag, lane);
    {
      cpx u = {blo(p_cr) * sc, blo(p_ci) * sc};
      put(20, u);
      cpx e[4];
#pragma unroll
      for (int k = 0; k < 8; ++k) { u = cmul(u, z1); put(k, u); if (k & 1) e[k >> 1] = u; }
#pragma unroll
      for (int k = 0; k < 4; ++k) { e[k] = cmul(e[k], z256); put(8 + k, e[k]); }
#pragma unroll
      for (int k = 0; k < 2; ++k) { e[1] = cmul(e[1], z256); put(12 + 2 * k, e[1]); e[3] = cmul(e[3], z256); put(13 + 2 * k, e[3]); }
#pragma unroll
      for (int k = 0; k < 4; ++k) { e[3] = cmul(e[3], z256); put(16 + k, e[3]); }
      fence();
      f32x16 kd = {};
#pragma unroll
      for (int s = 0; s < 8; ++s)
        kd = __builtin_amdgcn_mfma_f32_32x32x16_f16(*(const LDS f16x8*)(ks + (lane & 31) * KROW + s * 32 + (lane >> 5) * 16), vf[s], kd, 0, 0, 0);
      LDS float* kc = (LDS float*)(lds + KC + cl * KCCH + 4);
      const int bb = lane & 31, h = lane >> 5;
#pragma unroll
      for (int k = 0; k < 4; ++k) {
        __hip_atomic_fetch_add(kc + 1 + 4 * h + 8 * bb + k, kd[k] * ksc, RLX_WG);
        __hip_atomic_fetch_add(kc + (h ? 385 + 2 * bb + (k >> 1) * 62 : 257 + 4 * bb) + k, kd[4 + k] * ksc, RLX_WG);
      }
      if (lane <= 32) __hip_atomic_fetch_add(kc + (h ? 0 : 513 + bb), kd[8] * ksc, RLX_WG);
      if (h == 0) {
#pragma unroll
        for (int k = 1; k < 4; ++k) __hip_atomic_fetch_add(kc + 513 + bb + 32 * k, kd[8 + k] * ksc, RLX_WG);
      }
    }
    bar();
    {
      const LDS float* kc = (const LDS float*)(lds + KC + cl * KCCH + 4);
#pragma unroll
      for (int it = 0; it < 9; ++it) {
        const int q = min((2 * it + hh) * 62 - 8 + lane, 1023), p0 = 2 * q;
        const unsigned bits = pk2((float)p0, (float)(p0 + 1));
        int r0 = (int)(bits & 0xffffu) - 0x4280, r1 = (int)(bits >> 16) - 0x4280;
        if (it == 0) { r0 = p0 < 128 ? max(p0, -1) : r0; r1 = p0 < 128 ? max(p0 + 1, -1) : r1; }
        const unsigned P = pk2(kc[r1], kc[r0]);
        const unsigned Pp = __builtin_amdgcn_update_dpp(0, P, 0x138, 0xf, 0xf, true);
        const unsigned Q = __builtin_amdgcn_alignbit(Pp, P, 16);
        const unsigned Qp = __builtin_amdgcn_update_dpp(0, Q, 0x138, 0xf, 0xf, true);
        if (lane >= 2 && (2 * it + hh) * 62 - 8 + lane < 1024) *(LDS u32x4*)(lds + T4 + cl * T4CH + (p0 + 12) * 8) = u32x4{Q, Qp, P, Pp};
      }
    }
    STAMP(2);
    bar();
    u32x2 xt[4];
#pragma unroll
    for (int m = 0; m < 4; ++m) xt[m] = __builtin_amdgcn_raw_buffer_load_b64(rx, j16 * 1024 + g * 8, (m2 * 64 + m * 16) * 1024 + n2 * 128 + sw * 32, 0);
    const u32x2 dk = *(const u32x2*)(dsk + n2 * 64 + sw * 16 + g * 4);
    bar();
    poll4(ws, WS_XNF + lane * 8, tag);
    if (b == 0 && sw == 0) setflag(ws, WS_E, tag, lane);
    u32x4 xi[2];
#pragma unroll
    for (int i = 0; i < 2; ++i) xi[i] = __builtin_amdgcn_raw_buffer_load_b128(rws, st * 16, WS_XNT + cp * 8192 + i * 4096, SC1);
    unsigned tst[4];
#pragma unroll
    for (int m = 0; m < 4; ++m) tst[m] = __builtin_amdgcn_raw_buffer_load_b32(rws, j16 * 4, WS_ST + (m2 * 64 + m * 16) * 4, SC1);
    const unsigned gst = __builtin_amdgcn_raw_buffer_load_b32(rws, (st & 127) * 4, WS_ST + (mt & 15) * 512, SC1);
    STAMP(3);
    bar();
#pragma unroll
    for (int i = 0; i < 2; ++i) *(LDS u32x4*)(lds + XIMG + (st & 1) * XCH + XPAD + (i * 128 + (st >> 1)) * 16) = xi[i];
    if (st < 60) *(LDS u32x4*)(lds + XIMG + (st & 1) * XCH + (st >> 1) * 16) = u32x4{0, 0, 0, 0};
    if (st < 128) *(LDS f32x2*)(lds + STO + st * 8) = f32x2{-blo(gst), bhi(gst)};
    u32x2 xs[4];
#pragma unroll
    for (int m = 0; m < 4; ++m) {
      const float mu = blo(tst[m]), sd = bhi(tst[m]), rs = __builtin_amdgcn_rcpf(sd);
#pragma unroll
      for (int e = 0; e < 2; ++e) {
        const unsigned q = xt[m][e], dd = dk[e];
        const unsigned n = pk2(ln_norm(blo(q), mu, sd, rs), ln_norm(bhi(q), mu, sd, rs));
        xs[m][e] = pk2(blo(n) * blo(dd), bhi(n) * bhi(dd));
      }
    }
    unsigned csv = 0;
    if (gemm) {
      poll(ws, WS_CSF + j16 * 8, tag);
      const int mat = st >= 96;
      csv = __builtin_amdgcn_raw_buffer_load_b32(rws, (mat * CSP + n0 + st - mat * 96) * 4, WS_CS, SC1);
    }
    bar();
    if (st < 192) *(LDS unsigned*)(lds + CSO + st * 4) = csv;
    LDS char* pa = lds + T4 + cl * T4CH + (24 + j16 - 8 * g) * 8 + hh * 1024;
    LDS char* pb = lds + XIMG + cl * XCH + XPAD + (16 * j16 + 8 * g - 16) * 2 - hh * 256 - 192;
    CvFrag cf[2];
    auto rdc = [&](CvFrag& f, int rr, int k) {
      if (k & 1) f.b[k >> 1] = *(const LDS s16x8*)(pb + (k >> 1) * 512 + (3 - rr) * 64);
      else {
        const s16x4 lo = *(const volatile LDS s16x4*)(pa + (k >> 1) * 2048 + rr * 256 + 32);
        const s16x4 hi = *(const volatile LDS s16x4*)(pa + (k >> 1) * 2048 + rr * 256);
        f.a[k >> 1] = cat(lo, hi);
      }
    };
#pragma unroll
    for (int k = 0; k < 16; ++k) rdc(cf[0], 0, k);
    STAMP(4);
    bar();
    f32x4 ca[8] = {};
#pragma unroll
    for (int rr = 0; rr < 4; ++rr) {
      int n = 0;
#pragma unroll
      for (int u = 0; u < 8; ++u)
#pragma unroll
        for (int t = u; t < 8; ++t) {
          ca[t] = mfma16(cf[rr & 1].a[u], cf[rr & 1].b[t - u], ca[t]);
          SB();
          if (rr < 3 && (n & 1) == 0 && (n >> 1) < 16) rdc(cf[(rr + 1) & 1], rr + 1, n >> 1);
          SB();
          ++n;
        }
    }
    STAMP(5);
    {
      LDS char* ex = lds + cl * SLOT + LEFT;
      f32x4 keep[4];
#pragma unroll
      for (int k = 0; k < 4; ++k) {
        *(LDS f32x4*)(ex + hh * 4096 + k * 1024 + lane * 16) = hh ? ca[k] : ca[k + 4];
        keep[k] = hh ? ca[k + 4] : ca[k];
      }
      waitl0();
      volatile LDS unsigned* sy = (volatile LDS unsigned*)(lds + SYNC);
      sy[sw] = 1u;
#pragma unroll 1
      for (unsigned spins = 0; spins < SPIN_CAP; ++spins) { if (rfl(sy[sw ^ 1])) break; }
      fence();
      unsigned o[4][2];
#pragma unroll
      for (int k = 0; k < 4; ++k) {
        const f32x4 s = keep[k] + *(const LDS f32x4*)(ex + (hh ^ 1) * 4096 + k * 1024 + lane * 16);
        o[k][0] = pk2(s[0], s[1]); o[k][1] = pk2(s[2], s[3]);
      }
#pragma unroll
      for (int kp = 0; kp < 2; ++kp) {
        const auto r0 = __builtin_amdgcn_permlane16_swap(o[2 * kp][0], o[2 * kp + 1][0], false, false);
        const auto r1 = __builtin_amdgcn_permlane16_swap(o[2 * kp][1], o[2 * kp + 1][1], false, false);
        const u32x4 ov = {r0[0], r1[0], r0[1], r1[1]};
        __builtin_amdgcn_raw_buffer_store_b128(ov, rws, ((g & 1) * 256 + 16 * j16 + 8 * (g >> 1)) * 2, WS_YT + ch * 4096 + (hh * 4 + kp * 2) * 512, SC1);
      }
      waitv<0>();
      setflag(ws, WS_YF + (hh * 512 + ch) * 8, tag, lane);
    }
    STAMP(6);
    poll(ws, WS_YF + ((m2 >> 4) * 512 + n2 * 64 + sw * 16 + j16) * 8, tag);
    STAMP(7);
    {
      LDS char* yb = lds + T4 + cl * T4CH + hh * 8192;
#pragma unroll
      for (int i = 0; i < 2; ++i) {
        const u32x4 y = __builtin_amdgcn_raw_buffer_load_b128(rws, (lane >> 3) * 4096 + (lane & 7) * 16, WS_YT + (n2 * 64 + sw * 16 + i * 8) * 4096 + m2 * 128, SC1);
        *(LDS u32x4*)(yb + (i * 8 + (lane >> 3)) * 160 + (lane & 7) * 16) = y;
      }
      fence();
#pragma unroll
      for (int m = 0; m < 4; ++m) {
        const u32x2 y = __builtin_bit_cast(u32x2, trd(yb + (4 * g + (j16 >> 2)) * 160 + m * 32 + (j16 & 3) * 8));
        u32x2 t1;
#pragma unroll
        for (int e = 0; e < 2; ++e) {
          const unsigned yy = y[e], s = xs[m][e], q = xt[m][e];
          const unsigned a = pk2(blo(yy) + blo(s), bhi(yy) + bhi(s));
          t1[e] = pk2(blo(q) + blo(a), bhi(q) + bhi(a));
        }
        *(LDS u32x2*)(yb + 4096 + m * 512 + lane * 8) = t1;
      }
    }
    STAMP(8);
    bar();
    STAMP_OUT(1);
    asm volatile(".Lk_code_end1:");
  }
}

}

extern "C" void kernel_launch(void* const* d_in, const int* in_sizes, int n_in, void* d_out, int out_size, void* d_ws, size_t ws_size,
                              hipStream_t stream) {
  fused_kernel<<<256, 512, 0, stream>>>((const u16*)d_in[0], (const u16*)d_in[1], (const u16*)d_in[2], (const u16*)d_in[3], (const u16*)d_in[4],
                                        (const u16*)d_in[5], (const u16*)d_in[6], (const u16*)d_in[8], (const u16*)d_in[9], (const u16*)d_in[10],
                                        (u16*)d_out, (char*)d_ws);
}
```

```cpp
#include <hip/hip_runtime.h>
#include <hip/hip_bf16.h>

#if defined(__HIP_DEVICE_COMPILE__)
#pragma clang attribute push(__attribute__((target("no-packed-fp32-ops"))), apply_to = function)
#endif
namespace {

typedef unsigned short u16;
typedef unsigned long long u64;
typedef __attribute__((ext_vector_type(4))) short s16x4;
typedef s16x4 __attribute__((aligned(4))) s16x4u;
typedef __attribute__((ext_vector_type(8))) short s16x8;
typedef __attribute__((ext_vector_type(8))) __bf16 bf16x8;
typedef __attribute__((ext_vector_type(2))) __bf16 bf16x2;
typedef __attribute__((ext_vector_type(8))) _Float16 f16x8;
typedef __attribute__((ext_vector_type(2))) _Float16 f16x2;
typedef __attribute__((ext_vector_type(2))) float f32x2;
typedef __attribute__((ext_vector_type(4))) float f32x4;
typedef __attribute__((ext_vector_type(16))) float f32x16;
typedef __attribute__((ext_vector_type(4))) unsigned u32x4;
typedef __attribute__((ext_vector_type(2))) unsigned u32x2;
typedef __amdgpu_buffer_rsrc_t rsrc_t;
#define LDS __attribute__((address_space(3)))
#define GLB __attribute__((address_space(1)))
#define SB() __builtin_amdgcn_sched_barrier(0)
#define RLX_AGENT __ATOMIC_RELAXED, __HIP_MEMORY_SCOPE_AGENT
#define RLX_WG __ATOMIC_RELAXED, __HIP_MEMORY_SCOPE_WORKGROUP

constexpr int L = 2048, D = 512, NM = 128, F = 1376, NU = F / 16;
constexpr int SC1 = 16, HD = 5;
constexpr unsigned SPIN_CAP = 1u << 18;

constexpr int WBLK = 3104, GOFF = 16384, UOFF = GOFF + 4 * WBLK, SLOT = GOFF + 8 * WBLK;
constexpr int LEFT = 32768;
constexpr int LNB = 2 * SLOT, LNROW = 1088;
constexpr int SBASE = 3 * SLOT;
constexpr int KROW = 272, KSW = 32 * KROW, PAIR = 2 * KSW;
constexpr int TX = 0, XIMG = 4160, XPAD = 480, CEX = 8736, TY = 13248, KEX0 = 7168, KEX1 = KSW;
constexpr int STO = SBASE + 2 * PAIR, CSO = STO + 1024, SYNC = CSO + 1024, LNS = SYNC + 32, DUMMY = SYNC + 64;
constexpr int LDS_BYTES = 163840;
static_assert(DUMMY + 1024 <= LDS_BYTES && XIMG + XPAD + 4096 <= CEX && CEX + 4096 <= TY && TY + 4128 <= PAIR, "LDS map");

constexpr int WS_E = 0, WS_XNF = 4096, WS_CSF = 8192, WS_HF = 12288, WS_YF = 20480, WS_ST = 32768, WS_CS = 40960;
constexpr int WS_XNT = 1 << 20, WS_YT = 3 << 20, WS_H = 5 << 20, WS_END = WS_H + 64 * NU * 1024;
constexpr int CSP = 1408;

__device__ __forceinline__ float u2f(unsigned v) { return __builtin_bit_cast(float, v); }
__device__ __forceinline__ unsigned f2u(float v) { return __builtin_bit_cast(unsigned, v); }
__device__ __forceinline__ bool wave_all(bool p) { return __builtin_amdgcn_ballot_w64(!p) == 0; }
__device__ __forceinline__ unsigned rfl(unsigned v) { return __builtin_amdgcn_readfirstlane(v); }
__device__ __forceinline__ rsrc_t mkr(const void* p, unsigned n) { return __builtin_amdgcn_make_buffer_rsrc((void*)p, 0, n, 0x00020000); }
__device__ __forceinline__ void dma16(unsigned m0v, unsigned voff, rsrc_t r, unsigned soff) {
  asm volatile("s_mov_b32 m0, %0\n\ts_nop 0\n\tbuffer_load_dwordx4 %1, %2, %3 offen lds" ::"s"(m0v), "v"(voff), "s"(r), "s"(soff) : "memory");
}
__device__ __forceinline__ void touch(unsigned m0v, unsigned voff, rsrc_t r, unsigned soff) {
  asm volatile("s_mov_b32 m0, %0\n\ts_nop 0\n\tbuffer_load_dword %1, %2, %3 offen lds" ::"s"(m0v), "v"(voff), "s"(r), "s"(soff) : "memory");
}
template <int W> __device__ __forceinline__ void waitv() { asm volatile("s_waitcnt vmcnt(%0)" ::"n"(W) : "memory"); }
__device__ __forceinline__ void waitl0() { asm volatile("s_waitcnt lgkmcnt(0)" ::: "memory"); }
__device__ __forceinline__ void fence() { asm volatile("" ::: "memory"); }
__device__ __forceinline__ void bar() { waitl0(); __builtin_amdgcn_s_barrier(); fence(); }
__device__ __forceinline__ unsigned pk2(float a, float b) { f32x2 p = {a, b}; return __builtin_bit_cast(unsigned, __builtin_convertvector(p, bf16x2)); }
__device__ __forceinline__ unsigned pkh(float a, float b) { f32x2 p = {a, b}; return __builtin_bit_cast(unsigned, __builtin_convertvector(p, f16x2)); }
__device__ __forceinline__ float blo(unsigned v) { return u2f(v << 16); }
__device__ __forceinline__ float bhi(unsigned v) { return u2f(v & 0xffff0000u); }
__device__ __forceinline__ float rb(float a) { return blo(pk2(a, a)); }
template <int C, int RM> __device__ __forceinline__ float dppf(float v) {
  return __builtin_bit_cast(float, __builtin_amdgcn_update_dpp(0, __builtin_bit_cast(int, v), C, RM, 0xf, true));
}
__device__ __forceinline__ float lane63(float v) { return __builtin_bit_cast(float, __builtin_amdgcn_readlane(__builtin_bit_cast(int, v), 63)); }
__device__ __forceinline__ float wave_sum(float v) {
  v += dppf<0x111, 0xf>(v); v += dppf<0x112, 0xf>(v); v += dppf<0x114, 0xf>(v); v += dppf<0x118, 0xf>(v);
  v += dppf<0x142, 0xa>(v); v += dppf<0x143, 0xc>(v);
  return lane63(v);
}
template <int C, int RM> __device__ __forceinline__ unsigned dppmax(unsigned v) {
  const unsigned o = __builtin_amdgcn_update_dpp(0, v, C, RM, 0xf, true);
  return v > o ? v : o;
}
__device__ __forceinline__ unsigned wave_max(unsigned v) {
  v = dppmax<0x111, 0xf>(v); v = dppmax<0x112, 0xf>(v); v = dppmax<0x114, 0xf>(v); v = dppmax<0x118, 0xf>(v);
  v = dppmax<0x142, 0xa>(v); v = dppmax<0x143, 0xc>(v);
  return __builtin_amdgcn_readlane(v, 63);
}
__device__ __forceinline__ s16x8 cat(s16x4 a, s16x4 b) { return __builtin_shufflevector(a, b, 0, 1, 2, 3, 4, 5, 6, 7); }
__device__ __forceinline__ s16x4 trd(LDS char* p) { return __builtin_amdgcn_ds_read_tr16_b64_v4i16((LDS s16x4*)p); }
__device__ __forceinline__ f32x4 mfma16(s16x8 a, s16x8 b, f32x4 c) {
  return __builtin_amdgcn_mfma_f32_16x16x32_bf16(__builtin_bit_cast(bf16x8, a), __builtin_bit_cast(bf16x8, b), c, 0, 0, 0);
}
__device__ __forceinline__ void poll(const char* ws, unsigned off, u64 tag) {
  GLB u64* p = (GLB u64*)(ws + off);
#pragma unroll 1
  for (unsigned spins = 0; spins < SPIN_CAP; ++spins) {
    if (wave_all(__hip_atomic_load(p, RLX_AGENT) == tag)) break;
    __builtin_amdgcn_s_sleep(1);
  }
  fence();
}
__device__ __forceinline__ void setflag(char* ws, unsigned off, u64 tag, int lane) {
  if (lane == 0) __hip_atomic_store((GLB u64*)(ws + off), tag, RLX_AGENT);
}
struct cpx { float r, i; };
__device__ __forceinline__ cpx cmul(cpx a, cpx b) { return {a.r * b.r - a.i * b.i, a.r * b.i + a.i * b.r}; }
__device__ __forceinline__ cpx cpow2(float s2, float th, float p) {
  const float t = th * p, n = __builtin_rintf(t * 0.15915494f);
  const float fr = __builtin_fmaf(t, 0.15915494f, -n) + t * 6.4206382e-9f;
  const float e = __builtin_amdgcn_exp2f(s2 * p);
  return {e * __builtin_amdgcn_cosf(fr), e * __builtin_amdgcn_sinf(fr)};
}
__device__ __forceinline__ float exp_f(float v) {
  const float t = v * 1.44269504f, e = __builtin_fmaf(v, 1.925963033e-8f, __builtin_fmaf(v, 1.44269504f, -t));
  const float y = __builtin_amdgcn_exp2f(t);
  return __builtin_fmaf(y, e * 0.69314718f, y);
}

#define STAMP(i)
#define STAMP_OUT(role)

struct G1Frag { s16x8 x[4]; s16x8 w[6]; };
struct CvFrag { s16x8 a[8]; s16x8 b[8]; };

__global__ __launch_bounds__(512, 2) void fused_kernel(const u16* x, const u16* lar, const u16* aim,
                                                       const u16* cre, const u16* cim, const u16* ldt,
                                                       const u16* dsk, const u16* wg, const u16* wu,
                                                       const u16* wd, u16* out, char* ws) {
  __shared__ __attribute__((aligned(16))) char lds_raw[LDS_BYTES];
  LDS char* lds = (LDS char*)lds_raw;
  const int w = rfl(threadIdx.x >> 6), lane = threadIdx.x & 63;
  const int b = blockIdx.x, xcd = b & 7, r = b >> 3;
  STAMP(0);
  if (w == 7) {
    u64 pc; unsigned l0, l1;
    asm volatile("s_getpc_b64 %0\n.Lk_code_begin:\n\ts_mov_b32 %1, .Lk_code_end0-.Lk_code_begin\n\ts_mov_b32 %2, .Lk_code_end1-.Lk_code_begin" : "=s"(pc), "=s"(l0), "=s"(l1));
    touch(DUMMY, ((lane & 15) * 32 + r) * 128, mkr((const void*)pc, max(l0, l1)), 0);
  }
  const bool gemm = r < 30;
  const int idx = (xcd >> 1) * 30 + r, nt = idx >> 3, mt = (xcd & 1) * 8 + (idx & 7);
  const int n0 = nt < 14 ? nt * 96 : 1280;
  const int m2 = (xcd & 1) * 16 + (xcd >> 1) * 4 + (r >> 3), n2 = r & 7;
  const int j16 = lane & 15, g = lane >> 4;
  const int row0 = (xcd & 1) * 1024 + (xcd >> 1) * 256 + r * 8;
  asm volatile("" ::"s"(nt), "s"(mt), "s"(n0), "s"(m2), "s"(n2), "s"(row0), "v"(j16), "v"(g));
  asm volatile("" : "+s"(x), "+s"(lar), "+s"(aim), "+s"(cre), "+s"(cim), "+s"(ldt), "+s"(dsk), "+s"(wg), "+s"(wu), "+s"(wd), "+s"(out), "+s"(ws));
  const rsrc_t rx = mkr(x, L * D * 2);
  const rsrc_t rws = mkr(ws, WS_END);

  if (w < 4) {
    u32x4 xr[2];
    auto ldrows = [&]() {
#pragma unroll
      for (int i = 0; i < 2; ++i)
        asm volatile("buffer_load_dwordx4 %0, %1, %2, %3 offen" : "=v"(xr[i]) : "v"(lane * 16), "s"(rx), "s"((row0 + 2 * w + i) * 1024) : "memory");
      SB();
    };
    auto layernorm = [&]() {
      asm volatile("" : "+v"(xr[0]), "+v"(xr[1]));
      unsigned stat = 0;
#pragma unroll
      for (int i = 0; i < 2; ++i) {
        float v[8];
        const float x0 = blo(rfl(xr[i][0]));
#pragma unroll
        for (int e = 0; e < 4; ++e) { const unsigned q = xr[i][e]; v[2 * e] = blo(q) - x0; v[2 * e + 1] = bhi(q) - x0; }
        float q2 = v[0] * v[0];
#pragma unroll
        for (int e = 1; e < 8; ++e) q2 = __builtin_fmaf(v[e], v[e], q2);
        const float md = wave_sum(((v[0] + v[1]) + (v[2] + v[3])) + ((v[4] + v[5]) + (v[6] + v[7]))) * (1.f / D);
        const float var = rb(__builtin_fmaxf(__builtin_fmaf(-md, md, wave_sum(q2) * (1.f / D)), 0.f));
        const float m = rb(x0 + md), sd = rb(__builtin_amdgcn_sqrtf(rb(var + 1e-5f))), rs = __builtin_amdgcn_rcpf(sd), c = (x0 - m) * rs;
        u32x4 o;
#pragma unroll
        for (int e = 0; e < 4; ++e) o[e] = pk2(__builtin_fmaf(v[2 * e], rs, c), __builtin_fmaf(v[2 * e + 1], rs, c));
        *(LDS u32x4*)(lds + LNB + (2 * w + i) * LNROW + lane * 16) = o;
        if (lane == i) stat = pk2(m, sd);
      }
      if (lane < 2) *(LDS unsigned*)(lds + LNS + (2 * w + lane) * 4) = stat;
      STAMP(12);
      bar();
    };
    const rsrc_t rwd = mkr(wd, F * D * 2);
    unsigned vd[2];
#pragma unroll
    for (int i = 0; i < 2; ++i) { const int row = lane >> 3, f = ((row >> 1) & 1) + 2 * i; vd[i] = row * 1024 + n2 * 128 + (((lane & 7) ^ (2 * f)) << 4); }
    unsigned wl = w * 8192, wl6 = (w >> 1 ? 0 : 2 * SLOT) + LEFT + (w & 1) * 4096, k0 = w * 11;
    unsigned rot = (r >> 3) * 3;
    auto wdloc = [&](int pos) { return (unsigned)(((pos / 2 + 2) % 3) * SLOT + (pos & 1) * 4096); };
    auto kstep = [&](int s) { const unsigned t = s + rot; return k0 + (t >= 11 ? t - 11 : t); };
    auto issue_wd = [&](int s, int pos, int p) {
      const unsigned k = kstep(s);
      dma16((pos == 6 ? wl6 : wdloc(pos) + wl) + p * 1024, vd[p & 1], rwd, (k < 42 ? k : 42) * 32768 + p * 8192);
    };
    unsigned ad[4];
    s16x8 wf[4][4];
    auto rdwd = [&](s16x8* dst, int pos) {
#pragma unroll
      for (int u = 0; u < 4; ++u) {
        LDS char* p = lds + ad[u] + (pos == 6 ? wl6 - wl : wdloc(pos));
        dst[u] = cat(trd(p), trd(p + 512));
      }
    };
    auto wd_turn = [&]() {
      const int q = j16 >> 2, f = ((q >> 1) & 1) + 2 * (g & 1);
#pragma unroll
      for (int u = 0; u < 4; ++u) ad[u] = wl + (8 * g + q) * 128 + ((u ^ f) << 5) + (j16 & 3) * 8;
#pragma unroll
      for (int s = 0; s < 4; ++s) rdwd(wf[s], s);
    };
    u64 tag;

    if (gemm) {
      ldrows();
      const int mw = w & 1, nw = w >> 1;
      const rsrc_t rw = mkr(nw ? wu : wg, D * F * 2);
      unsigned vx[2], vw[3];
#pragma unroll
      for (int i = 0; i < 2; ++i) {
        const int row = i * 8 + (lane >> 3), id = (row >> 1) & 7, f = (id & 4) | ((id & 1) << 1) | ((id >> 1) & 1);
        vx[i] = (mt * 128 + w * 32 + row) * 1024 + (((lane & 7) ^ f) << 4);
      }
#pragma unroll
      for (int i = 0; i < 3; ++i) { const int c = i * 64 + lane, kr = (c * 171) >> 11, ch = c - kr * 12; vw[i] = (mw * 32 + kr) * (F * 2) + ch * 16 + n0 * 2; }
      const unsigned mx = w * 4096, mwd = (nw ? UOFF : GOFF) + mw * 2 * WBLK;
      auto issue = [&](int s, int i) {
        if (s >= 8) {
          if (i < 8) issue_wd((s - 8) * 2 + (i >> 2), (s - 8) * 2 + (i >> 2), i & 3);
          else if (s > 8) issue_wd(6, 6, (s - 9) * 2 + i - 8);
          return;
        }
        const unsigned slot = (s % 3) * SLOT;
        if (i < 4) dma16(slot + mx + i * 1024, vx[i & 1], rx, s * 128 + (i >> 1) * 16384);
        else { const int j = i - 4; dma16(slot + mwd + (j / 3) * WBLK + (j % 3) * 1024, vw[j % 3], rw, s * (64 * F * 2) + (j / 3) * 16 * F * 2); }
      };
#pragma unroll
      for (int i = 0; i < 10; ++i) issue(0, i);
      waitv<10>();
      STAMP(10);
      layernorm();
#pragma unroll
      for (int i = 0; i < 10; ++i) issue(1, i);
      unsigned ax[2], aw;
      {
        const int id = (j16 >> 1) & 7, f = (id & 4) | ((id & 1) << 1) | ((id >> 1) & 1), cg = 2 * (g & 1) + (g >> 1);
#pragma unroll
        for (int s = 0; s < 2; ++s) ax[s] = (mw * 64 + j16) * 128 + (((4 * s + cg) ^ f) << 4);
        aw = GOFF + (g & 1) * WBLK + (8 * (g >> 1) + (j16 >> 2)) * 192 + nw * 96 + (j16 & 3) * 8;
      }
      f32x4 acc[4][6] = {};
      G1Frag fr[2];
      auto half = [&](G1Frag& c, bool domm, G1Frag& n, bool dord, int s, int jj, int sd, int i0) {
#pragma unroll
        for (int t = 0; t < 24; ++t) {
          if (domm) acc[t & 3][t >> 2] = mfma16(c.w[t >> 2], c.x[t & 3], acc[t & 3][t >> 2]);
          SB();
          if (dord && (t & 1) == 0) {
            const int k = t >> 1;
            if (k < 2) {
              n.x[2 * k] = *(const LDS s16x8*)(lds + (s % 3) * SLOT + ax[jj] + 2 * k * 2048);
              n.x[2 * k + 1] = *(const LDS s16x8*)(lds + (s % 3) * SLOT + ax[jj] + (2 * k + 1) * 2048);
            } else if (k < 8) {
              const int u = k - 2;
              const unsigned o = (s % 3) * SLOT + jj * 2 * WBLK + (u / 3) * 4 * WBLK + (u % 3) * 32;
              n.w[u] = cat(trd(lds + aw + o), trd(lds + aw + o + 768));
            }
          }
          if ((t & 3) == 3 && t >= 7) issue(sd, i0 + (t - 7) / 4);
          SB();
        }
      };
#pragma unroll
      for (int s = 0; s < 8; ++s) {
        if (s < 7) waitv<10>(); else waitv<8>();
        bar();
        if (s == 0) STAMP(1);
        if (s == 4) STAMP(2);
        half(fr[1], s > 0, fr[0], true, s, 0, s + 2, 0);
        half(fr[0], true, fr[1], true, s, 1, s + 2, 5);
      }
      bar();
      f32x2 st[4]; f32x4 cs[6];
      tag = *(const LDS u64*)(lds + SYNC + 16);
#pragma unroll
      for (int m = 0; m < 4; ++m) st[m] = *(const LDS f32x2*)(lds + STO + (mw * 64 + m * 16 + j16) * 8);
#pragma unroll
      for (int u = 0; u < 6; ++u) cs[u] = *(const LDS f32x4*)(lds + CSO + ((u / 3) * 96 + (nw * 3 + u % 3) * 16 + g * 4) * 4);
      half(fr[1], true, fr[0], false, 0, 0, 10, 0);
      STAMP(3);
      waitv<7>();
      wd_turn();
      SB();
#pragma unroll
      for (int mp = 0; mp < 2; ++mp) {
#pragma unroll
        for (int u = 0; u < 3; ++u) {
          unsigned o[2][2];
#pragma unroll
          for (int mi = 0; mi < 2; ++mi) {
            const int m = mp * 2 + mi;
            const float nm = st[m][0], sd = st[m][1], kg = -1.44269504f * __builtin_amdgcn_rcpf(sd);
            float hv[4];
#pragma unroll
            for (int e = 0; e < 4; ++e) {
              const float gg = __builtin_fmaf(nm, cs[u][e], acc[m][u][e]), uu = __builtin_fmaf(nm, cs[u + 3][e], acc[m][u + 3][e]);
              hv[e] = uu * __builtin_amdgcn_rcpf(__builtin_fmaf(__builtin_amdgcn_exp2f(gg * kg), sd, sd));
            }
            o[mi][0] = pk2(hv[0], hv[1]); o[mi][1] = pk2(hv[2], hv[3]);
            SB();
            const int q = (mp * 3 + u) * 2 + mi;
            if (q == 0) waitl0();
#pragma unroll
            for (int i = q * 3; i < q * 3 + 3; ++i) {
              if (i < 5) issue(10, 5 + i);
              else if (i < 21) issue_wd(7 + ((i - 5) >> 2), (i - 5) >> 2, (i - 5) & 3);
            }
            SB();
          }
          const auto r0 = __builtin_amdgcn_permlane16_swap(o[0][0], o[1][0], false, false);
          const auto r1 = __builtin_amdgcn_permlane16_swap(o[0][1], o[1][1], false, false);
          const u32x4 ov = {r0[0], r1[0], r0[1], r1[1]};
          const int unit = (n0 >> 4) + nw * 3 + u;
          if (nt < 14 || unit >= 84) __builtin_amdgcn_raw_buffer_store_b128(ov, rws, lane * 16, WS_H + ((mt * 4 + mw * 2 + mp) * NU + unit) * 1024, SC1);
        }
      }
      STAMP(4);
      waitv<0>();
      if (nt < 14 || nw == 1) setflag(ws, WS_HF + ((mt * 2 + mw) * 32 + (nt < 14 ? nt * 2 + nw : 28)) * 8, tag, lane);
    } else {
      ldrows();
      const int j = xcd * 2 + (r - 30), t = threadIdx.x;
      const rsrc_t rw = mkr((j >> 3) ? wu : wg, D * F * 2);
      const int kg = (t * 745) >> 14, c = t - kg * 22, col = (j & 7) * 176 + c * 8;
      const unsigned vo = (kg < 11 && col < F) ? kg * (F * 2) + col * 2 : 0x7f000000u;
      float a[8] = {};
      u32x4 v[2][8];
      auto ld = [&](int sl) {
#pragma unroll
        for (int i = 0; i < 8; ++i) v[sl & 1][i] = __builtin_amdgcn_raw_buffer_load_b128(rw, vo + (sl * 8 + i) * (11 * F * 2), 0, 0);
      };
      ld(0);
      waitv<8>();
      layernorm();
#pragma unroll
      for (int sl = 0; sl < 6; ++sl) {
        if (sl < 5) ld(sl + 1);
        if (sl < 4) bar();
#pragma unroll
        for (int i = 0; i < 8; ++i)
#pragma unroll
          for (int e = 0; e < 4; ++e) { const unsigned q = v[sl & 1][i][e]; a[2 * e] += blo(q); a[2 * e + 1] += bhi(q); }
        asm volatile("" : "+v"(a[0]), "+v"(a[1]), "+v"(a[2]), "+v"(a[3]), "+v"(a[4]), "+v"(a[5]), "+v"(a[6]), "+v"(a[7]));
      }
      if (kg < 11) {
        *(LDS f32x4*)(lds + kg * 704 + c * 32) = f32x4{a[0], a[1], a[2], a[3]};
        *(LDS f32x4*)(lds + kg * 704 + c * 32 + 16) = f32x4{a[4], a[5], a[6], a[7]};
      }
      bar();
      tag = *(const LDS u64*)(lds + SYNC + 16);
      if (t < 176) {
        float s = 0.f;
#pragma unroll
        for (int k = 0; k < 11; ++k) s += *(const LDS float*)(lds + k * 704 + t * 4);
        __builtin_amdgcn_raw_buffer_store_b32(f2u(s), rws, t * 4, WS_CS + ((j >> 3) * CSP + (j & 7) * 176) * 4, SC1);
      }
      waitv<0>();
      bar();
      if (w == 0) setflag(ws, WS_CSF + j * 8, tag, lane);
      STAMP(11);
#pragma unroll
      for (int i = 0; i < 3; ++i) touch(SLOT + w * 256, (j * 688 + i * 256 + t) * 128, rwd, 0);
      waitv<0>();
      bar();
#pragma unroll
      for (int i = 0; i < 28; ++i) issue_wd(i >> 2, i >> 2, i & 3);
      bar();
      bar();
      waitv<12>();
      wd_turn();
      waitl0();
      SB();
#pragma unroll
      for (int i = 0; i < 16; ++i) issue_wd(7 + (i >> 2), i >> 2, i & 3);
    }

    STAMP(5);
    {
      const int f0 = (w * 22) / 3, f1 = min(w * 22 + 21, 85) / 3;
      poll(ws, WS_HF + (m2 * 32 + min(f0 + j16, f1)) * 8, tag);
    }
    STAMP(6);
    asm volatile("" : "+s"(wl), "+s"(wl6), "+s"(k0), "+s"(rot));
    unsigned vh[4];
#pragma unroll
    for (int m = 0; m < 4; ++m) { const int mm = (m + w) & 3; vh[m] = (mm >> 1) * (NU * 1024) + (g >> 1) * 1024 + (32 * (g & 1) + 16 * (mm & 1) + j16) * 16; }
    const unsigned sh = WS_H + m2 * 2 * NU * 1024;
    s16x8 hf[HD][4];
    auto ldh = [&](int s) {
      const unsigned k = kstep(s);
      const rsrc_t rh = mkr(ws, k < 43 ? WS_END : 0);
#pragma unroll
      for (int m = 0; m < 4; ++m) hf[s % HD][m] = __builtin_bit_cast(s16x8, __builtin_amdgcn_raw_buffer_load_b128(rh, vh[m], sh + k * 2048, SC1));
    };
    f32x4 a2[4][4] = {};
    ldh(0); ldh(1);
    {
      const unsigned t = 2 + (lane >> 2) + rot, k = k0 + (t >= 11 ? t - 11 : t), q = n2 * 4 + (lane & 3);
      const unsigned vo = (lane < 36 && k < 43) ? k * 2048 + (q >> 4) * (NU * 1024) + (q & 15) * 128 : 0x7f000000u;
      asm volatile("s_mov_b32 m0, %0\n\ts_nop 0\n\tbuffer_load_dword %1, %2, %3 offen sc1 lds" ::"s"(DUMMY + w * 256), "v"(vo), "s"(rws), "s"(sh) : "memory");
    }
#pragma unroll
    for (int s = 2; s < HD; ++s) ldh(s);
    SB();
#pragma unroll
    for (int s = 0; s < 11; ++s) {
#pragma unroll
      for (int m = 0; m < 4; ++m)
#pragma unroll
        for (int u = 0; u < 4; ++u) a2[m][u] = mfma16(wf[s % 4][u], hf[s % HD][m], a2[m][u]);
      SB();
      if (s + HD < 11) ldh(s + HD);
      if (s + 4 < 11) rdwd(wf[s % 4], s < 3 ? s + 4 : s - 3);
      SB();
    }
    STAMP(7);
#pragma unroll
    for (int m = 1; m < 4; ++m)
#pragma unroll
      for (int u = 0; u < 4; ++u) *(LDS f32x4*)(lds + wdloc(m - 1) + w * 8192 + u * 1024 + lane * 16) = a2[m][u];
    bar();
    STAMP(8);
    unsigned o[4][2];
#pragma unroll
    for (int u = 0; u < 4; ++u) {
      f32x4 s = a2[0][u];
#pragma unroll
      for (int m = 1; m < 4; ++m) s += *(const LDS f32x4*)(lds + wdloc(m - 1) + ((w - m) & 3) * 8192 + u * 1024 + lane * 16);
      s += *(const LDS f32x4*)(lds + SBASE + (u >> 1) * PAIR + (u & 1) * 4096 + w * 1024 + lane * 16);
      o[u][0] = pk2(s[0], s[1]); o[u][1] = pk2(s[2], s[3]);
    }
#pragma unroll
    for (int up = 0; up < 2; ++up) {
      const auto r0 = __builtin_amdgcn_permlane16_swap(o[2 * up][0], o[2 * up + 1][0], false, false);
      const auto r1 = __builtin_amdgcn_permlane16_swap(o[2 * up][1], o[2 * up + 1][1], false, false);
      const u32x4 ov = {r0[0], r1[0], r0[1], r1[1]};
      __builtin_amdgcn_raw_buffer_store_b128(ov, mkr(out, L * D * 2), j16 * 1024 + (g & 1) * 32 + (g >> 1) * 16 + up * 64, (m2 * 64 + w * 16) * 1024 + n2 * 128, 0);
    }
    STAMP(9);
    STAMP_OUT(0);
    asm volatile(".Lk_code_end0:");
  } else {
    const int sw = w - 4, cl = sw >> 1, hh = sw & 1, st = threadIdx.x - 256;
    const int cp = xcd * 32 + r, ch = cp * 2 + cl;
    const int pi = ch * NM + hh * 64 + lane;
    auto ldg = [](const u16* p, unsigned i) { return (unsigned)*(const GLB u16*)((const GLB char*)p + i * 2); };
    const unsigned p_lar = ldg(lar, pi), p_aim = ldg(aim, pi), p_cr = ldg(cre, pi), p_ci = ldg(cim, pi), p_ldt = ldg(ldt, ch), p_dsk = ldg(dsk, ch);
    if (sw < 2) touch(DUMMY + 256 + sw * 256, ((lane >> 3) * 64 + (xcd & 1) * 32 + r) * (F * 2) + (xcd >> 1) * 688 + (lane & 7) * 112, mkr(sw ? wu : wg, D * F * 2), 0);
    u64 tag = *(const __attribute__((address_space(4))) u64*)(ws + WS_E) + 0x9E3779B97F4A7C15ull;
    if (st == 0) { *(LDS u32x4*)(lds + SYNC) = u32x4{0, 0, 0, 0}; *(LDS u64*)(lds + SYNC + 16) = tag; }
    const float dt = rb(exp_f(blo(p_ldt)));
    const float s2 = -rb(exp_f(blo(p_lar))) * dt * 1.44269504f, th = blo(p_aim) * dt;
    const unsigned eb = min(max(wave_max(max(p_cr & 0x7fffu, p_ci & 0x7fffu)) >> 7, 1u), 253u);
    const float sc = u2f((254u - eb) << 23), ksc = 2.f * dt * u2f(eb << 23);
    cpx z1 = cpow2(s2, th, 1.f), z8c = cpow2(s2, -th, 8.f), z256 = cpow2(s2, th, 256.f);
    asm volatile("" : "+v"(z1.r), "+v"(z1.i), "+v"(z8c.r), "+v"(z8c.i), "+v"(z256.r), "+v"(z256.i), "+s"(tag));
    STAMP(1);
    bar();
#pragma unroll
    for (int i = 0; i < 2; ++i) {
      LDS char* p = lds + LNB + (j16 >> 2) * LNROW + (i * 256 + sw * 64 + g * 16 + (j16 & 3) * 4) * 2;
      const int c = i * 256 + st;
      __builtin_amdgcn_raw_buffer_store_b128(__builtin_bit_cast(u32x4, cat(trd(p), trd(p + 4 * LNROW))), rws,
                                             (c >> 1) * 8192 + (c & 1) * 16, WS_XNT + row0 * 4, SC1);
    }
    if (st < 2) __builtin_amdgcn_raw_buffer_store_b128(*(const LDS u32x4*)(lds + LNS + st * 16), rws, st * 16, WS_ST + row0 * 4, SC1);
    STAMP(2);
    LDS char* pr = lds + SBASE + cl * PAIR;
    LDS char* ks = pr + hh * KSW;
    auto put = [&](int row, cpx v) { *(LDS unsigned*)(ks + row * KROW + lane * 4) = pkh(v.r, v.i); };
    auto putu = [&](int reg, int h, cpx v) { put((reg & 3) + 4 * h + 8 * (reg >> 2), v); };
    f16x8 vf[8];
    {
      cpx v[4];
      v[0] = {1.f, 0.f}; v[1] = z8c; v[2] = cmul(z8c, z8c); v[3] = cmul(v[2], z8c);
      const cpx w4 = cmul(v[2], v[2]);
#pragma unroll
      for (int k = 0; k < 32; ++k) {
        if (k == 4) v[0] = w4; else if (k > 4) v[k & 3] = cmul(v[k & 3], w4);
        put(k, v[k & 3]);
      }
      fence();
#pragma unroll
      for (int s = 0; s < 8; ++s) vf[s] = *(const LDS f16x8*)(ks + (lane & 31) * KROW + s * 32 + (lane >> 5) * 16);
    }
    STAMP(3);
    bar();
    cpx e[5];
    {
      const cpx z2 = cmul(z1, z1);
      e[0] = {blo(p_cr) * sc, blo(p_ci) * sc};
      putu(0, 0, e[0]);
#pragma unroll
      for (int k = 0; k < 4; ++k) { putu(2 * k + 1, 0, cmul(e[k], z1)); e[k + 1] = cmul(e[k], z2); putu(2 * k + 2, 0, e[k + 1]); }
#pragma unroll
      for (int k = 0; k < 5; ++k) e[k] = cmul(e[k], z256);
      putu(0, 1, e[0]); putu(2, 1, e[1]); putu(3, 1, e[2]); putu(4, 1, e[2]); putu(5, 1, e[2]); putu(6, 1, e[3]); putu(7, 1, e[4]); putu(8, 1, e[4]);
    }
    waitv<0>();
    STAMP(4);
    bar();
    if (sw == 0) setflag(ws, WS_XNF + b * 8, tag, lane);
#pragma unroll
    for (int k = 0; k < 6; ++k) {
      const int reg = k < 2 ? 9 + 2 * k : k < 5 ? 5 + 2 * k : 13, h = k < 2 || k == 5;
      e[0] = cmul(e[0], z256); putu(reg, h, e[0]);
      e[4] = cmul(e[4], z256); putu(reg + 1, h, e[4]);
      if (k < 2) { e[2] = cmul(e[2], z256); putu(k ? 1 : 15, 1, e[2]); }
    }
    STAMP(5);
    bar();
    f32x16 kd = {};
#pragma unroll
    for (int s = 0; s < 8; ++s)
      kd = __builtin_amdgcn_mfma_f32_32x32x16_f16(*(const LDS f16x8*)(ks + (lane & 31) * KROW + s * 32 + (lane >> 5) * 16), vf[s], kd, 0, 0, 0);
    kd *= ksc;
    if (hh) {
      *(LDS f32x4*)(pr + KEX1 + lane * 16) = f32x4{kd[0], kd[1], kd[2], kd[3]};
      *(LDS f32x4*)(pr + KEX1 + 1024 + lane * 16) = f32x4{kd[4], kd[5], kd[6], kd[7]};
      *(LDS f32x4*)(pr + KEX1 + 2048 + lane * 16) = f32x4{kd[8], kd[13], kd[14], kd[15]};
    } else {
      *(LDS f32x4*)(pr + KEX0 + lane * 16) = f32x4{kd[9], kd[10], kd[11], kd[12]};
      *(LDS f32x2*)(pr + KEX0 + 1024 + lane * 8) = f32x2{kd[15], kd[1]};
    }
    STAMP(6);
    bar();
    {
      const int bb = lane & 31;
      const bool h = lane >> 5, odd = lane & 1;
      auto chunk = [&](int c, float a0, float a1, float a2, float a3, float a4, float a5, float a6, float a7, float a8) {
        *(LDS u32x4*)(pr + TX + (255 - c) * 16) = u32x4{pk2(a7, a6), pk2(a5, a4), pk2(a3, a2), pk2(a1, a0)};
        *(LDS u32x4*)(pr + TY + (255 - c) * 16) = u32x4{pk2(a8, a7), pk2(a6, a5), pk2(a4, a3), pk2(a2, a1)};
      };
      if (hh) {
        const f32x4 p = *(const LDS f32x4*)(pr + KEX0 + lane * 16);
        const f32x2 q = *(const LDS f32x2*)(pr + KEX0 + 1024 + lane * 8);
#pragma unroll
        for (int k = 0; k < 2; ++k) {
          const float a = kd[9 + 2 * k] + p[2 * k], c = kd[10 + 2 * k] + p[2 * k + 1], m = kd[k ? 1 : 15] + q[k];
          chunk((h ? 64 : 128) + 32 * k + bb, a, a, a, h ? m : a, h ? m : odd ? c : a, h ? m : c, c, c, c);
        }
      } else {
        const f32x4 p0 = *(const LDS f32x4*)(pr + KEX1 + lane * 16), p1 = *(const LDS f32x4*)(pr + KEX1 + 1024 + lane * 16);
        const f32x4 p2 = *(const LDS f32x4*)(pr + KEX1 + 2048 + lane * 16);
        const float a0 = kd[0] + p0[0] + (lane == 0 ? blo(p_dsk) : 0.f);
        chunk(lane, a0, h ? a0 : kd[1] + p0[1], kd[2] + p0[2], kd[3] + p0[3], kd[4] + p1[0], kd[5] + p1[1], kd[6] + p1[2], kd[7] + p1[3], kd[8] + p2[0]);
        const float a = kd[13] + p2[1], c = kd[14] + p2[2];
        chunk(192 + lane, a, a, a, a, odd ? c : a, c, c, c, c);
        if (lane < 4) *(LDS u32x4*)(pr + (lane & 2 ? TX : TY) + 4096 + (lane & 1) * 16) = u32x4{lane == 0 ? pk2(a0, 0.f) : 0, 0, 0, 0};
      }
    }
    STAMP(7);
    bar();
    poll(ws, WS_XNF + st * 8, tag);
    u32x2 xt[4];
#pragma unroll
    for (int m = 0; m < 4; ++m) xt[m] = __builtin_amdgcn_raw_buffer_load_b64(rx, j16 * 1024 + g * 8, (m2 * 64 + m * 16) * 1024 + n2 * 128 + sw * 32, 0);
    STAMP(8);
    bar();
    if (b == 0 && sw == 0) setflag(ws, WS_E, tag, lane);
    u32x4 xi[2];
#pragma unroll
    for (int i = 0; i < 2; ++i) xi[i] = __builtin_amdgcn_raw_buffer_load_b128(rws, st * 16, WS_XNT + cp * 8192 + i * 4096, SC1);
    const unsigned gst = __builtin_amdgcn_raw_buffer_load_b32(rws, (st & 127) * 4, WS_ST + (mt & 15) * 512, SC1);
    if (st < 60) *(LDS u32x4*)(lds + SBASE + (st & 1) * PAIR + XIMG + (st >> 1) * 16) = u32x4{0, 0, 0, 0};
    bar();
#pragma unroll
    for (int i = 0; i < 2; ++i) *(LDS u32x4*)(lds + SBASE + (st & 1) * PAIR + XIMG + XPAD + (i * 128 + (st >> 1)) * 16) = xi[i];
    if (st < 128) *(LDS f32x2*)(lds + STO + st * 8) = f32x2{-blo(gst), bhi(gst)};
    unsigned csv = 0;
    if (gemm) {
      poll(ws, WS_CSF + j16 * 8, tag);
      const int mat = st >= 96;
      csv = __builtin_amdgcn_raw_buffer_load_b32(rws, (mat * CSP + n0 + st - mat * 96) * 4, WS_CS, SC1);
    }
    STAMP(9);
    bar();
    *(LDS unsigned*)(lds + CSO + st * 4) = csv;
    unsigned pa[4];
#pragma unroll
    for (int i = 0; i < 4; ++i) {
      pa[i] = SBASE + cl * PAIR + (j16 & 1 ? TX : TY) + (847 - 256 * i - hh * 64 - ((15 + j16 - 8 * g) >> 1)) * 4;
      asm volatile("" : "+v"(pa[i]));
    }
    LDS char* pb = pr + XIMG + XPAD + (16 * j16 + 8 * g - 16) * 2 - hh * 256 - 192;
    CvFrag cf[2];
    auto rdc = [&](CvFrag& f, int rr, int k) {
      if (k & 1) f.b[k >> 1] = *(const LDS s16x8*)(pb + (k >> 1) * 512 + (3 - rr) * 64);
      else {
        const LDS s16x4u* q = (const LDS s16x4u*)(lds + pa[k >> 2] + (k & 2 ? 0 : 512) + (3 - rr) * 64);
        f.a[k >> 1] = cat(q[0], q[1]);
      }
    };
#pragma unroll
    for (int k = 0; k < 16; ++k) rdc(cf[0], 0, k);
    STAMP(10);
    bar();
    f32x4 ca[8] = {};
#pragma unroll
    for (int rr = 0; rr < 4; ++rr) {
      int n = 0;
#pragma unroll
      for (int u = 0; u < 8; ++u)
#pragma unroll
        for (int t = u; t < 8; ++t) {
          ca[t] = mfma16(cf[rr & 1].a[u], cf[rr & 1].b[t - u], ca[t]);
          SB();
          if (rr < 3 && (n & 1) == 0 && (n >> 1) < 16) rdc(cf[(rr + 1) & 1], rr + 1, n >> 1);
          SB();
          ++n;
        }
    }
    STAMP(11);
    {
      LDS char* exa = pr + CEX + lane * 16;
      LDS char* exb = lds + SLOT + LEFT + cl * 4096 + lane * 16;
      f32x4 keep[4];
#pragma unroll
      for (int k = 0; k < 4; ++k) {
        *(LDS f32x4*)((hh ? exa : exb) + k * 1024) = hh ? ca[k] : ca[k + 4];
        keep[k] = hh ? ca[k + 4] : ca[k];
      }
      waitl0();
      volatile LDS unsigned* sy = (volatile LDS unsigned*)(lds + SYNC);
      sy[sw] = 1u;
#pragma unroll 1
      for (unsigned spins = 0; spins < SPIN_CAP; ++spins) { if (rfl(sy[sw ^ 1])) break; }
      fence();
      unsigned o[4][2];
#pragma unroll
      for (int k = 0; k < 4; ++k) {
        const f32x4 s = keep[k] + *(const LDS f32x4*)((hh ? exb : exa) + k * 1024);
        o[k][0] = pk2(s[0], s[1]); o[k][1] = pk2(s[2], s[3]);
      }
#pragma unroll
      for (int kp = 0; kp < 2; ++kp) {
        const auto r0 = __builtin_amdgcn_permlane16_swap(o[2 * kp][0], o[2 * kp + 1][0], false, false);
        const auto r1 = __builtin_amdgcn_permlane16_swap(o[2 * kp][1], o[2 * kp + 1][1], false, false);
        const u32x4 ov = {r0[0], r1[0], r0[1], r1[1]};
        __builtin_amdgcn_raw_buffer_store_b128(ov, rws, ((g & 1) * 256 + 16 * j16 + 8 * (g >> 1)) * 2, WS_YT + ch * 4096 + (hh * 4 + kp * 2) * 512, SC1);
      }
      waitv<0>();
      setflag(ws, WS_YF + (hh * 512 + ch) * 8, tag, lane);
    }
    STAMP(12);
    poll(ws, WS_YF + ((m2 >> 4) * 512 + n2 * 64 + sw * 16 + j16) * 8, tag);
    STAMP(13);
    {
      LDS char* yb = pr + hh * 4096;
#pragma unroll
      for (int i = 0; i < 2; ++i) {
        const u32x4 y = __builtin_amdgcn_raw_buffer_load_b128(rws, (lane >> 3) * 4096 + (lane & 7) * 16, WS_YT + (n2 * 64 + sw * 16 + i * 8) * 4096 + m2 * 128, SC1);
        *(LDS u32x4*)(yb + (i * 8 + (lane >> 3)) * 160 + (lane & 7) * 16) = y;
      }
      fence();
      u32x2 y[4];
#pragma unroll
      for (int m = 0; m < 4; ++m) y[m] = __builtin_bit_cast(u32x2, trd(yb + (4 * g + (j16 >> 2)) * 160 + m * 32 + (j16 & 3) * 8));
      waitl0();
#pragma unroll
      for (int m = 0; m < 4; ++m) {
        const unsigned y0 = y[m][0], y1 = y[m][1], q0 = xt[m][0], q1 = xt[m][1];
        *(LDS f32x4*)(yb + m * 1024 + lane * 16) = f32x4{blo(y0) + blo(q0), bhi(y0) + bhi(q0), blo(y1) + blo(q1), bhi(y1) + bhi(q1)};
      }
    }
    STAMP(14);
    bar();
    STAMP_OUT(1);
    asm volatile(".Lk_code_end1:");
  }
}

}
#if defined(__HIP_DEVICE_COMPILE__)
#pragma clang attribute pop
#endif

extern "C" void kernel_launch(void* const* d_in, const int* in_sizes, int n_in, void* d_out, int out_size, void* d_ws, size_t ws_size,
                              hipStream_t stream) {
  fused_kernel<<<256, 512, 0, stream>>>((const u16*)d_in[0], (const u16*)d_in[1], (const u16*)d_in[2], (const u16*)d_in[3], (const u16*)d_in[4],
                                        (const u16*)d_in[5], (const u16*)d_in[6], (const u16*)d_in[8], (const u16*)d_in[9], (const u16*)d_in[10],
                                        (u16*)d_out, (char*)d_ws);
}
```

```cpp
#include <hip/hip_runtime.h>
#include <hip/hip_bf16.h>

#if defined(__HIP_DEVICE_COMPILE__)
#pragma clang attribute push(__attribute__((target("no-packed-fp32-ops"))), apply_to = function)
#endif
namespace {

typedef unsigned short u16;
typedef unsigned long long u64;
typedef __attribute__((ext_vector_type(4))) short s16x4;
typedef s16x4 __attribute__((aligned(4))) s16x4u;
typedef __attribute__((ext_vector_type(8))) short s16x8;
typedef __attribute__((ext_vector_type(8))) __bf16 bf16x8;
typedef __attribute__((ext_vector_type(2))) __bf16 bf16x2;
typedef __attribute__((ext_vector_type(8))) _Float16 f16x8;
typedef __attribute__((ext_vector_type(2))) _Float16 f16x2;
typedef __attribute__((ext_vector_type(2))) float f32x2;
typedef __attribute__((ext_vector_type(4))) float f32x4;
typedef __attribute__((ext_vector_type(16))) float f32x16;
typedef __attribute__((ext_vector_type(4))) unsigned u32x4;
typedef __attribute__((ext_vector_type(2))) unsigned u32x2;
typedef __amdgpu_buffer_rsrc_t rsrc_t;
#define LDS __attribute__((address_space(3)))
#define GLB __attribute__((address_space(1)))
#define SB() __builtin_amdgcn_sched_barrier(0)
#define RLX_AGENT __ATOMIC_RELAXED, __HIP_MEMORY_SCOPE_AGENT
#define RLX_WG __ATOMIC_RELAXED, __HIP_MEMORY_SCOPE_WORKGROUP

constexpr int L = 2048, D = 512, NM = 128, F = 1376, NU = F / 16;
constexpr int SC1 = 16, HD = 5;
constexpr unsigned SPIN_CAP = 1u << 18;

constexpr int WBLK = 3104, GOFF = 16384, UOFF = GOFF + 4 * WBLK, SLOT = GOFF + 8 * WBLK;
constexpr int LEFT = 32768;
constexpr int LNB = 2 * SLOT, LNROW = 1088;
constexpr int SBASE = 3 * SLOT;
constexpr int KROW = 272, KSW = 32 * KROW, PAIR = 2 * KSW;
constexpr int TX = 0, XIMG = 4160, XPAD = 480, CEX = 8736, TY = 13248, KEX0 = 7168, KEX1 = KSW;
constexpr int STO = SBASE + 2 * PAIR, CSO = STO + 1024, SYNC = CSO + 1024, LNS = SYNC + 32, DUMMY = SYNC + 64;
constexpr int LDS_BYTES = 163840;
static_assert(DUMMY + 1024 <= LDS_BYTES && XIMG + XPAD + 4096 <= CEX && CEX + 4096 <= TY && TY + 4128 <= PAIR, "LDS map");

constexpr int WS_E = 0, WS_XNF = 4096, WS_CSF = 8192, WS_HF = 12288, WS_YF = 20480, WS_ST = 32768, WS_CS = 40960;
constexpr int WS_XNT = 1 << 20, WS_YT = 3 << 20, WS_H = 5 << 20, WS_END = WS_H + 64 * NU * 1024;
constexpr int CSP = 1408;

__device__ __forceinline__ float u2f(unsigned v) { return __builtin_bit_cast(float, v); }
__device__ __forceinline__ unsigned f2u(float v) { return __builtin_bit_cast(unsigned, v); }
__device__ __forceinline__ bool wave_all(bool p) { return __builtin_amdgcn_ballot_w64(!p) == 0; }
__device__ __forceinline__ unsigned rfl(unsigned v) { return __builtin_amdgcn_readfirstlane(v); }
__device__ __forceinline__ rsrc_t mkr(const void* p, unsigned n) { return __builtin_amdgcn_make_buffer_rsrc((void*)p, 0, n, 0x00020000); }
__device__ __forceinline__ void dma16(unsigned m0v, unsigned voff, rsrc_t r, unsigned soff) {
  asm volatile("s_mov_b32 m0, %0\n\ts_nop 0\n\tbuffer_load_dwordx4 %1, %2, %3 offen lds" ::"s"(m0v), "v"(voff), "s"(r), "s"(soff) : "memory");
}
__device__ __forceinline__ void touch(unsigned m0v, unsigned voff, rsrc_t r, unsigned soff) {
  asm volatile("s_mov_b32 m0, %0\n\ts_nop 0\n\tbuffer_load_dword %1, %2, %3 offen lds" ::"s"(m0v), "v"(voff), "s"(r), "s"(soff) : "memory");
}
template <int W> __device__ __forceinline__ void waitv() { asm volatile("s_waitcnt vmcnt(%0)" ::"n"(W) : "memory"); }
__device__ __forceinline__ void waitl0() { asm volatile("s_waitcnt lgkmcnt(0)" ::: "memory"); }
__device__ __forceinline__ void fence() { asm volatile("" ::: "memory"); }
__device__ __forceinline__ void bar() { waitl0(); __builtin_amdgcn_s_barrier(); fence(); }
__device__ __forceinline__ unsigned pk2(float a, float b) { f32x2 p = {a, b}; return __builtin_bit_cast(unsigned, __builtin_convertvector(p, bf16x2)); }
__device__ __forceinline__ unsigned pkh(float a, float b) { f32x2 p = {a, b}; return __builtin_bit_cast(unsigned, __builtin_convertvector(p, f16x2)); }
__device__ __forceinline__ float blo(unsigned v) { return u2f(v << 16); }
__device__ __forceinline__ float bhi(unsigned v) { return u2f(v & 0xffff0000u); }
__device__ __forceinline__ float rb(float a) { return blo(pk2(a, a)); }
template <int C, int RM> __device__ __forceinline__ float dppf(float v) {
  return __builtin_bit_cast(float, __builtin_amdgcn_update_dpp(0, __builtin_bit_cast(int, v), C, RM, 0xf, true));
}
__device__ __forceinline__ float lane63(float v) { return __builtin_bit_cast(float, __builtin_amdgcn_readlane(__builtin_bit_cast(int, v), 63)); }
__device__ __forceinline__ float wave_sum(float v) {
  v += dppf<0x111, 0xf>(v); v += dppf<0x112, 0xf>(v); v += dppf<0x114, 0xf>(v); v += dppf<0x118, 0xf>(v);
  v += dppf<0x142, 0xa>(v); v += dppf<0x143, 0xc>(v);
  return lane63(v);
}
template <int C, int RM> __device__ __forceinline__ unsigned dppmax(unsigned v) {
  const unsigned o = __builtin_amdgcn_update_dpp(0, v, C, RM, 0xf, true);
  return v > o ? v : o;
}
__device__ __forceinline__ unsigned wave_max(unsigned v) {
  v = dppmax<0x111, 0xf>(v); v = dppmax<0x112, 0xf>(v); v = dppmax<0x114, 0xf>(v); v = dppmax<0x118, 0xf>(v);
  v = dppmax<0x142, 0xa>(v); v = dppmax<0x143, 0xc>(v);
  return __builtin_amdgcn_readlane(v, 63);
}
__device__ __forceinline__ s16x8 cat(s16x4 a, s16x4 b) { return __builtin_shufflevector(a, b, 0, 1, 2, 3, 4, 5, 6, 7); }
__device__ __forceinline__ s16x4 trd(LDS char* p) { return __builtin_amdgcn_ds_read_tr16_b64_v4i16((LDS s16x4*)p); }
__device__ __forceinline__ f32x4 mfma16(s16x8 a, s16x8 b, f32x4 c) {
  return __builtin_amdgcn_mfma_f32_16x16x32_bf16(__builtin_bit_cast(bf16x8, a), __builtin_bit_cast(bf16x8, b), c, 0, 0, 0);
}
__device__ __forceinline__ void poll(const char* ws, unsigned off, u64 tag) {
  GLB u64* p = (GLB u64*)(ws + off);
#pragma unroll 1
  for (unsigned spins = 0; spins < SPIN_CAP; ++spins) {
    if (wave_all(__hip_atomic_load(p, RLX_AGENT) == tag)) break;
    __builtin_amdgcn_s_sleep(1);
  }
  fence();
}
__device__ __forceinline__ void setflag(char* ws, unsigned off, u64 tag, int lane) {
  if (lane == 0) __hip_atomic_store((GLB u64*)(ws + off), tag, RLX_AGENT);
}
struct cpx { float r, i; };
__device__ __forceinline__ cpx cmul(cpx a, cpx b) { return {a.r * b.r - a.i * b.i, a.r * b.i + a.i * b.r}; }
__device__ __forceinline__ cpx cpow2(float s2, float th, float p) {
  const float t = th * p, n = __builtin_rintf(t * 0.15915494f);
  const float fr = __builtin_fmaf(t, 0.15915494f, -n) + t * 6.4206382e-9f;
  const float e = __builtin_amdgcn_exp2f(s2 * p);
  return {e * __builtin_amdgcn_cosf(fr), e * __builtin_amdgcn_sinf(fr)};
}
__device__ __forceinline__ float exp_f(float v) {
  const float t = v * 1.44269504f, e = __builtin_fmaf(v, 1.925963033e-8f, __builtin_fmaf(v, 1.44269504f, -t));
  const float y = __builtin_amdgcn_exp2f(t);
  return __builtin_fmaf(y, e * 0.69314718f, y);
}

#define DIAG
#define NSTAMP 20
#define RT(v) asm volatile("s_memrealtime %0\n\ts_waitcnt lgkmcnt(0)" : "=s"(v)::"memory")
#define STAMP(i) do { if ((i) == 0 || (i) == 0) { SB(); RT(ts[i]); SB(); } } while (0)
#define STAMP_OUT(role) do { if ((role) == 0 && w == 0) { GLB u64* q_ = (GLB u64*)(ws + (16 << 20)); \
  if (lane == 0) { __hip_atomic_store(q_ + b, ts[0], RLX_AGENT); __hip_atomic_store(q_ + 256 + b, ts[0] ? ts[0] : ts[0], RLX_AGENT); } \
  if (b == 100) { u64 t1_; for (int i_ = 0; i_ < 100000; ++i_) { RT(t1_); if (t1_ - ts[0] >= 4000) break; } \
    u64 mn_ = ~0ull, mx_ = 0; for (int i_ = 0; i_ < 256; i_ += 64) { u64 a_ = __hip_atomic_load(q_ + i_ + lane, RLX_AGENT), c_ = __hip_atomic_load(q_ + 256 + i_ + lane, RLX_AGENT); mn_ = a_ < mn_ ? a_ : mn_; mx_ = c_ > mx_ ? c_ : mx_; } \
    unsigned lo_ = (unsigned)(ts[0] - mn_) + 100000u, hi_ = (unsigned)(mx_ - ts[0]) + 100000u; lo_ = wave_max(lo_); hi_ = wave_max(hi_); mn_ = ts[0] - (lo_ - 100000u); mx_ = ts[0] + (hi_ - 100000u); \
    const u64 d_ = 5000 + 0; for (int i_ = 0; i_ < 200000; ++i_) { RT(t1_); if (t1_ - mn_ >= d_) break; } } } } while (0)

struct G1Frag { s16x8 x[4]; s16x8 w[6]; };
struct CvFrag { s16x8 a[8]; s16x8 b[8]; };

__global__ __launch_bounds__(512, 2) void fused_kernel(const u16* x, const u16* lar, const u16* aim,
                                                       const u16* cre, const u16* cim, const u16* ldt,
                                                       const u16* dsk, const u16* wg, const u16* wu,
                                                       const u16* wd, u16* out, char* ws) {
  __shared__ __attribute__((aligned(16))) char lds_raw[LDS_BYTES];
  LDS char* lds = (LDS char*)lds_raw;
  u64 ts[NSTAMP] = {};
  const int w = rfl(threadIdx.x >> 6), lane = threadIdx.x & 63;
  const int b = blockIdx.x, xcd = b & 7, r = b >> 3;
  STAMP(0);
  if (w == 7) {
    u64 pc; unsigned l0, l1;
    asm volatile("s_getpc_b64 %0\n.Lk_code_begin:\n\ts_mov_b32 %1, .Lk_code_end0-.Lk_code_begin\n\ts_mov_b32 %2, .Lk_code_end1-.Lk_code_begin" : "=s"(pc), "=s"(l0), "=s"(l1));
    touch(DUMMY, ((lane & 15) * 32 + r) * 128, mkr((const void*)pc, max(l0, l1)), 0);
  }
  const bool gemm = r < 30;
  const int idx = (xcd >> 1) * 30 + r, nt = idx >> 3, mt = (xcd & 1) * 8 + (idx & 7);
  const int n0 = nt < 14 ? nt * 96 : 1280;
  const int m2 = (xcd & 1) * 16 + (xcd >> 1) * 4 + (r >> 3), n2 = r & 7;
  const int j16 = lane & 15, g = lane >> 4;
  const int row0 = (xcd & 1) * 1024 + (xcd >> 1) * 256 + r * 8;
  asm volatile("" ::"s"(nt), "s"(mt), "s"(n0), "s"(m2), "s"(n2), "s"(row0), "v"(j16), "v"(g));
  asm volatile("" : "+s"(x), "+s"(lar), "+s"(aim), "+s"(cre), "+s"(cim), "+s"(ldt), "+s"(dsk), "+s"(wg), "+s"(wu), "+s"(wd), "+s"(out), "+s"(ws));
  const rsrc_t rx = mkr(x, L * D * 2);
  const rsrc_t rws = mkr(ws, WS_END);

  if (w < 4) {
    u32x4 xr[2];
    auto ldrows = [&]() {
#pragma unroll
      for (int i = 0; i < 2; ++i)
        asm volatile("buffer_load_dwordx4 %0, %1, %2, %3 offen" : "=v"(xr[i]) : "v"(lane * 16), "s"(rx), "s"((row0 + 2 * w + i) * 1024) : "memory");
      SB();
    };
    auto layernorm = [&]() {
      asm volatile("" : "+v"(xr[0]), "+v"(xr[1]));
      unsigned stat = 0;
#pragma unroll
      for (int i = 0; i < 2; ++i) {
        float v[8];
        const float x0 = blo(rfl(xr[i][0]));
#pragma unroll
        for (int e = 0; e < 4; ++e) { const unsigned q = xr[i][e]; v[2 * e] = blo(q) - x0; v[2 * e + 1] = bhi(q) - x0; }
        float q2 = v[0] * v[0];
#pragma unroll
        for (int e = 1; e < 8; ++e) q2 = __builtin_fmaf(v[e], v[e], q2);
        const float md = wave_sum(((v[0] + v[1]) + (v[2] + v[3])) + ((v[4] + v[5]) + (v[6] + v[7]))) * (1.f / D);
        const float var = rb(__builtin_fmaxf(__builtin_fmaf(-md, md, wave_sum(q2) * (1.f / D)), 0.f));
        const float m = rb(x0 + md), sd = rb(__builtin_amdgcn_sqrtf(rb(var + 1e-5f))), rs = __builtin_amdgcn_rcpf(sd), c = (x0 - m) * rs;
        u32x4 o;
#pragma unroll
        for (int e = 0; e < 4; ++e) o[e] = pk2(__builtin_fmaf(v[2 * e], rs, c), __builtin_fmaf(v[2 * e + 1], rs, c));
        *(LDS u32x4*)(lds + LNB + (2 * w + i) * LNROW + lane * 16) = o;
        if (lane == i) stat = pk2(m, sd);
      }
      if (lane < 2) *(LDS unsigned*)(lds + LNS + (2 * w + lane) * 4) = stat;
      STAMP(12);
      bar();
    };
    const rsrc_t rwd = mkr(wd, F * D * 2);
    unsigned vd[2];
#pragma unroll
    for (int i = 0; i < 2; ++i) { const int row = lane >> 3, f = ((row >> 1) & 1) + 2 * i; vd[i] = row * 1024 + n2 * 128 + (((lane & 7) ^ (2 * f)) << 4); }
    unsigned wl = w * 8192, wl6 = (w >> 1 ? 0 : 2 * SLOT) + LEFT + (w & 1) * 4096, k0 = w * 11;
    unsigned rot = (r >> 3) * 3;
    auto wdloc = [&](int pos) { return (unsigned)(((pos / 2 + 2) % 3) * SLOT + (pos & 1) * 4096); };
    auto kstep = [&](int s) { const unsigned t = s + rot; return k0 + (t >= 11 ? t - 11 : t); };
    auto issue_wd = [&](int s, int pos, int p) {
      const unsigned k = kstep(s);
      dma16((pos == 6 ? wl6 : wdloc(pos) + wl) + p * 1024, vd[p & 1], rwd, (k < 42 ? k : 42) * 32768 + p * 8192);
    };
    unsigned ad[4];
    s16x8 wf[4][4];
    auto rdwd = [&](s16x8* dst, int pos) {
#pragma unroll
      for (int u = 0; u < 4; ++u) {
        LDS char* p = lds + ad[u] + (pos == 6 ? wl6 - wl : wdloc(pos));
        dst[u] = cat(trd(p), trd(p + 512));
      }
    };
    auto wd_turn = [&]() {
      const int q = j16 >> 2, f = ((q >> 1) & 1) + 2 * (g & 1);
#pragma unroll
      for (int u = 0; u < 4; ++u) ad[u] = wl + (8 * g + q) * 128 + ((u ^ f) << 5) + (j16 & 3) * 8;
#pragma unroll
      for (int s = 0; s < 4; ++s) rdwd(wf[s], s);
    };
    u64 tag;

    if (gemm) {
      ldrows();
      const int mw = w & 1, nw = w >> 1;
      const rsrc_t rw = mkr(nw ? wu : wg, D * F * 2);
      unsigned vx[2], vw[3];
#pragma unroll
      for (int i = 0; i < 2; ++i) {
        const int row = i * 8 + (lane >> 3), id = (row >> 1) & 7, f = (id & 4) | ((id & 1) << 1) | ((id >> 1) & 1);
        vx[i] = (mt * 128 + w * 32 + row) * 1024 + (((lane & 7) ^ f) << 4);
      }
#pragma unroll
      for (int i = 0; i < 3; ++i) { const int c = i * 64 + lane, kr = (c * 171) >> 11, ch = c - kr * 12; vw[i] = (mw * 32 + kr) * (F * 2) + ch * 16 + n0 * 2; }
      const unsigned mx = w * 4096, mwd = (nw ? UOFF : GOFF) + mw * 2 * WBLK;
      auto issue = [&](int s, int i) {
        if (s >= 8) {
          if (i < 8) issue_wd((s - 8) * 2 + (i >> 2), (s - 8) * 2 + (i >> 2), i & 3);
          else if (s > 8) issue_wd(6, 6, (s - 9) * 2 + i - 8);
          return;
        }
        const unsigned slot = (s % 3) * SLOT;
        if (i < 4) dma16(slot + mx + i * 1024, vx[i & 1], rx, s * 128 + (i >> 1) * 16384);
        else { const int j = i - 4; dma16(slot + mwd + (j / 3) * WBLK + (j % 3) * 1024, vw[j % 3], rw, s * (64 * F * 2) + (j / 3) * 16 * F * 2); }
      };
#pragma unroll
      for (int i = 0; i < 10; ++i) issue(0, i);
      waitv<10>();
      STAMP(10);
      layernorm();
#pragma unroll
      for (int i = 0; i < 10; ++i) issue(1, i);
      unsigned ax[2], aw;
      {
        const int id = (j16 >> 1) & 7, f = (id & 4) | ((id & 1) << 1) | ((id >> 1) & 1), cg = 2 * (g & 1) + (g >> 1);
#pragma unroll
        for (int s = 0; s < 2; ++s) ax[s] = (mw * 64 + j16) * 128 + (((4 * s + cg) ^ f) << 4);
        aw = GOFF + (g & 1) * WBLK + (8 * (g >> 1) + (j16 >> 2)) * 192 + nw * 96 + (j16 & 3) * 8;
      }
      f32x4 acc[4][6] = {};
      G1Frag fr[2];
      auto half = [&](G1Frag& c, bool domm, G1Frag& n, bool dord, int s, int jj, int sd, int i0) {
#pragma unroll
        for (int t = 0; t < 24; ++t) {
          if (domm) acc[t & 3][t >> 2] = mfma16(c.w[t >> 2], c.x[t & 3], acc[t & 3][t >> 2]);
          SB();
          if (dord && (t & 1) == 0) {
            const int k = t >> 1;
            if (k < 2) {
              n.x[2 * k] = *(const LDS s16x8*)(lds + (s % 3) * SLOT + ax[jj] + 2 * k * 2048);
              n.x[2 * k + 1] = *(const LDS s16x8*)(lds + (s % 3) * SLOT + ax[jj] + (2 * k + 1) * 2048);
            } else if (k < 8) {
              const int u = k - 2;
              const unsigned o = (s % 3) * SLOT + jj * 2 * WBLK + (u / 3) * 4 * WBLK + (u % 3) * 32;
              n.w[u] = cat(trd(lds + aw + o), trd(lds + aw + o + 768));
            }
          }
          if ((t & 3) == 3 && t >= 7) issue(sd, i0 + (t - 7) / 4);
          SB();
        }
      };
#pragma unroll
      for (int s = 0; s < 8; ++s) {
        if (s < 7) waitv<10>(); else waitv<8>();
        bar();
        if (s == 0) STAMP(1);
        if (s == 4) STAMP(2);
        half(fr[1], s > 0, fr[0], true, s, 0, s + 2, 0);
        half(fr[0], true, fr[1], true, s, 1, s + 2, 5);
      }
      bar();
      f32x2 st[4]; f32x4 cs[6];
      tag = *(const LDS u64*)(lds + SYNC + 16);
#pragma unroll
      for (int m = 0; m < 4; ++m) st[m] = *(const LDS f32x2*)(lds + STO + (mw * 64 + m * 16 + j16) * 8);
#pragma unroll
      for (int u = 0; u < 6; ++u) cs[u] = *(const LDS f32x4*)(lds + CSO + ((u / 3) * 96 + (nw * 3 + u % 3) * 16 + g * 4) * 4);
      half(fr[1], true, fr[0], false, 0, 0, 10, 0);
      STAMP(3);
      waitv<7>();
      wd_turn();
      SB();
#pragma unroll
      for (int mp = 0; mp < 2; ++mp) {
#pragma unroll
        for (int u = 0; u < 3; ++u) {
          unsigned o[2][2];
#pragma unroll
          for (int mi = 0; mi < 2; ++mi) {
            const int m = mp * 2 + mi;
            const float nm = st[m][0], sd = st[m][1], kg = -1.44269504f * __builtin_amdgcn_rcpf(sd);
            float hv[4];
#pragma unroll
            for (int e = 0; e < 4; ++e) {
              const float gg = __builtin_fmaf(nm, cs[u][e], acc[m][u][e]), uu = __builtin_fmaf(nm, cs[u + 3][e], acc[m][u + 3][e]);
              hv[e] = uu * __builtin_amdgcn_rcpf(__builtin_fmaf(__builtin_amdgcn_exp2f(gg * kg), sd, sd));
            }
            o[mi][0] = pk2(hv[0], hv[1]); o[mi][1] = pk2(hv[2], hv[3]);
            SB();
            const int q = (mp * 3 + u) * 2 + mi;
            if (q == 0) waitl0();
#pragma unroll
            for (int i = q * 3; i < q * 3 + 3; ++i) {
              if (i < 5) issue(10, 5 + i);
              else if (i < 21) issue_wd(7 + ((i - 5) >> 2), (i - 5) >> 2, (i - 5) & 3);
            }
            SB();
          }
          const auto r0 = __builtin_amdgcn_permlane16_swap(o[0][0], o[1][0], false, false);
          const auto r1 = __builtin_amdgcn_permlane16_swap(o[0][1], o[1][1], false, false);
          const u32x4 ov = {r0[0], r1[0], r0[1], r1[1]};
          const int unit = (n0 >> 4) + nw * 3 + u;
          if (nt < 14 || unit >= 84) __builtin_amdgcn_raw_buffer_store_b128(ov, rws, lane * 16, WS_H + ((mt * 4 + mw * 2 + mp) * NU + unit) * 1024, SC1);
        }
      }
      STAMP(4);
      waitv<0>();
      if (nt < 14 || nw == 1) setflag(ws, WS_HF + ((mt * 2 + mw) * 32 + (nt < 14 ? nt * 2 + nw : 28)) * 8, tag, lane);
    } else {
      ldrows();
      const int j = xcd * 2 + (r - 30), t = threadIdx.x;
      const rsrc_t rw = mkr((j >> 3) ? wu : wg, D * F * 2);
      const int kg = (t * 745) >> 14, c = t - kg * 22, col = (j & 7) * 176 + c * 8;
      const unsigned vo = (kg < 11 && col < F) ? kg * (F * 2) + col * 2 : 0x7f000000u;
      float a[8] = {};
      u32x4 v[2][8];
      auto ld = [&](int sl) {
#pragma unroll
        for (int i = 0; i < 8; ++i) v[sl & 1][i] = __builtin_amdgcn_raw_buffer_load_b128(rw, vo + (sl * 8 + i) * (11 * F * 2), 0, 0);
      };
      ld(0);
      waitv<8>();
      layernorm();
#pragma unroll
      for (int sl = 0; sl < 6; ++sl) {
        if (sl < 5) ld(sl + 1);
        if (sl < 4) bar();
#pragma unroll
        for (int i = 0; i < 8; ++i)
#pragma unroll
          for (int e = 0; e < 4; ++e) { const unsigned q = v[sl & 1][i][e]; a[2 * e] += blo(q); a[2 * e + 1] += bhi(q); }
        asm volatile("" : "+v"(a[0]), "+v"(a[1]), "+v"(a[2]), "+v"(a[3]), "+v"(a[4]), "+v"(a[5]), "+v"(a[6]), "+v"(a[7]));
      }
      if (kg < 11) {
        *(LDS f32x4*)(lds + kg * 704 + c * 32) = f32x4{a[0], a[1], a[2], a[3]};
        *(LDS f32x4*)(lds + kg * 704 + c * 32 + 16) = f32x4{a[4], a[5], a[6], a[7]};
      }
      bar();
      tag = *(const LDS u64*)(lds + SYNC + 16);
      if (t < 176) {
        float s = 0.f;
#pragma unroll
        for (int k = 0; k < 11; ++k) s += *(const LDS float*)(lds + k * 704 + t * 4);
        __builtin_amdgcn_raw_buffer_store_b32(f2u(s), rws, t * 4, WS_CS + ((j >> 3) * CSP + (j & 7) * 176) * 4, SC1);
      }
      waitv<0>();
      bar();
      if (w == 0) setflag(ws, WS_CSF + j * 8, tag, lane);
      STAMP(11);
#pragma unroll
      for (int i = 0; i < 3; ++i) touch(SLOT + w * 256, (j * 688 + i * 256 + t) * 128, rwd, 0);
      waitv<0>();
      bar();
#pragma unroll
      for (int i = 0; i < 28; ++i) issue_wd(i >> 2, i >> 2, i & 3);
      bar();
      bar();
      waitv<12>();
      wd_turn();
      waitl0();
      SB();
#pragma unroll
      for (int i = 0; i < 16; ++i) issue_wd(7 + (i >> 2), i >> 2, i & 3);
    }

    STAMP(5);
    {
      const int f0 = (w * 22) / 3, f1 = min(w * 22 + 21, 85) / 3;
      poll(ws, WS_HF + (m2 * 32 + min(f0 + j16, f1)) * 8, tag);
    }
    STAMP(6);
    asm volatile("" : "+s"(wl), "+s"(wl6), "+s"(k0), "+s"(rot));
    unsigned vh[4];
#pragma unroll
    for (int m = 0; m < 4; ++m) { const int mm = (m + w) & 3; vh[m] = (mm >> 1) * (NU * 1024) + (g >> 1) * 1024 + (32 * (g & 1) + 16 * (mm & 1) + j16) * 16; }
    const unsigned sh = WS_H + m2 * 2 * NU * 1024;
    s16x8 hf[HD][4];
    auto ldh = [&](int s) {
      const unsigned k = kstep(s);
      const rsrc_t rh = mkr(ws, k < 43 ? WS_END : 0);
#pragma unroll
      for (int m = 0; m < 4; ++m) hf[s % HD][m] = __builtin_bit_cast(s16x8, __builtin_amdgcn_raw_buffer_load_b128(rh, vh[m], sh + k * 2048, SC1));
    };
    f32x4 a2[4][4] = {};
    ldh(0); ldh(1);
    {
      const unsigned t = 2 + (lane >> 2) + rot, k = k0 + (t >= 11 ? t - 11 : t), q = n2 * 4 + (lane & 3);
      const unsigned vo = (lane < 36 && k < 43) ? k * 2048 + (q >> 4) * (NU * 1024) + (q & 15) * 128 : 0x7f000000u;
      asm volatile("s_mov_b32 m0, %0\n\ts_nop 0\n\tbuffer_load_dword %1, %2, %3 offen sc1 lds" ::"s"(DUMMY + w * 256), "v"(vo), "s"(rws), "s"(sh) : "memory");
    }
#pragma unroll
    for (int s = 2; s < HD; ++s) ldh(s);
    SB();
#pragma unroll
    for (int s = 0; s < 11; ++s) {
#pragma unroll
      for (int m = 0; m < 4; ++m)
#pragma unroll
        for (int u = 0; u < 4; ++u) a2[m][u] = mfma16(wf[s % 4][u], hf[s % HD][m], a2[m][u]);
      SB();
      if (s + HD < 11) ldh(s + HD);
      if (s + 4 < 11) rdwd(wf[s % 4], s < 3 ? s + 4 : s - 3);
      SB();
    }
    STAMP(7);
#pragma unroll
    for (int m = 1; m < 4; ++m)
#pragma unroll
      for (int u = 0; u < 4; ++u) *(LDS f32x4*)(lds + wdloc(m - 1) + w * 8192 + u * 1024 + lane * 16) = a2[m][u];
    bar();
    STAMP(8);
    unsigned o[4][2];
#pragma unroll
    for (int u = 0; u < 4; ++u) {
      f32x4 s = a2[0][u];
#pragma unroll
      for (int m = 1; m < 4; ++m) s += *(const LDS f32x4*)(lds + wdloc(m - 1) + ((w - m) & 3) * 8192 + u * 1024 + lane * 16);
      s += *(const LDS f32x4*)(lds + SBASE + (u >> 1) * PAIR + (u & 1) * 4096 + w * 1024 + lane * 16);
      o[u][0] = pk2(s[0], s[1]); o[u][1] = pk2(s[2], s[3]);
    }
#pragma unroll
    for (int up = 0; up < 2; ++up) {
      const auto r0 = __builtin_amdgcn_permlane16_swap(o[2 * up][0], o[2 * up + 1][0], false, false);
      const auto r1 = __builtin_amdgcn_permlane16_swap(o[2 * up][1], o[2 * up + 1][1], false, false);
      const u32x4 ov = {r0[0], r1[0], r0[1], r1[1]};
      __builtin_amdgcn_raw_buffer_store_b128(ov, mkr(out, L * D * 2), j16 * 1024 + (g & 1) * 32 + (g >> 1) * 16 + up * 64, (m2 * 64 + w * 16) * 1024 + n2 * 128, SC1);
    }
    STAMP(9);
    STAMP_OUT(0);
    asm volatile(".Lk_code_end0:");
  } else {
    const int sw = w - 4, cl = sw >> 1, hh = sw & 1, st = threadIdx.x - 256;
    const int cp = xcd * 32 + r, ch = cp * 2 + cl;
    const int pi = ch * NM + hh * 64 + lane;
    auto ldg = [](const u16* p, unsigned i) { return (unsigned)*(const GLB u16*)((const GLB char*)p + i * 2); };
    const unsigned p_lar = ldg(lar, pi), p_aim = ldg(aim, pi), p_cr = ldg(cre, pi), p_ci = ldg(cim, pi), p_ldt = ldg(ldt, ch), p_dsk = ldg(dsk, ch);
    if (sw < 2) touch(DUMMY + 256 + sw * 256, ((lane >> 3) * 64 + (xcd & 1) * 32 + r) * (F * 2) + (xcd >> 1) * 688 + (lane & 7) * 112, mkr(sw ? wu : wg, D * F * 2), 0);
    u64 tag = *(const __attribute__((address_space(4))) u64*)(ws + WS_E) + 0x9E3779B97F4A7C15ull;
    if (st == 0) { *(LDS u32x4*)(lds + SYNC) = u32x4{0, 0, 0, 0}; *(LDS u64*)(lds + SYNC + 16) = tag; }
    const float dt = rb(exp_f(blo(p_ldt)));
    const float s2 = -rb(exp_f(blo(p_lar))) * dt * 1.44269504f, th = blo(p_aim) * dt;
    const unsigned eb = min(max(wave_max(max(p_cr & 0x7fffu, p_ci & 0x7fffu)) >> 7, 1u), 253u);
    const float sc = u2f((254u - eb) << 23), ksc = 2.f * dt * u2f(eb << 23);
    cpx z1 = cpow2(s2, th, 1.f), z8c = cpow2(s2, -th, 8.f), z256 = cpow2(s2, th, 256.f);
    asm volatile("" : "+v"(z1.r), "+v"(z1.i), "+v"(z8c.r), "+v"(z8c.i), "+v"(z256.r), "+v"(z256.i), "+s"(tag));
    STAMP(1);
    bar();
#pragma unroll
    for (int i = 0; i < 2; ++i) {
      LDS char* p = lds + LNB + (j16 >> 2) * LNROW + (i * 256 + sw * 64 + g * 16 + (j16 & 3) * 4) * 2;
      const int c = i * 256 + st;
      __builtin_amdgcn_raw_buffer_store_b128(__builtin_bit_cast(u32x4, cat(trd(p), trd(p + 4 * LNROW))), rws,
                                             (c >> 1) * 8192 + (c & 1) * 16, WS_XNT + row0 * 4, SC1);
    }
    if (st < 2) __builtin_amdgcn_raw_buffer_store_b128(*(const LDS u32x4*)(lds + LNS + st * 16), rws, st * 16, WS_ST + row0 * 4, SC1);
    STAMP(2);
    LDS char* pr = lds + SBASE + cl * PAIR;
    LDS char* ks = pr + hh * KSW;
    auto put = [&](int row, cpx v) { *(LDS unsigned*)(ks + row * KROW + lane * 4) = pkh(v.r, v.i); };
    auto putu = [&](int reg, int h, cpx v) { put((reg & 3) + 4 * h + 8 * (reg >> 2), v); };
    f16x8 vf[8];
    {
      cpx v[4];
      v[0] = {1.f, 0.f}; v[1] = z8c; v[2] = cmul(z8c, z8c); v[3] = cmul(v[2], z8c);
      const cpx w4 = cmul(v[2], v[2]);
#pragma unroll
      for (int k = 0; k < 32; ++k) {
        if (k == 4) v[0] = w4; else if (k > 4) v[k & 3] = cmul(v[k & 3], w4);
        put(k, v[k & 3]);
      }
      fence();
#pragma unroll
      for (int s = 0; s < 8; ++s) vf[s] = *(const LDS f16x8*)(ks + (lane & 31) * KROW + s * 32 + (lane >> 5) * 16);
    }
    STAMP(3);
    bar();
    cpx e[5];
    {
      const cpx z2 = cmul(z1, z1);
      e[0] = {blo(p_cr) * sc, blo(p_ci) * sc};
      putu(0, 0, e[0]);
#pragma unroll
      for (int k = 0; k < 4; ++k) { putu(2 * k + 1, 0, cmul(e[k], z1)); e[k + 1] = cmul(e[k], z2); putu(2 * k + 2, 0, e[k + 1]); }
#pragma unroll
      for (int k = 0; k < 5; ++k) e[k] = cmul(e[k], z256);
      putu(0, 1, e[0]); putu(2, 1, e[1]); putu(3, 1, e[2]); putu(4, 1, e[2]); putu(5, 1, e[2]); putu(6, 1, e[3]); putu(7, 1, e[4]); putu(8, 1, e[4]);
    }
    waitv<0>();
    STAMP(4);
    bar();
    if (sw == 0) setflag(ws, WS_XNF + b * 8, tag, lane);
#pragma unroll
    for (int k = 0; k < 6; ++k) {
      const int reg = k < 2 ? 9 + 2 * k : k < 5 ? 5 + 2 * k : 13, h = k < 2 || k == 5;
      e[0] = cmul(e[0], z256); putu(reg, h, e[0]);
      e[4] = cmul(e[4], z256); putu(reg + 1, h, e[4]);
      if (k < 2) { e[2] = cmul(e[2], z256); putu(k ? 1 : 15, 1, e[2]); }
    }
    STAMP(5);
    bar();
    f32x16 kd = {};
#pragma unroll
    for (int s = 0; s < 8; ++s)
      kd = __builtin_amdgcn_mfma_f32_32x32x16_f16(*(const LDS f16x8*)(ks + (lane & 31) * KROW + s * 32 + (lane >> 5) * 16), vf[s], kd, 0, 0, 0);
    kd *= ksc;
    if (hh) {
      *(LDS f32x4*)(pr + KEX1 + lane * 16) = f32x4{kd[0], kd[1], kd[2], kd[3]};
      *(LDS f32x4*)(pr + KEX1 + 1024 + lane * 16) = f32x4{kd[4], kd[5], kd[6], kd[7]};
      *(LDS f32x4*)(pr + KEX1 + 2048 + lane * 16) = f32x4{kd[8], kd[13], kd[14], kd[15]};
    } else {
      *(LDS f32x4*)(pr + KEX0 + lane * 16) = f32x4{kd[9], kd[10], kd[11], kd[12]};
      *(LDS f32x2*)(pr + KEX0 + 1024 + lane * 8) = f32x2{kd[15], kd[1]};
    }
    STAMP(6);
    bar();
    {
      const int bb = lane & 31;
      const bool h = lane >> 5, odd = lane & 1;
      auto chunk = [&](int c, float a0, float a1, float a2, float a3, float a4, float a5, float a6, float a7, float a8) {
        *(LDS u32x4*)(pr + TX + (255 - c) * 16) = u32x4{pk2(a7, a6), pk2(a5, a4), pk2(a3, a2), pk2(a1, a0)};
        *(LDS u32x4*)(pr + TY + (255 - c) * 16) = u32x4{pk2(a8, a7), pk2(a6, a5), pk2(a4, a3), pk2(a2, a1)};
      };
      if (hh) {
        const f32x4 p = *(const LDS f32x4*)(pr + KEX0 + lane * 16);
        const f32x2 q = *(const LDS f32x2*)(pr + KEX0 + 1024 + lane * 8);
#pragma unroll
        for (int k = 0; k < 2; ++k) {
          const float a = kd[9 + 2 * k] + p[2 * k], c = kd[10 + 2 * k] + p[2 * k + 1], m = kd[k ? 1 : 15] + q[k];
          chunk((h ? 64 : 128) + 32 * k + bb, a, a, a, h ? m : a, h ? m : odd ? c : a, h ? m : c, c, c, c);
        }
      } else {
        const f32x4 p0 = *(const LDS f32x4*)(pr + KEX1 + lane * 16), p1 = *(const LDS f32x4*)(pr + KEX1 + 1024 + lane * 16);
        const f32x4 p2 = *(const LDS f32x4*)(pr + KEX1 + 2048 + lane * 16);
        const float a0 = kd[0] + p0[0] + (lane == 0 ? blo(p_dsk) : 0.f);
        chunk(lane, a0, h ? a0 : kd[1] + p0[1], kd[2] + p0[2], kd[3] + p0[3], kd[4] + p1[0], kd[5] + p1[1], kd[6] + p1[2], kd[7] + p1[3], kd[8] + p2[0]);
        const float a = kd[13] + p2[1], c = kd[14] + p2[2];
        chunk(192 + lane, a, a, a, a, odd ? c : a, c, c, c, c);
        if (lane < 4) *(LDS u32x4*)(pr + (lane & 2 ? TX : TY) + 4096 + (lane & 1) * 16) = u32x4{lane == 0 ? pk2(a0, 0.f) : 0, 0, 0, 0};
      }
    }
    STAMP(7);
    bar();
    poll(ws, WS_XNF + st * 8, tag);
    u32x2 xt[4];
#pragma unroll
    for (int m = 0; m < 4; ++m) xt[m] = __builtin_amdgcn_raw_buffer_load_b64(rx, j16 * 1024 + g * 8, (m2 * 64 + m * 16) * 1024 + n2 * 128 + sw * 32, 0);
    STAMP(8);
    bar();
    if (b == 0 && sw == 0) setflag(ws, WS_E, tag, lane);
    u32x4 xi[2];
#pragma unroll
    for (int i = 0; i < 2; ++i) xi[i] = __builtin_amdgcn_raw_buffer_load_b128(rws, st * 16, WS_XNT + cp * 8192 + i * 4096, SC1);
    const unsigned gst = __builtin_amdgcn_raw_buffer_load_b32(rws, (st & 127) * 4, WS_ST + (mt & 15) * 512, SC1);
    if (st < 60) *(LDS u32x4*)(lds + SBASE + (st & 1) * PAIR + XIMG + (st >> 1) * 16) = u32x4{0, 0, 0, 0};
    bar();
#pragma unroll
    for (int i = 0; i < 2; ++i) *(LDS u32x4*)(lds + SBASE + (st & 1) * PAIR + XIMG + XPAD + (i * 128 + (st >> 1)) * 16) = xi[i];
    if (st < 128) *(LDS f32x2*)(lds + STO + st * 8) = f32x2{-blo(gst), bhi(gst)};
    unsigned csv = 0;
    if (gemm) {
      poll(ws, WS_CSF + j16 * 8, tag);
      const int mat = st >= 96;
      csv = __builtin_amdgcn_raw_buffer_load_b32(rws, (mat * CSP + n0 + st - mat * 96) * 4, WS_CS, SC1);
    }
    STAMP(9);
    bar();
    *(LDS unsigned*)(lds + CSO + st * 4) = csv;
    unsigned pa[4];
#pragma unroll
    for (int i = 0; i < 4; ++i) {
      pa[i] = SBASE + cl * PAIR + (j16 & 1 ? TX : TY) + (847 - 256 * i - hh * 64 - ((15 + j16 - 8 * g) >> 1)) * 4;
      asm volatile("" : "+v"(pa[i]));
    }
    LDS char* pb = pr + XIMG + XPAD + (16 * j16 + 8 * g - 16) * 2 - hh * 256 - 192;
    CvFrag cf[2];
    auto rdc = [&](CvFrag& f, int rr, int k) {
      if (k & 1) f.b[k >> 1] = *(const LDS s16x8*)(pb + (k >> 1) * 512 + (3 - rr) * 64);
      else {
        const LDS s16x4u* q = (const LDS s16x4u*)(lds + pa[k >> 2] + (k & 2 ? 0 : 512) + (3 - rr) * 64);
        f.a[k >> 1] = cat(q[0], q[1]);
      }
    };
#pragma unroll
    for (int k = 0; k < 16; ++k) rdc(cf[0], 0, k);
    STAMP(10);
    bar();
    f32x4 ca[8] = {};
#pragma unroll
    for (int rr = 0; rr < 4; ++rr) {
      int n = 0;
#pragma unroll
      for (int u = 0; u < 8; ++u)
#pragma unroll
        for (int t = u; t < 8; ++t) {
          ca[t] = mfma16(cf[rr & 1].a[u], cf[rr & 1].b[t - u], ca[t]);
          SB();
          if (rr < 3 && (n & 1) == 0 && (n >> 1) < 16) rdc(cf[(rr + 1) & 1], rr + 1, n >> 1);
          SB();
          ++n;
        }
    }
    STAMP(11);
    {
      LDS char* exa = pr + CEX + lane * 16;
      LDS char* exb = lds + SLOT + LEFT + cl * 4096 + lane * 16;
      f32x4 keep[4];
#pragma unroll
      for (int k = 0; k < 4; ++k) {
        *(LDS f32x4*)((hh ? exa : exb) + k * 1024) = hh ? ca[k] : ca[k + 4];
        keep[k] = hh ? ca[k + 4] : ca[k];
      }
      waitl0();
      volatile LDS unsigned* sy = (volatile LDS unsigned*)(lds + SYNC);
      sy[sw] = 1u;
#pragma unroll 1
      for (unsigned spins = 0; spins < SPIN_CAP; ++spins) { if (rfl(sy[sw ^ 1])) break; }
      fence();
      unsigned o[4][2];
#pragma unroll
      for (int k = 0; k < 4; ++k) {
        const f32x4 s = keep[k] + *(const LDS f32x4*)((hh ? exb : exa) + k * 1024);
        o[k][0] = pk2(s[0], s[1]); o[k][1] = pk2(s[2], s[3]);
      }
#pragma unroll
      for (int kp = 0; kp < 2; ++kp) {
        const auto r0 = __builtin_amdgcn_permlane16_swap(o[2 * kp][0], o[2 * kp + 1][0], false, false);
        const auto r1 = __builtin_amdgcn_permlane16_swap(o[2 * kp][1], o[2 * kp + 1][1], false, false);
        const u32x4 ov = {r0[0], r1[0], r0[1], r1[1]};
        __builtin_amdgcn_raw_buffer_store_b128(ov, rws, ((g & 1) * 256 + 16 * j16 + 8 * (g >> 1)) * 2, WS_YT + ch * 4096 + (hh * 4 + kp * 2) * 512, SC1);
      }
      waitv<0>();
      setflag(ws, WS_YF + (hh * 512 + ch) * 8, tag, lane);
    }
    STAMP(12);
    poll(ws, WS_YF + ((m2 >> 4) * 512 + n2 * 64 + sw * 16 + j16) * 8, tag);
    STAMP(13);
    {
      LDS char* yb = pr + hh * 4096;
#pragma unroll
      for (int i = 0; i < 2; ++i) {
        const u32x4 y = __builtin_amdgcn_raw_buffer_load_b128(rws, (lane >> 3) * 4096 + (lane & 7) * 16, WS_YT + (n2 * 64 + sw * 16 + i * 8) * 4096 + m2 * 128, SC1);
        *(LDS u32x4*)(yb + (i * 8 + (lane >> 3)) * 160 + (lane & 7) * 16) = y;
      }
      fence();
      u32x2 y[4];
#pragma unroll
      for (int m = 0; m < 4; ++m) y[m] = __builtin_bit_cast(u32x2, trd(yb + (4 * g + (j16 >> 2)) * 160 + m * 32 + (j16 & 3) * 8));
      waitl0();
#pragma unroll
      for (int m = 0; m < 4; ++m) {
        const unsigned y0 = y[m][0], y1 = y[m][1], q0 = xt[m][0], q1 = xt[m][1];
        *(LDS f32x4*)(yb + m * 1024 + lane * 16) = f32x4{blo(y0) + blo(q0), bhi(y0) + bhi(q0), blo(y1) + blo(q1), bhi(y1) + bhi(q1)};
      }
    }
    STAMP(14);
    bar();
    STAMP_OUT(1);
    asm volatile(".Lk_code_end1:");
  }
}

}
#if defined(__HIP_DEVICE_COMPILE__)
#pragma clang attribute pop
#endif

__global__ void e_small(char* p) { if (p == nullptr) *(volatile char*)p = 0; }
__global__ __launch_bounds__(512, 2) void e_big(char* p) { __shared__ char l[163840]; if (p == nullptr) { l[threadIdx.x] = 1; __syncthreads(); *(volatile char*)p = l[threadIdx.x ^ 1]; } }
extern "C" void kernel_launch(void* const* d_in, const int* in_sizes, int n_in, void* d_out, int out_size, void* d_ws, size_t ws_size,
                              hipStream_t stream) {
  fused_kernel<<<256, 512, 0, stream>>>((const u16*)d_in[0], (const u16*)d_in[1], (const u16*)d_in[2], (const u16*)d_in[3], (const u16*)d_in[4],
                                        (const u16*)d_in[5], (const u16*)d_in[6], (const u16*)d_in[8], (const u16*)d_in[9], (const u16*)d_in[10],
                                        (u16*)d_out, (char*)d_ws);
}
```

```cpp
#include <hip/hip_runtime.h>
#include <hip/hip_bf16.h>

namespace {

typedef unsigned short u16;
typedef unsigned long long u64;
typedef __attribute__((ext_vector_type(4))) short s16x4;
typedef s16x4 __attribute__((aligned(4))) s16x4u;
typedef __attribute__((ext_vector_type(8))) short s16x8;
typedef __attribute__((ext_vector_type(8))) __bf16 bf16x8;
typedef __attribute__((ext_vector_type(2))) __bf16 bf16x2;
typedef __attribute__((ext_vector_type(8))) _Float16 f16x8;
typedef __attribute__((ext_vector_type(2))) _Float16 f16x2;
typedef __attribute__((ext_vector_type(2))) float f32x2;
typedef __attribute__((ext_vector_type(4))) float f32x4;
typedef __attribute__((ext_vector_type(16))) float f32x16;
typedef __attribute__((ext_vector_type(4))) unsigned u32x4;
typedef __attribute__((ext_vector_type(2))) unsigned u32x2;
typedef __amdgpu_buffer_rsrc_t rsrc_t;
#define LDS __attribute__((address_space(3)))
#define GLB __attribute__((address_space(1)))
#define INL __attribute__((always_inline))
#define SB() __builtin_amdgcn_sched_barrier(0)
#define RLX_AGENT __ATOMIC_RELAXED, __HIP_MEMORY_SCOPE_AGENT
#define RLX_WG __ATOMIC_RELAXED, __HIP_MEMORY_SCOPE_WORKGROUP

constexpr int L = 2048, D = 512, NM = 128, F = 1376, NU = F / 16;
constexpr int SC1 = 16;
#define HD 3
#define YSLEEP 16
constexpr unsigned SPIN_CAP = 1u << 18;

constexpr int WBLK = 3104, GOFF = 16384, UOFF = GOFF + 4 * WBLK, SLOT = GOFF + 8 * WBLK;
constexpr int LEFT = 32768;
constexpr int LNB = 2 * SLOT, LNROW = 1088;
constexpr int SBASE = 3 * SLOT;
constexpr int KROW = 272, KSW = 32 * KROW, PAIR = 2 * KSW;
constexpr int TX = 0, XIMG = 4160, XPAD = 480, CEX = 8736, TY = 13248, KEX0 = 7168, KEX1 = KSW;
constexpr int STO = SBASE + 2 * PAIR, CSO = STO + 1024, SYNC = CSO + 1024, DUMMY = SYNC + 64;
constexpr int LDS_BYTES = 163840;
static_assert(DUMMY + 256 <= LDS_BYTES && XIMG + XPAD + 4096 <= CEX && CEX + 4096 <= TY && TY + 4128 <= PAIR, "LDS map");

constexpr int WS_E = 0, WS_XNF = 4096, WS_HF = 12288, WS_YF = 20480, WS_ST = 32768, WS_CS = 40960;
constexpr int WS_XNT = 1 << 20, WS_YT = 3 << 20, WS_H = 5 << 20, WS_END = WS_H + 64 * NU * 1024;
constexpr int CSP = 1408;

__device__ __forceinline__ float u2f(unsigned v) { return __builtin_bit_cast(float, v); }
__device__ __forceinline__ unsigned f2u(float v) { return __builtin_bit_cast(unsigned, v); }
__device__ __forceinline__ bool wave_all(bool p) { return __builtin_amdgcn_ballot_w64(!p) == 0; }
__device__ __forceinline__ unsigned rfl(unsigned v) { return __builtin_amdgcn_readfirstlane(v); }
__device__ __forceinline__ rsrc_t mkr(const void* p, unsigned n) { return __builtin_amdgcn_make_buffer_rsrc((void*)p, 0, n, 0x00020000); }
__device__ __forceinline__ void dma16(unsigned m0v, unsigned voff, rsrc_t r, unsigned soff) {
  asm volatile("s_mov_b32 m0, %0\n\ts_nop 0\n\tbuffer_load_dwordx4 %1, %2, %3 offen lds" ::"s"(m0v), "v"(voff), "s"(r), "s"(soff) : "memory");
}
__device__ __forceinline__ void touch(unsigned m0v, unsigned voff, rsrc_t r, unsigned soff) {
  asm volatile("s_mov_b32 m0, %0\n\ts_nop 0\n\tbuffer_load_dword %1, %2, %3 offen lds" ::"s"(m0v), "v"(voff), "s"(r), "s"(soff) : "memory");
}
__device__ __forceinline__ void st16(u32x4 v, rsrc_t r, unsigned voff, unsigned soff) {
  asm volatile("buffer_store_dwordx4 %0, %1, %2, %3 offen sc1\n\ts_nop 1" ::"v"(v), "v"(voff), "s"(r), "s"(soff) : "memory");
}
template <int W> __device__ __forceinline__ void waitv() { asm volatile("s_waitcnt vmcnt(%0)" ::"n"(W) : "memory"); }
__device__ __forceinline__ void waitl0() { asm volatile("s_waitcnt lgkmcnt(0)" ::: "memory"); }
__device__ __forceinline__ void fence() { asm volatile("" ::: "memory"); }
__device__ __forceinline__ void bar() { waitl0(); __builtin_amdgcn_s_barrier(); fence(); }
__device__ __forceinline__ unsigned pk2(float a, float b) { f32x2 p = {a, b}; return __builtin_bit_cast(unsigned, __builtin_convertvector(p, bf16x2)); }
__device__ __forceinline__ unsigned pkh(float a, float b) { f32x2 p = {a, b}; return __builtin_bit_cast(unsigned, __builtin_convertvector(p, f16x2)); }
__device__ __forceinline__ float blo(unsigned v) { return u2f(v << 16); }
__device__ __forceinline__ float bhi(unsigned v) { return u2f(v & 0xffff0000u); }
__device__ __forceinline__ float rb(float a) { return blo(pk2(a, a)); }
template <int C, int RM> __device__ __forceinline__ float dppf(float v) {
  return __builtin_bit_cast(float, __builtin_amdgcn_update_dpp(0, __builtin_bit_cast(int, v), C, RM, 0xf, true));
}
__device__ __forceinline__ float lane63(float v) { return __builtin_bit_cast(float, __builtin_amdgcn_readlane(__builtin_bit_cast(int, v), 63)); }
__device__ __forceinline__ float wave_sum(float v) {
  v += dppf<0x111, 0xf>(v); v += dppf<0x112, 0xf>(v); v += dppf<0x114, 0xf>(v); v += dppf<0x118, 0xf>(v);
  v += dppf<0x142, 0xa>(v); v += dppf<0x143, 0xc>(v);
  return lane63(v);
}
template <int C, int RM> __device__ __forceinline__ unsigned dppmax(unsigned v) {
  const unsigned o = __builtin_amdgcn_update_dpp(0, v, C, RM, 0xf, true);
  return v > o ? v : o;
}
__device__ __forceinline__ unsigned wave_max(unsigned v) {
  v = dppmax<0x111, 0xf>(v); v = dppmax<0x112, 0xf>(v); v = dppmax<0x114, 0xf>(v); v = dppmax<0x118, 0xf>(v);
  v = dppmax<0x142, 0xa>(v); v = dppmax<0x143, 0xc>(v);
  return __builtin_amdgcn_readlane(v, 63);
}
__device__ __forceinline__ s16x8 cat(s16x4 a, s16x4 b) { return __builtin_shufflevector(a, b, 0, 1, 2, 3, 4, 5, 6, 7); }
__device__ __forceinline__ s16x4 trd(LDS char* p) { return __builtin_amdgcn_ds_read_tr16_b64_v4i16((LDS s16x4*)p); }
__device__ __forceinline__ f32x4 mfma16(s16x8 a, s16x8 b, f32x4 c) {
  return __builtin_amdgcn_mfma_f32_16x16x32_bf16(__builtin_bit_cast(bf16x8, a), __builtin_bit_cast(bf16x8, b), c, 0, 0, 0);
}
template <int NAP> __device__ __forceinline__ void poll(const char* ws, unsigned off, u64 tag) {
  GLB u64* p = (GLB u64*)(ws + off);
#pragma unroll 1
  for (unsigned spins = 0; spins < SPIN_CAP; ++spins) {
    if (wave_all(__hip_atomic_load(p, RLX_AGENT) == tag)) break;
    __builtin_amdgcn_s_sleep(NAP);
  }
  fence();
}
__device__ __forceinline__ void lds_wait(LDS char* p) {
#pragma unroll 1
  for (unsigned spins = 0; spins < SPIN_CAP; ++spins) { if (rfl(*(volatile LDS unsigned*)p)) break; }
  fence();
}
__device__ __forceinline__ void setflag(char* ws, unsigned off, u64 tag, int lane) {
  if (lane == 0) __hip_atomic_store((GLB u64*)(ws + off), tag, RLX_AGENT);
}
__device__ __forceinline__ f32x2 fma2(f32x2 a, f32x2 b, f32x2 c) { return __builtin_elementwise_fma(a, b, c); }
__device__ __forceinline__ f32x2 dup(float a) { return f32x2{a, a}; }
struct cmulr { f32x2 z, rot; };
__device__ __forceinline__ cmulr mul_by(f32x2 z) { return {z, f32x2{-z[1], z[0]}}; }
__device__ __forceinline__ f32x2 cmul(f32x2 a, cmulr m) { return fma2(dup(a[0]), m.z, dup(a[1]) * m.rot); }
__device__ __forceinline__ f32x2 cpow2(float s2, float th, float p) {
  const float t = th * p, n = __builtin_rintf(t * 0.15915494f);
  const float fr = __builtin_fmaf(t, 0.15915494f, -n) + t * 6.4206382e-9f;
  const float e = __builtin_amdgcn_exp2f(s2 * p);
  return f32x2{e * __builtin_amdgcn_cosf(fr), e * __builtin_amdgcn_sinf(fr)};
}
__device__ __forceinline__ float exp_f(float v) {
  const float t = v * 1.44269504f, e = __builtin_fmaf(v, 1.925963033e-8f, __builtin_fmaf(v, 1.44269504f, -t));
  const float y = __builtin_amdgcn_exp2f(t);
  return __builtin_fmaf(y, e * 0.69314718f, y);
}

#define STAMP(i)
#define STAMP_OUT(role)

struct G1Frag { s16x8 x[4]; s16x8 w[6]; };
struct CvFrag { s16x8 a[8]; s16x8 b[8]; };

__global__ __launch_bounds__(512, 2) void fused_kernel(const u16* x, const u16* lar, const u16* aim, const u16* cre, const u16* cim, const u16* ldt,
                                                       const u16* dsk, const u16* wg, const u16* wu, const u16* wd, u16* out, char* ws) {
  __shared__ __attribute__((aligned(16))) char lds_raw[LDS_BYTES];
  LDS char* lds = (LDS char*)lds_raw;
  int w = rfl(threadIdx.x >> 6);
  const int lane = threadIdx.x & 63, tid = threadIdx.x & 255;
  const int b = blockIdx.x, xcd = b & 7, r = b >> 3;
  STAMP(0);
  if (w == 7) {
    u64 pc; unsigned l0, l1;
    asm volatile("s_getpc_b64 %0\n.Lk_code_begin:\n\ts_mov_b32 %1, .Lk_code_end0-.Lk_code_begin\n\ts_mov_b32 %2, .Lk_code_end1-.Lk_code_begin" : "=s"(pc), "=s"(l0), "=s"(l1));
    touch(DUMMY, lane * 1024, mkr((const void*)pc, max(l0, l1) - 896), (r & 7) * 128);
  }
  const int row0 = (xcd & 1) * 1024 + (xcd >> 1) * 256 + r * 8;
  const unsigned vt = lane < 16 ? (lane >> 3) * (64 * F * 2) + (lane & 7) * 96 : 0x7f000000u;
  const unsigned so_t = ((w & 3) * 128 + (xcd & 1) * 32 + r) * (F * 2) + (xcd >> 1) * 688;
  asm volatile("" ::"s"(row0), "v"(vt), "s"(so_t));
  asm volatile("" : "+s"(x), "+s"(lar), "+s"(aim), "+s"(cre), "+s"(cim), "+s"(ldt), "+s"(dsk), "+s"(wg), "+s"(wu), "+s"(wd), "+s"(out), "+s"(ws));
  const rsrc_t rx = mkr(x, L * D * 2);
  const int w4 = w & 3, mw = w & 1, nw = w4 >> 1;
  const int cp = xcd * 32 + r, ch = cp * 2 + nw;
  u32x4 xr[2];
  u64 tag;
  if (w < 4) {
#pragma unroll
    for (int i = 0; i < 2; ++i)
      asm volatile("buffer_load_dwordx4 %0, %1, %2, %3 offen" : "=v"(xr[i]) : "v"(lane * 16), "s"(rx), "s"((row0 + 2 * w4 + i) * 1024) : "memory");
  } else {
    const unsigned pi = (ch * NM + mw * 64 + lane) * 2;
    asm volatile("global_load_ushort %0, %6, %8\n\tglobal_load_ushort %1, %7, %9\n\tglobal_load_ushort %2, %7, %10\n\tglobal_load_ushort %3, %7, %11\n\t"
                 "global_load_ushort %4, %7, %12\n\tglobal_load_ushort %5, %6, %13"
                 : "=&v"(xr[0][0]), "=&v"(xr[0][1]), "=&v"(xr[0][2]), "=&v"(xr[0][3]), "=&v"(xr[1][0]), "=&v"(xr[1][1])
                 : "v"(ch * 2), "v"(pi), "s"(ldt), "s"(lar), "s"(aim), "s"(cre), "s"(cim), "s"(dsk) : "memory");
  }
  u64 epoch;
  asm volatile("s_load_dwordx2 %0, %1, 0x0" : "=s"(epoch) : "s"(ws) : "memory");
  __builtin_amdgcn_s_barrier();
  touch(DUMMY, vt, mkr(w >> 2 ? wu : wg, D * F * 2), so_t);
  auto get_tag = [&]() INL {
    unsigned lo, hi;
    asm volatile("s_waitcnt lgkmcnt(0)\n\ts_add_u32 %0, %2, 0x7F4A7C15\n\ts_addc_u32 %1, %3, 0x9E3779B9"
                 : "=&s"(lo), "=s"(hi) : "s"((unsigned)epoch), "s"((unsigned)(epoch >> 32)) : "scc", "memory");
    tag = (u64)hi << 32 | lo;
  };
  asm volatile("" : "+s"(w));
  SB();

  const rsrc_t rws = mkr(ws, WS_END), rwd = mkr(wd, F * D * 2);
  const bool gemm = r < 30;
  const int idx = (xcd >> 1) * 30 + r, nt = idx >> 3, mt = (xcd & 1) * 8 + (idx & 7);
  const int n0 = nt < 14 ? nt * 96 : 1280;
  const int m2 = (xcd & 1) * 16 + (xcd >> 1) * 4 + (r >> 3), n2 = r & 7;
  const int j16 = lane & 15, g = lane >> 4;

  const rsrc_t rw = mkr((gemm ? nw : xcd >> 2) ? wu : wg, D * F * 2);
  unsigned vx[2], vw[3];
  auto addr_x = [&]() INL {
#pragma unroll
    for (int i = 0; i < 2; ++i) {
      const int row = i * 8 + (lane >> 3), id = (row >> 1) & 7, f = (id & 4) | ((id & 1) << 1) | ((id >> 1) & 1);
      vx[i] = (mt * 128 + w4 * 32 + row) * 1024 + (((lane & 7) ^ f) << 4);
    }
  };
  auto addr_w = [&]() INL {
    const int kr = (lane * 171) >> 11, c = lane - kr * 12;
    vw[0] = (mw * 32 + kr) * (F * 2) + c * 16 + n0 * 2;
    vw[1] = vw[0] + (c >= 8 ? 6 * F * 2 - 128 : 5 * F * 2 + 64);
    vw[2] = vw[0] + (c >= 4 ? 11 * F * 2 - 64 : 10 * F * 2 + 128);
  };
  auto issue = [&](int s, int i) INL {
    const unsigned slot = (s % 3) * SLOT;
    if (i < 4) dma16(slot + w4 * 4096 + i * 1024, vx[i & 1], rx, s * 128 + (i >> 1) * 16384);
    else {
      const int j = i - 4;
      dma16(slot + (nw ? UOFF : GOFF) + (mw * 2 + j / 3) * WBLK + (j % 3) * 1024, vw[j % 3], rw, s * (64 * F * 2) + (j / 3) * 16 * F * 2);
    }
  };
  const unsigned wl = w4 * 8192, wl6 = (nw ? 0 : 2 * SLOT) + LEFT + mw * 4096;
  auto wdloc = [&](int pos) INL { return (unsigned)(((pos / 2 + 2) % 3) * SLOT + (pos & 1) * 4096); };
  unsigned ko[11];
  auto ksteps = [&](unsigned mul, unsigned add) INL {
    const unsigned first = add + w4 * 11 * mul, end = first + (w4 == 3 ? 10 : 11) * mul;
    unsigned cur = first + (r >> 3) * 3 * mul;
#pragma unroll
    for (int s = 0; s < 11; ++s) {
      ko[s] = cur;
      asm volatile("" : "+s"(ko[s]));
      cur = ko[s] + mul;
      cur = cur == end ? first : cur;
    }
  };

  if (__builtin_expect(w < 4, 1)) {
    auto layernorm = [&]() INL {
      get_tag();
      STAMP(1);
      u32x2 stat;
#pragma unroll
      for (int i = 0; i < 2; ++i) {
        f32x2 v[4];
#pragma unroll
        for (int e = 0; e < 4; ++e) asm volatile("v_lshlrev_b32 %0, 16, %2\n\tv_and_b32 %1, 0xffff0000, %2" : "=&v"(v[e][0]), "=v"(v[e][1]) : "v"(xr[i][e]));
        const float x0 = u2f(rfl(f2u(v[0][0])));
#pragma unroll
        for (int e = 0; e < 4; ++e) v[e] -= dup(x0);
        const f32x2 s1 = (v[0] + v[1]) + (v[2] + v[3]), s2 = fma2(v[0], v[0], v[1] * v[1]) + fma2(v[2], v[2], v[3] * v[3]);
        const float md = wave_sum(s1[0] + s1[1]) * (1.f / D);
        const float var = rb(__builtin_fmaxf(__builtin_fmaf(-md, md, wave_sum(s2[0] + s2[1]) * (1.f / D)), 0.f));
        const float m = rb(x0 + md), sd = rb(__builtin_amdgcn_sqrtf(rb(var + 1e-5f))), rs = __builtin_amdgcn_rcpf(sd), c = (x0 - m) * rs;
        u32x4 o;
#pragma unroll
        for (int e = 0; e < 4; ++e) { const f32x2 n = fma2(v[e], dup(rs), dup(c)); o[e] = pk2(n[0], n[1]); }
        *(LDS u32x4*)(lds + LNB + (2 * w4 + i) * LNROW + lane * 16) = o;
        stat[i] = pk2(m, sd);
      }
      __builtin_amdgcn_raw_buffer_store_b64(stat, rws, lane ? 0x7f000000u : 0u, WS_ST + (row0 + 2 * w4) * 4, SC1);
      bar();
#pragma unroll
      for (int i = 0; i < 2; ++i) {
        LDS char* p = lds + LNB + (j16 >> 2) * LNROW + (i * 256 + w4 * 64 + g * 16 + (j16 & 3) * 4) * 2;
        st16(__builtin_bit_cast(u32x4, cat(trd(p), trd(p + 4 * LNROW))), rws, tid * 16, WS_XNT + row0 * 1024 + i * 4096);
      }
    };
    auto touch_wd = [&]() INL { touch(DUMMY, (lane < 11 ? lane * 128 : 0x7f000000u) + (b * 43 + w4 * 11) * 128, rwd, 0); };
    unsigned ad[3][4], vh[4], vpoll;
    s16x8 wf[11][4];
    auto wd_addr = [&](int i) INL {
      const int q = j16 >> 2, f = ((q >> 1) & 1) + 2 * (g & 1);
#pragma unroll
      for (int u = 0; u < 4; ++u) {
        ad[i][u] = (i == 0 ? wl : i == 1 ? wl + 2 * SLOT : wl6) + (8 * g + q) * 128 + ((u ^ f) << 5) + (j16 & 3) * 8;
        asm volatile("" : "+v"(ad[i][u]));
      }
    };
    auto rdwd = [&](int s, int u) INL {
      const int pos = s % 7;
      LDS char* p = lds + (pos == 6 ? ad[2][u] : pos < 2 ? ad[1][u] + pos * 4096 : ad[0][u] + wdloc(pos));
      wf[s][u] = cat(trd(p), trd(p + 512));
    };
    auto h_addr = [&]() INL {
      wd_addr(0); wd_addr(1); wd_addr(2);
#pragma unroll
      for (int m = 0; m < 4; ++m) { const int mm = (m + w4) & 3; vh[m] = (mm >> 1) * (NU * 1024) + (g >> 1) * 1024 + (32 * (g & 1) + 16 * (mm & 1) + j16) * 16; }
      const int f0 = (w4 * 22) / 3, f1 = min(w4 * 22 + 21, 85) / 3;
      vpoll = WS_HF + (m2 * 32 + min(f0 + j16, f1)) * 8;
      asm volatile("" : "+v"(vh[0]), "+v"(vh[1]), "+v"(vh[2]), "+v"(vh[3]), "+v"(vpoll));
      ksteps(2048, WS_H + m2 * 2 * NU * 1024);
      if (w4 == 3) ko[10] = 0x7f000000u;
    };

    if (__builtin_expect(gemm, 1)) {
      addr_x();
#pragma unroll
      for (int i = 0; i < 4; ++i) issue(0, i);
      SB();
      addr_w();
#pragma unroll
      for (int i = 4; i < 10; ++i) issue(0, i);
      waitv<11>();
      layernorm();
#pragma unroll
      for (int i = 0; i < 10; ++i) issue(1, i);
      touch_wd();
      unsigned ax[2], aw;
      {
        const int id = (j16 >> 1) & 7, f = (id & 4) | ((id & 1) << 1) | ((id >> 1) & 1), cg = 2 * (g & 1) + (g >> 1);
#pragma unroll
        for (int s = 0; s < 2; ++s) ax[s] = (mw * 64 + j16) * 128 + (((4 * s + cg) ^ f) << 4);
        aw = GOFF + (g & 1) * WBLK + (8 * (g >> 1) + (j16 >> 2)) * 192 + nw * 96 + (j16 & 3) * 8;
      }
      unsigned a_st = STO + (mw * 64 + j16) * 8, a_cs = CSO + (nw * 48 + g * 4) * 4, so_h[3];
#pragma unroll
      for (int u = 0; u < 3; ++u) {
        const int unit = (n0 >> 4) + nw * 3 + u;
        so_h[u] = nt < 14 || unit >= 84 ? WS_H + ((mt * 4 + mw * 2) * NU + unit) * 1024 : 0x7f000000u;
      }
      unsigned vflag = (lane == 0 && (nt < 14 || nw == 1)) ? WS_HF + ((mt * 2 + mw) * 32 + (nt < 14 ? nt * 2 + nw : 28)) * 8 : 0x7f000000u;
      asm volatile("" : "+v"(a_st), "+v"(a_cs), "+s"(so_h[0]), "+s"(so_h[1]), "+s"(so_h[2]), "+v"(vflag));
      h_addr();
      f32x4 acc[4][6] = {};
      G1Frag fr[2];
      auto half = [&](G1Frag& c, bool domm, G1Frag& n, bool dord, int s, int jj) INL {
#pragma unroll
        for (int t = 0; t < 24; ++t) {
          if (domm) acc[t & 3][t >> 2] = mfma16(c.w[t >> 2], c.x[t & 3], acc[t & 3][t >> 2]);
          SB();
          if (dord && (t & 1) == 0) {
            const int k = t >> 1;
            if (k < 2) {
              n.x[2 * k] = *(const LDS s16x8*)(lds + (s % 3) * SLOT + ax[jj] + 2 * k * 2048);
              n.x[2 * k + 1] = *(const LDS s16x8*)(lds + (s % 3) * SLOT + ax[jj] + (2 * k + 1) * 2048);
            } else if (k < 8) {
              const int u = k - 2;
              const unsigned o = (s % 3) * SLOT + jj * 2 * WBLK + (u / 3) * 4 * WBLK + (u % 3) * 32;
              n.w[u] = cat(trd(lds + aw + o), trd(lds + aw + o + 768));
            }
          }
          SB();
        }
      };
#pragma unroll
      for (int s = 0; s < 8; ++s) {
        if (s == 0) waitv<11>(); else if (s == 1) waitv<0>();
        if (s == 0) STAMP(19);
        bar();
        STAMP(2 + s);
        half(fr[1], s > 0, fr[0], true, s, 0);
        half(fr[0], true, fr[1], true, s, 1);
      }
      bar();
      STAMP(10);
      f32x2 st[4]; f32x4 cs[6];
      float kg[4];
#pragma unroll
      for (int t = 0; t < 24; ++t) {
        acc[t & 3][t >> 2] = mfma16(fr[1].w[t >> 2], fr[1].x[t & 3], acc[t & 3][t >> 2]);
        SB();
        if (t < 4) st[t] = *(const LDS f32x2*)(lds + a_st + t * 128);
        else if (t < 10) cs[t - 4] = *(const LDS f32x4*)(lds + a_cs + ((t - 4) / 3) * 384 + ((t - 4) % 3) * 64);
        else if (t >= 16 && t < 20) { kg[t - 16] = -1.44269504f * __builtin_amdgcn_rcpf(st[t - 16][1]); asm volatile("" : "+v"(kg[t - 16])); }
        SB();
      }
      STAMP(11);
#pragma unroll
      for (int mp = 0; mp < 2; ++mp) {
        if (mp) bar();
#pragma unroll
        for (int u = 0; u < 3; ++u) {
          unsigned o[2][2];
#pragma unroll
          for (int mi = 0; mi < 2; ++mi) {
            const int m = mp * 2 + mi;
            if (mp && u < 2) {
#pragma unroll
              for (int k = 0; k < 4; ++k) rdwd(u * 2 + mi, k);
            }
            const float nm = st[m][0], sd = st[m][1];
#pragma unroll
            for (int e = 0; e < 2; ++e) {
              const f32x2 gg = fma2(dup(nm), f32x2{cs[u][2 * e], cs[u][2 * e + 1]}, f32x2{acc[m][u][2 * e], acc[m][u][2 * e + 1]}) * dup(kg[m]);
              const f32x2 uu = fma2(dup(nm), f32x2{cs[u + 3][2 * e], cs[u + 3][2 * e + 1]}, f32x2{acc[m][u + 3][2 * e], acc[m][u + 3][2 * e + 1]});
              const f32x2 dn = fma2(f32x2{__builtin_amdgcn_exp2f(gg[0]), __builtin_amdgcn_exp2f(gg[1])}, dup(sd), dup(sd));
              const f32x2 hv = uu * f32x2{__builtin_amdgcn_rcpf(dn[0]), __builtin_amdgcn_rcpf(dn[1])};
              o[mi][e] = pk2(hv[0], hv[1]);
            }
          }
          const auto r0 = __builtin_amdgcn_permlane16_swap(o[0][0], o[1][0], false, false);
          const auto r1 = __builtin_amdgcn_permlane16_swap(o[0][1], o[1][1], false, false);
          const u32x4 ov = {r0[0], r1[0], r0[1], r1[1]};
          st16(ov, rws, lane * 16 + mp * (NU * 1024), so_h[u]);
          SB();
        }
      }
      STAMP(12);
      bar();
      waitv<0>();
      __builtin_amdgcn_raw_buffer_store_b64(u32x2{(unsigned)tag, (unsigned)(tag >> 32)}, rws, vflag, 0, SC1);
    } else {
      const int cj = (xcd & 3) * 2 + (r - 30), kg = (tid * 745) >> 14, cc = tid - kg * 22, col = cj * 176 + cc * 8;
      const unsigned vo = (kg < 11 && col < F) ? kg * (F * 2) + col * 2 : 0x7f000000u;
      u32x4 cv[2][8];
      auto ldcs = [&](int sl) INL {
#pragma unroll
        for (int i = 0; i < 8; ++i) cv[sl & 1][i] = __builtin_amdgcn_raw_buffer_load_b128(rw, vo + (sl * 8 + i) * (11 * F * 2), 0, 0);
      };
      ldcs(0);
      waitv<9>();
      layernorm();
      f32x2 a[4] = {};
#pragma unroll
      for (int sl = 0; sl < 6; ++sl) {
        if (sl == 1) waitv<0>();
        if (sl < 5) ldcs(sl + 1);
        if (sl == 0) touch_wd();
        if (sl < 4) bar();
#pragma unroll
        for (int i = 0; i < 8; ++i)
#pragma unroll
          for (int e = 0; e < 4; ++e) { const unsigned q = cv[sl & 1][i][e]; a[e] += f32x2{blo(q), bhi(q)}; }
        asm volatile("" : "+v"(a[0]), "+v"(a[1]), "+v"(a[2]), "+v"(a[3]));
      }
      if (kg < 11) {
        *(LDS f32x4*)(lds + kg * 704 + cc * 32) = f32x4{a[0][0], a[0][1], a[1][0], a[1][1]};
        *(LDS f32x4*)(lds + kg * 704 + cc * 32 + 16) = f32x4{a[2][0], a[2][1], a[3][0], a[3][1]};
      }
      bar();
      if (tid < 176) {
        float s = 0.f;
#pragma unroll
        for (int k = 0; k < 11; ++k) s += *(const LDS float*)(lds + k * 704 + tid * 4);
        __builtin_amdgcn_raw_buffer_store_b64(u32x2{f2u(s), (unsigned)tag}, rws, tid * 8, WS_CS + ((xcd >> 2) * CSP + cj * 176) * 8, SC1);
      }
      h_addr();
      bar(); bar(); bar(); bar(); bar();
#pragma unroll
      for (int k = 0; k < 16; ++k) rdwd(k >> 2, k & 3);
      bar();
    }

    STAMP(13);
    poll<2>(ws, vpoll, tag);
    STAMP(14);
    auto fs = [](int n) INL { return n < 32 ? n >> 2 : 8 + (n - 32) % 3; };
    auto fm = [](int n) INL { return ((n < 32 ? n : (n - 32) / 3) + 1) & 3; };
    s16x8 hf[44];
    auto ldh = [&](int n) INL {
      hf[n] = __builtin_bit_cast(s16x8, __builtin_amdgcn_raw_buffer_load_b128(rws, vh[fm(n)], ko[fs(n)], SC1));
    };
    f32x4 a2[4][4] = {};
#pragma unroll
    for (int n = 0; n < 4 * HD; ++n) { ldh(n); SB(); }
    unsigned landed;
    SB();
#pragma unroll
    for (int n = 0; n < 44; ++n) {
      if (n == 14) landed = *(volatile LDS unsigned*)(lds + SYNC + 16 + w4 * 4);
      if (n == 16) { if (!rfl(landed)) lds_wait(lds + SYNC + 16 + w4 * 4); fence(); }
#pragma unroll
      for (int u = 0; u < 4; ++u) {
        a2[fm(n)][u] = mfma16(wf[fs(n)][u], hf[n], a2[fm(n)][u]);
        SB();
        if (u == 1 && n >= 4 && n < 32) rdwd((n >> 2) + 3, n & 3);
        if (u == 3 && n + 4 * HD < 44) ldh(n + 4 * HD);
        if (n >= 35 && (n - 35) % 3 == 1) *(LDS f32x4*)(lds + wdloc((n - 35) / 3) + wl + u * 1024 + lane * 16) = a2[(n - 35) / 3 + 1][u];
        SB();
      }
      if (n == 3) STAMP(15);
    }
    STAMP(16);
    bar();
    STAMP(17);
    unsigned o[4][2];
#pragma unroll
    for (int u = 0; u < 4; ++u) {
      f32x4 s = a2[0][u];
#pragma unroll
      for (int m = 1; m < 4; ++m) s += *(const LDS f32x4*)(lds + wdloc(m - 1) + ((w4 - m) & 3) * 8192 + u * 1024 + lane * 16);
      s += *(const LDS f32x4*)(lds + SBASE + (u >> 1) * PAIR + (u & 1) * 4096 + w4 * 1024 + lane * 16);
      o[u][0] = pk2(s[0], s[1]); o[u][1] = pk2(s[2], s[3]);
    }
#pragma unroll
    for (int up = 0; up < 2; ++up) {
      const auto r0 = __builtin_amdgcn_permlane16_swap(o[2 * up][0], o[2 * up + 1][0], false, false);
      const auto r1 = __builtin_amdgcn_permlane16_swap(o[2 * up][1], o[2 * up + 1][1], false, false);
      const u32x4 ov = {r0[0], r1[0], r0[1], r1[1]};
      st16(ov, mkr(out, L * D * 2), j16 * 1024 + (g & 1) * 32 + (g >> 1) * 16 + up * 64, (m2 * 64 + w4 * 16) * 1024 + n2 * 128);
    }
    STAMP(18);
    STAMP_OUT(0);
    asm volatile(".Lk_code_end0:");
  } else {
    const int cl = nw, hh = mw;
    addr_x();
    addr_w();
    asm volatile("" : "+v"(vx[0]), "+v"(vx[1]), "+v"(vw[0]), "+v"(vw[1]), "+v"(vw[2]));
    auto stage = [&](int s) INL {
      if (gemm) {
#pragma unroll
        for (int i = 0; i < 10; ++i) issue(s, i);
      }
    };
    waitv<1>();
    get_tag();
    STAMP(1);
    float prm[6];
#pragma unroll
    for (int i = 0; i < 6; ++i) asm volatile("v_lshlrev_b32 %0, 16, %1" : "=v"(prm[i]) : "v"(xr[i >> 2][i & 3]));
    const float p_ldt = prm[0], p_lar = prm[1], p_aim = prm[2], p_cr = prm[3], p_ci = prm[4], p_dsk = prm[5];
    const float dt = rb(exp_f(p_ldt));
    const float s2 = -rb(exp_f(p_lar)) * dt * 1.44269504f, th = p_aim * dt;
    const unsigned eb = min(max(wave_max(max(f2u(p_cr) << 1, f2u(p_ci) << 1)) >> 24, 1u), 253u);
    const float sc = u2f((254u - eb) << 23), ksc = 2.f * dt * u2f(eb << 23);
    f32x2 z1 = cpow2(s2, th, 1.f), z8c = cpow2(s2, -th, 8.f), zb = cpow2(s2, th, 256.f);
    asm volatile("" : "+v"(z1), "+v"(z8c), "+v"(zb));
    const cmulr z256 = mul_by(zb);
    STAMP(2);
    bar();
    LDS char* pr = lds + SBASE + cl * PAIR;
    LDS char* ks = pr + hh * KSW;
    auto put = [&](int row, f32x2 v) INL { *(LDS unsigned*)(ks + row * KROW + lane * 4) = pkh(v[0], v[1]); };
    auto putu = [&](int reg, int h, f32x2 v) INL { put((reg & 3) + 4 * h + 8 * (reg >> 2), v); };
    f16x8 vf[8];
    {
      f32x2 v[4];
      const cmulr m1 = mul_by(z8c);
      v[0] = f32x2{1.f, 0.f}; v[1] = z8c; v[2] = cmul(z8c, m1); v[3] = cmul(v[2], m1);
      const cmulr m4 = mul_by(cmul(v[3], m1));
#pragma unroll
      for (int k = 0; k < 32; ++k) {
        if (k == 4) v[0] = m4.z; else if (k > 4) v[k & 3] = cmul(v[k & 3], m4);
        put(k, v[k & 3]);
      }
      fence();
#pragma unroll
      for (int s = 0; s < 8; ++s) vf[s] = *(const LDS f16x8*)(ks + (lane & 31) * KROW + s * 32 + (lane >> 5) * 16);
      fence();
    }
    STAMP(3);
    {
      f32x2 e[5];
      const cmulr m1 = mul_by(z1), m2_ = mul_by(cmul(z1, m1));
      e[0] = f32x2{p_cr, p_ci} * dup(sc);
      putu(0, 0, e[0]);
#pragma unroll
      for (int k = 0; k < 4; ++k) { putu(2 * k + 1, 0, cmul(e[k], m1)); e[k + 1] = cmul(e[k], m2_); putu(2 * k + 2, 0, e[k + 1]); }
#pragma unroll
      for (int k = 0; k < 5; ++k) e[k] = cmul(e[k], z256);
      putu(0, 1, e[0]); putu(2, 1, e[1]); putu(3, 1, e[2]); putu(4, 1, e[2]); putu(5, 1, e[2]); putu(6, 1, e[3]); putu(7, 1, e[4]); putu(8, 1, e[4]);
#pragma unroll
      for (int k = 0; k < 6; ++k) {
        const int reg = k < 2 ? 9 + 2 * k : k < 5 ? 5 + 2 * k : 13, h = k < 2 || k == 5;
        e[0] = cmul(e[0], z256); putu(reg, h, e[0]);
        e[4] = cmul(e[4], z256); putu(reg + 1, h, e[4]);
        if (k < 2) { e[2] = cmul(e[2], z256); putu(k ? 1 : 15, 1, e[2]); }
      }
      fence();
    }
    STAMP(4);
    f32x16 kd = {};
    {
      f16x8 uf[8];
#pragma unroll
      for (int s = 0; s < 8; ++s) uf[s] = *(const LDS f16x8*)(ks + (lane & 31) * KROW + s * 32 + (lane >> 5) * 16);
      SB();
#pragma unroll
      for (int s = 0; s < 8; ++s) kd = __builtin_amdgcn_mfma_f32_32x32x16_f16(uf[s], vf[s], kd, 0, 0, 0);
    }
    kd *= ksc;
    if (hh) {
      *(LDS f32x4*)(pr + KEX1 + lane * 16) = f32x4{kd[0], kd[1], kd[2], kd[3]};
      *(LDS f32x4*)(pr + KEX1 + 1024 + lane * 16) = f32x4{kd[4], kd[5], kd[6], kd[7]};
      *(LDS f32x4*)(pr + KEX1 + 2048 + lane * 16) = f32x4{kd[8], kd[13], kd[14], kd[15]};
    } else {
      *(LDS f32x4*)(pr + KEX0 + lane * 16) = f32x4{kd[9], kd[10], kd[11], kd[12]};
      *(LDS f32x2*)(pr + KEX0 + 1024 + lane * 8) = f32x2{kd[15], kd[1]};
    }
    STAMP(5);
    bar();
    stage(2);
    {
      const int bb = lane & 31;
      const bool h = lane >> 5, odd = lane & 1;
      auto chunk = [&](int c, float a0, float a1, float a2, float a3, float a4, float a5, float a6, float a7, float a8) INL {
        *(LDS u32x4*)(pr + TX + (255 - c) * 16) = u32x4{pk2(a7, a6), pk2(a5, a4), pk2(a3, a2), pk2(a1, a0)};
        *(LDS u32x4*)(pr + TY + (255 - c) * 16) = u32x4{pk2(a8, a7), pk2(a6, a5), pk2(a4, a3), pk2(a2, a1)};
      };
      if (hh) {
        const f32x4 p = *(const LDS f32x4*)(pr + KEX0 + lane * 16);
        const f32x2 q = *(const LDS f32x2*)(pr + KEX0 + 1024 + lane * 8);
#pragma unroll
        for (int k = 0; k < 2; ++k) {
          const float a = kd[9 + 2 * k] + p[2 * k], c = kd[10 + 2 * k] + p[2 * k + 1], m = kd[k ? 1 : 15] + q[k];
          chunk((h ? 64 : 128) + 32 * k + bb, a, a, a, h ? m : a, h ? m : odd ? c : a, h ? m : c, c, c, c);
        }
      } else {
        const f32x4 p0 = *(const LDS f32x4*)(pr + KEX1 + lane * 16), p1 = *(const LDS f32x4*)(pr + KEX1 + 1024 + lane * 16);
        const f32x4 p2 = *(const LDS f32x4*)(pr + KEX1 + 2048 + lane * 16);
        const float a0 = kd[0] + p0[0] + (lane == 0 ? p_dsk : 0.f);
        chunk(lane, a0, h ? a0 : kd[1] + p0[1], kd[2] + p0[2], kd[3] + p0[3], kd[4] + p1[0], kd[5] + p1[1], kd[6] + p1[2], kd[7] + p1[3], kd[8] + p2[0]);
        const float a = kd[13] + p2[1], c = kd[14] + p2[2];
        chunk(192 + lane, a, a, a, a, odd ? c : a, c, c, c, c);
        if (lane < 4) *(LDS u32x4*)(pr + (lane & 2 ? TX : TY) + 4096 + (lane & 1) * 16) = u32x4{lane == 0 ? pk2(a0, 0.f) : 0, 0, 0, 0};
      }
    }
    STAMP(6);
    bar();
    stage(3);
    if (w4 == 0) setflag(ws, WS_XNF + b * 8, tag, lane);
    if (tid < 62) *(LDS u32x4*)(lds + (tid < 60 ? SBASE + (tid & 1) * PAIR + XIMG + (tid >> 1) * 16 : SYNC + (tid & 1) * 16)) = u32x4{0, 0, 0, 0};
    waitv<10>();
    bar();
    stage(4);
    unsigned vd[4], vxt, vcs, pa[4];
#pragma unroll
    for (int i = 0; i < 4; ++i) { const int row = lane >> 3, f = ((row >> 1) & 1) + 2 * (i & 1); vd[i] = (i * 8 + row) * 1024 + n2 * 128 + (((lane & 7) ^ (2 * f)) << 4); }
    vxt = (lane >> 3) * 1024 + (((lane & 7) ^ g) << 4);
    vcs = tid < 96 ? (n0 + tid) * 8 : (CSP + n0 + min(tid, 191) - 96) * 8;
#pragma unroll
    for (int i = 0; i < 4; ++i) pa[i] = SBASE + cl * PAIR + (j16 & 1 ? TX : TY) + (847 - 256 * i - hh * 64 - ((15 + j16 - 8 * g) >> 1)) * 4;
    asm volatile("" : "+v"(vd[0]), "+v"(vd[1]), "+v"(vd[2]), "+v"(vd[3]), "+v"(vxt), "+v"(vcs), "+v"(pa[0]), "+v"(pa[1]), "+v"(pa[2]), "+v"(pa[3]));
    auto issue_wd = [&](int s, int p) INL {
      const int pos = s % 7;
      dma16((pos == 6 ? wl6 : wdloc(pos) + wl) + p * 1024, vd[p], rwd, ko[s]);
    };
    waitv<10>();
    bar();
    stage(5);
    ksteps(32768, 0);
    waitv<10>();
    bar();
    stage(6);
    waitv<10>();
    bar();
    {
      u64 fl;
      asm volatile("buffer_load_dwordx2 %0, %1, %2, %3 offen sc1" : "=v"(fl) : "v"(tid * 8), "s"(rws), "s"(WS_XNF) : "memory");
      stage(7);
      if (gemm) waitv<10>(); else waitv<0>();
      asm volatile("" : "+v"(fl));
      if (!wave_all(fl == tag)) poll<4>(ws, WS_XNF + tid * 8, tag);
    }
    STAMP(7);
    bar();
    unsigned gst; u32x2 csg; u32x4 xi[2];
    asm volatile("buffer_load_dword %0, %1, %2, %3 offen sc1" : "=v"(gst) : "v"((tid & 127) * 4), "s"(rws), "s"(WS_ST + (mt & 15) * 512) : "memory");
    asm volatile("buffer_load_dwordx2 %0, %1, %2, %3 offen sc1" : "=v"(csg) : "v"(vcs), "s"(rws), "s"(WS_CS) : "memory");
#pragma unroll
    for (int i = 0; i < 2; ++i)
      asm volatile("buffer_load_dwordx4 %0, %1, %2, %3 offen sc1" : "=v"(xi[i]) : "v"((tid >> 1) * 8192 + (tid & 1) * 16), "s"(rws), "s"(WS_XNT + cp * 32 + i * (128 * 8192)) : "memory");
    waitv<4>();
    STAMP(8);
    bar();
#pragma unroll
    for (int i = 0; i < 2; ++i) dma16(SBASE + cl * PAIR + CEX + hh * 2048 + i * 1024, vxt ^ (i * 64), rx, (m2 * 64 + w4 * 16 + i * 8) * 1024 + n2 * 128);
    waitv<4>();
    asm volatile("" : "+v"(gst), "+v"(csg));
    if (tid < 128) *(LDS f32x2*)(lds + STO + tid * 8) = f32x2{-blo(gst), bhi(gst)};
    if (gemm) {
#pragma unroll 1
      for (unsigned spins = 0; spins < SPIN_CAP && !wave_all(csg[1] == (unsigned)tag); ++spins) {
        __builtin_amdgcn_s_sleep(2);
        asm volatile("buffer_load_dwordx2 %0, %1, %2, %3 offen sc1\n\ts_waitcnt vmcnt(0)" : "=&v"(csg) : "v"(vcs), "s"(rws), "s"(WS_CS) : "memory");
      }
    }
    *(LDS unsigned*)(lds + CSO + tid * 4) = csg[0];
#pragma unroll
    for (int i = 0; i < 8; ++i) issue_wd(i >> 2, i & 3);
    STAMP(9);
    bar();
#pragma unroll
    for (int i = 8; i < 16; ++i) issue_wd(i >> 2, i & 3);
    waitv<18>();
    asm volatile("" : "+v"(xi[0]), "+v"(xi[1]));
#pragma unroll
    for (int i = 0; i < 2; ++i) *(LDS u32x4*)(lds + SBASE + (tid & 1) * PAIR + XIMG + XPAD + (i * 128 + (tid >> 1)) * 16) = xi[i];
    if (b == 0 && w4 == 0) setflag(ws, WS_E, tag, lane);
#pragma unroll
    for (int i = 16; i < 24; ++i) issue_wd(i >> 2, i & 3);
    waitv<8>();
    STAMP(10);
    bar();
#pragma unroll
    for (int i = 24; i < 28; ++i) issue_wd(i >> 2, i & 3);
    u32x2 xt[4];
#pragma unroll
    for (int m = 0; m < 4; ++m)
      xt[m] = *(const LDS u32x2*)(lds + SBASE + (m >> 1) * PAIR + CEX + (m & 1) * 2048 + j16 * 128 + (((2 * w4 + (g >> 1)) ^ (j16 >> 1)) << 4) + (g & 1) * 8);
    LDS char* pb = pr + XIMG + XPAD + (16 * j16 + 8 * g - 16) * 2 - hh * 256 - 192;
    CvFrag cf[2];
    auto rdc = [&](CvFrag& f, int rr, int k) INL {
      if (k & 1) f.b[k >> 1] = *(const LDS s16x8*)(pb + (k >> 1) * 512 + (3 - rr) * 64);
      else {
        const LDS s16x4u* q = (const LDS s16x4u*)(lds + pa[k >> 2] + (k & 2 ? 0 : 512) + (3 - rr) * 64);
        f.a[k >> 1] = cat(q[0], q[1]);
      }
    };
#pragma unroll
    for (int k = 0; k < 16; ++k) rdc(cf[0], 0, k);
    waitv<0>();
    bar();
    STAMP(11);
    f32x4 ca[8] = {};
#pragma unroll
    for (int rr = 0; rr < 4; ++rr) {
      int n = 0;
#pragma unroll
      for (int u = 0; u < 8; ++u)
#pragma unroll
        for (int t = u; t < 8; ++t) {
          ca[t] = mfma16(cf[rr & 1].a[u], cf[rr & 1].b[t - u], ca[t]);
          SB();
          if (rr < 3 && (n & 1) == 0 && (n >> 1) < 16) rdc(cf[(rr + 1) & 1], rr + 1, n >> 1);
          const int q = rr * 6 + n / 6;
          if (n % 6 == 1 && q < 16) issue_wd(7 + (q >> 2), q & 3);
          SB();
          ++n;
        }
    }
    STAMP(12);
    {
      LDS char* exa = pr + CEX + lane * 16;
      LDS char* exb = lds + SLOT + LEFT + cl * 4096 + lane * 16;
      f32x4 keep[4];
#pragma unroll
      for (int k = 0; k < 4; ++k) {
        *(LDS f32x4*)((hh ? exa : exb) + k * 1024) = hh ? ca[k] : ca[k + 4];
        keep[k] = hh ? ca[k + 4] : ca[k];
      }
      fence();
      *(volatile LDS unsigned*)(lds + SYNC + w4 * 4) = 1u;
      waitv<0>();
      *(volatile LDS unsigned*)(lds + SYNC + 16 + w4 * 4) = 1u;
      lds_wait(lds + SYNC + (w4 ^ 1) * 4);
      unsigned o[4][2];
#pragma unroll
      for (int k = 0; k < 4; ++k) {
        const f32x4 s = keep[k] + *(const LDS f32x4*)((hh ? exb : exa) + k * 1024);
        o[k][0] = pk2(s[0], s[1]); o[k][1] = pk2(s[2], s[3]);
      }
#pragma unroll
      for (int kp = 0; kp < 2; ++kp) {
        const auto r0 = __builtin_amdgcn_permlane16_swap(o[2 * kp][0], o[2 * kp + 1][0], false, false);
        const auto r1 = __builtin_amdgcn_permlane16_swap(o[2 * kp][1], o[2 * kp + 1][1], false, false);
        const u32x4 ov = {r0[0], r1[0], r0[1], r1[1]};
        st16(ov, rws, ((g & 1) * 256 + 16 * j16 + 8 * (g >> 1)) * 2, WS_YT + ch * 4096 + (hh * 4 + kp * 2) * 512);
      }
      STAMP(13);
      waitv<0>();
      setflag(ws, WS_YF + (hh * 512 + ch) * 8, tag, lane);
    }
    STAMP(14);
    __builtin_amdgcn_s_sleep(YSLEEP);
    poll<8>(ws, WS_YF + ((m2 >> 4) * 512 + n2 * 64 + w4 * 16 + j16) * 8, tag);
    STAMP(15);
    {
      LDS char* yb = pr + hh * 4096;
#pragma unroll
      for (int i = 0; i < 2; ++i) {
        const u32x4 y = __builtin_amdgcn_raw_buffer_load_b128(rws, (lane >> 3) * 4096 + (lane & 7) * 16, WS_YT + (n2 * 64 + w4 * 16 + i * 8) * 4096 + m2 * 128, SC1);
        *(LDS u32x4*)(yb + (i * 8 + (lane >> 3)) * 160 + (lane & 7) * 16) = y;
      }
      fence();
      u32x2 y[4];
#pragma unroll
      for (int m = 0; m < 4; ++m) y[m] = __builtin_bit_cast(u32x2, trd(yb + (4 * g + (j16 >> 2)) * 160 + m * 32 + (j16 & 3) * 8));
      waitl0();
#pragma unroll
      for (int m = 0; m < 4; ++m) {
        const unsigned y0 = y[m][0], y1 = y[m][1], q0 = xt[m][0], q1 = xt[m][1];
        *(LDS f32x4*)(yb + m * 1024 + lane * 16) = f32x4{blo(y0) + blo(q0), bhi(y0) + bhi(q0), blo(y1) + blo(q1), bhi(y1) + bhi(q1)};
      }
    }
    STAMP(16);
    bar();
    STAMP_OUT(1);
    asm volatile(".Lk_code_end1:");
  }
}

}

extern "C" void kernel_launch(void* const* d_in, const int* in_sizes, int n_in, void* d_out, int out_size, void* d_ws, size_t ws_size,
                              hipStream_t stream) {
  fused_kernel<<<256, 512, 0, stream>>>((const u16*)d_in[0], (const u16*)d_in[1], (const u16*)d_in[2], (const u16*)d_in[3], (const u16*)d_in[4],
                                        (const u16*)d_in[5], (const u16*)d_in[6], (const u16*)d_in[8], (const u16*)d_in[9], (const u16*)d_in[10],
                                        (u16*)d_out, (char*)d_ws);
}
```

```cpp
#include <hip/hip_runtime.h>
#include <hip/hip_bf16.h>

namespace {

typedef unsigned short u16;
typedef unsigned long long u64;
typedef __attribute__((ext_vector_type(4))) short s16x4;
typedef s16x4 __attribute__((aligned(4))) s16x4u;
typedef __attribute__((ext_vector_type(8))) short s16x8;
typedef __attribute__((ext_vector_type(8))) __bf16 bf16x8;
typedef __attribute__((ext_vector_type(2))) __bf16 bf16x2;
typedef __attribute__((ext_vector_type(8))) _Float16 f16x8;
typedef __attribute__((ext_vector_type(2))) _Float16 f16x2;
typedef __attribute__((ext_vector_type(2))) float f32x2;
typedef __attribute__((ext_vector_type(4))) float f32x4;
typedef __attribute__((ext_vector_type(16))) float f32x16;
typedef __attribute__((ext_vector_type(4))) unsigned u32x4;
typedef __attribute__((ext_vector_type(2))) unsigned u32x2;
typedef __amdgpu_buffer_rsrc_t rsrc_t;
#define LDS __attribute__((address_space(3)))
#define GLB __attribute__((address_space(1)))
#define INL __attribute__((always_inline))
#define SB() __builtin_amdgcn_sched_barrier(0)
#define RLX_AGENT __ATOMIC_RELAXED, __HIP_MEMORY_SCOPE_AGENT
#define RLX_WG __ATOMIC_RELAXED, __HIP_MEMORY_SCOPE_WORKGROUP

constexpr int L = 2048, D = 512, NM = 128, F = 1376, NU = F / 16;
constexpr int SC1 = 16;
#define HD 3
#define PRIO(x)
#define TW3 11
#define SPRIO(x) __builtin_amdgcn_s_setprio(x)
#define HFS 64
#define YFS 64
#define HNAP 2
#define YNAP 8
#define RD0 1
#define RD1 3
#define PACE 4
#define XTP 0
#define TWP 0
#define PEND 0
#define D2F 6
#define NW7 2
#define NOTAB 0
#define YSLEEP 16
constexpr unsigned SPIN_CAP = 1u << 18;

constexpr int WBLK = 3104, GOFF = 16384, UOFF = GOFF + 4 * WBLK, SLOT = GOFF + 8 * WBLK;
constexpr int LEFT = 32768;
constexpr int LNB = 2 * SLOT, LNROW = 1088;
constexpr int SBASE = 3 * SLOT;
constexpr int KROW = 272, KSW = 32 * KROW, PAIR = 2 * KSW;
constexpr int TX = 0, XIMG = 4160, XPAD = 480, CEX = 8736, TY = 13248, KEX0 = 7168, KEX1 = KSW;
constexpr int STO = SBASE + 2 * PAIR, CSO = STO + 1024, SYNC = CSO + 1024, LANDED = SYNC + 16, USED = SYNC + 32, DUMMY = SYNC + 64;
constexpr int LDS_BYTES = 163840;
static_assert(DUMMY + 256 <= LDS_BYTES && XIMG + XPAD + 4096 <= CEX && CEX + 4096 <= TY && TY + 4128 <= PAIR, "LDS map");

constexpr int WS_E = 0, WS_XNF = 4096, WS_HF = 65536, WS_YF = 196608, WS_ST = 32768, WS_CS = 40960;
constexpr int XNP = 8320, WS_XNT = 1 << 19, WS_YT = 3 << 20, WS_H = 5 << 20, WS_END = WS_H + 64 * NU * 1024;
constexpr int CSP = 1408;

__device__ __forceinline__ float u2f(unsigned v) { return __builtin_bit_cast(float, v); }
__device__ __forceinline__ unsigned f2u(float v) { return __builtin_bit_cast(unsigned, v); }
__device__ __forceinline__ bool wave_all(bool p) { return __builtin_amdgcn_ballot_w64(!p) == 0; }
__device__ __forceinline__ unsigned rfl(unsigned v) { return __builtin_amdgcn_readfirstlane(v); }
__device__ __forceinline__ rsrc_t mkr(const void* p, unsigned n) { return __builtin_amdgcn_make_buffer_rsrc((void*)p, 0, n, 0x00020000); }
__device__ __forceinline__ void dma16(unsigned base, int dst, unsigned voff, rsrc_t r, unsigned soff, int off = 0) {
  asm volatile("s_add_u32 m0, %0, %1\n\ts_nop 0\n\tbuffer_load_dwordx4 %2, %3, %4 offen offset:%5 lds"
               ::"s"(base), "n"(dst - off), "v"(voff), "s"(r), "s"(soff), "n"(off) : "scc", "memory");
}
__device__ __forceinline__ void touch(unsigned m0v, unsigned voff, rsrc_t r, unsigned soff) {
  asm volatile("s_mov_b32 m0, %0\n\ts_nop 0\n\tbuffer_load_dword %1, %2, %3 offen lds" ::"s"(m0v), "v"(voff), "s"(r), "s"(soff) : "memory");
}
__device__ __forceinline__ void st16(u32x4 v, rsrc_t r, unsigned voff, unsigned soff) {
  asm volatile("buffer_store_dwordx4 %0, %1, %2, %3 offen sc1\n\ts_nop 1" ::"v"(v), "v"(voff), "s"(r), "s"(soff) : "memory");
}
template <int W> __device__ __forceinline__ void waitv() { asm volatile("s_waitcnt vmcnt(%0)" ::"n"(W) : "memory"); }
__device__ __forceinline__ void waitl0() { asm volatile("" ::: "memory"); __builtin_amdgcn_s_waitcnt(0xc07f); asm volatile("" ::: "memory"); }
__device__ __forceinline__ void fence() { asm volatile("" ::: "memory"); }
__device__ __forceinline__ void bar() { waitl0(); __builtin_amdgcn_s_barrier(); fence(); }
__device__ __forceinline__ unsigned pk2(float a, float b) { f32x2 p = {a, b}; return __builtin_bit_cast(unsigned, __builtin_convertvector(p, bf16x2)); }
__device__ __forceinline__ unsigned pkh(float a, float b) { f32x2 p = {a, b}; return __builtin_bit_cast(unsigned, __builtin_convertvector(p, f16x2)); }
__device__ __forceinline__ float blo(unsigned v) { return u2f(v << 16); }
__device__ __forceinline__ float bhi(unsigned v) { return u2f(v & 0xffff0000u); }
__device__ __forceinline__ float rb(float a) { return blo(pk2(a, a)); }
template <int C, int RM> __device__ __forceinline__ float dppf(float v) {
  return __builtin_bit_cast(float, __builtin_amdgcn_update_dpp(0, __builtin_bit_cast(int, v), C, RM, 0xf, true));
}
__device__ __forceinline__ float lane63(float v) { return __builtin_bit_cast(float, __builtin_amdgcn_readlane(__builtin_bit_cast(int, v), 63)); }
__device__ __forceinline__ float wave_sum(float v) {
  v += dppf<0x111, 0xf>(v); v += dppf<0x112, 0xf>(v); v += dppf<0x114, 0xf>(v); v += dppf<0x118, 0xf>(v);
  v += dppf<0x142, 0xa>(v); v += dppf<0x143, 0xc>(v);
  return lane63(v);
}
template <int C, int RM> __device__ __forceinline__ unsigned dppmax(unsigned v) {
  const unsigned o = __builtin_amdgcn_update_dpp(0, v, C, RM, 0xf, true);
  return v > o ? v : o;
}
__device__ __forceinline__ unsigned wave_max(unsigned v) {
  v = dppmax<0x111, 0xf>(v); v = dppmax<0x112, 0xf>(v); v = dppmax<0x114, 0xf>(v); v = dppmax<0x118, 0xf>(v);
  v = dppmax<0x142, 0xa>(v); v = dppmax<0x143, 0xc>(v);
  return __builtin_amdgcn_readlane(v, 63);
}
__device__ __forceinline__ s16x8 cat(s16x4 a, s16x4 b) { return __builtin_shufflevector(a, b, 0, 1, 2, 3, 4, 5, 6, 7); }
__device__ __forceinline__ s16x4 trd(LDS char* p) { return __builtin_amdgcn_ds_read_tr16_b64_v4i16((LDS s16x4*)p); }
__device__ __forceinline__ f32x4 mfma16(s16x8 a, s16x8 b, f32x4 c) {
  return __builtin_amdgcn_mfma_f32_16x16x32_bf16(__builtin_bit_cast(bf16x8, a), __builtin_bit_cast(bf16x8, b), c, 0, 0, 0);
}
template <int NAP> __device__ __forceinline__ void poll(const char* ws, unsigned off, u64 tag) {
  GLB u64* p = (GLB u64*)(ws + off);
#pragma unroll 1
  for (unsigned spins = 0; spins < SPIN_CAP; ++spins) {
    if (wave_all(__hip_atomic_load(p, RLX_AGENT) == tag)) break;
    __builtin_amdgcn_s_sleep(NAP);
  }
  fence();
}
__device__ __forceinline__ void lds_wait(LDS char* p, unsigned v) {
#pragma unroll 1
  for (unsigned spins = 0; spins < SPIN_CAP; ++spins) { if (rfl(*(volatile LDS unsigned*)p) >= v) break; __builtin_amdgcn_s_sleep(1); }
  fence();
}
__device__ __forceinline__ void setflag(char* ws, unsigned off, u64 tag, int lane) {
  if (lane == 0) __hip_atomic_store((GLB u64*)(ws + off), tag, RLX_AGENT);
}
__device__ __forceinline__ f32x2 fma2(f32x2 a, f32x2 b, f32x2 c) { return __builtin_elementwise_fma(a, b, c); }
__device__ __forceinline__ float one(float v) { asm("" : "+v"(v)); return v; }
__device__ __forceinline__ f32x2 dup(float a) { return f32x2{a, a}; }
struct cmulr { f32x2 z, rot; };
__device__ __forceinline__ cmulr mul_by(f32x2 z) { return {z, f32x2{-z[1], z[0]}}; }
__device__ __forceinline__ f32x2 cmul(f32x2 a, cmulr m) { return fma2(dup(a[0]), m.z, dup(a[1]) * m.rot); }
__device__ __forceinline__ f32x2 cpow2(float s2, float th, float p) {
  const float t = th * p, n = __builtin_rintf(t * 0.15915494f);
  const float fr = __builtin_fmaf(t, 0.15915494f, -n) + t * 6.4206382e-9f;
  const float e = __builtin_amdgcn_exp2f(s2 * p);
  return f32x2{e * __builtin_amdgcn_cosf(fr), e * __builtin_amdgcn_sinf(fr)};
}
__device__ __forceinline__ float exp_f(float v) {
  const float t = v * 1.44269504f, e = __builtin_fmaf(v, 1.925963033e-8f, __builtin_fmaf(v, 1.44269504f, -t));
  const float y = __builtin_amdgcn_exp2f(t);
  return __builtin_fmaf(y, e * 0.69314718f, y);
}

#define STAMP(i)
#define STAMP_OUT(role)

struct G1Frag { s16x8 x[4]; s16x8 w[6]; };
struct CvFrag { s16x8 a[8]; s16x8 b[8]; };

__global__ __launch_bounds__(512, 2) void fused_kernel(const u16* x, const u16* lar, const u16* aim, const u16* cre, const u16* cim, const u16* ldt,
                                                       const u16* dsk, const u16* wg, const u16* wu, const u16* wd, u16* out, char* ws) {
  __shared__ __attribute__((aligned(16))) char lds_raw[LDS_BYTES];
  LDS char* lds = (LDS char*)lds_raw;
  int w = rfl(threadIdx.x >> 6);
  const int lane = threadIdx.x & 63, tid = threadIdx.x & 255;
  const int b = blockIdx.x, xcd = b & 7, r = b >> 3;
  STAMP(0);
  if (w == 7) {
    u64 pc; unsigned l0, l1;
    asm volatile("s_getpc_b64 %0\n.Lk_code_begin:\n\ts_mov_b32 %1, .Lk_code_end0-.Lk_code_begin\n\ts_mov_b32 %2, .Lk_code_end1-.Lk_code_begin" : "=s"(pc), "=s"(l0), "=s"(l1));
    touch(DUMMY, lane * 1024, mkr((const void*)pc, max(l0, l1) - 896), (r & 7) * 128);
  }
  const int row0 = (xcd & 1) * 1024 + (xcd >> 1) * 256 + r * 8;
  const unsigned vt = lane < 16 ? (lane >> 3) * (64 * F * 2) + (lane & 7) * 96 : 0x7f000000u;
  const unsigned so_t = ((w & 3) * 128 + (xcd & 1) * 32 + r) * (F * 2) + (xcd >> 1) * 688;
  asm volatile("" ::"s"(row0), "v"(vt), "s"(so_t));
  STAMP(28);
  asm volatile("" : "+s"(x), "+s"(lar), "+s"(aim), "+s"(cre), "+s"(cim), "+s"(ldt), "+s"(dsk), "+s"(wg), "+s"(wu), "+s"(wd), "+s"(out), "+s"(ws));
  STAMP(24);
  const rsrc_t rx = mkr(x, L * D * 2);
  const int w4 = w & 3, mw = w & 1, nw = w4 >> 1;
  const int cp = xcd * 32 + r, ch = cp * 2 + nw;
  u32x4 xr[2];
  u64 tag;
  if (w < 4) {
#pragma unroll
    for (int i = 0; i < 2; ++i)
      asm volatile("buffer_load_dwordx4 %0, %1, %2, %3 offen" : "=v"(xr[i]) : "v"(lane * 16), "s"(rx), "s"((row0 + 2 * w4 + i) * 1024) : "memory");
  } else {
    const unsigned pi = (ch * NM + mw * 64 + lane) * 2;
    asm volatile("global_load_ushort %0, %6, %8\n\tglobal_load_ushort %1, %7, %9\n\tglobal_load_ushort %2, %7, %10\n\tglobal_load_ushort %3, %7, %11\n\t"
                 "global_load_ushort %4, %7, %12\n\tglobal_load_ushort %5, %6, %13"
                 : "=&v"(xr[0][0]), "=&v"(xr[0][1]), "=&v"(xr[0][2]), "=&v"(xr[0][3]), "=&v"(xr[1][0]), "=&v"(xr[1][1])
                 : "v"(ch * 2), "v"(pi), "s"(ldt), "s"(lar), "s"(aim), "s"(cre), "s"(cim), "s"(dsk) : "memory");
  }
  u64 epoch;
  asm volatile("s_load_dwordx2 %0, %1, 0x0" : "=s"(epoch) : "s"(ws) : "memory");
  __builtin_amdgcn_s_barrier();
  STAMP(25);
  touch(DUMMY, vt, mkr(w >> 2 ? wu : wg, D * F * 2), so_t);
  auto get_tag = [&]() INL {
    unsigned lo, hi;
    asm volatile("s_waitcnt lgkmcnt(0)\n\ts_add_u32 %0, %2, 0x7F4A7C15\n\ts_addc_u32 %1, %3, 0x9E3779B9"
                 : "=&s"(lo), "=s"(hi) : "s"((unsigned)epoch), "s"((unsigned)(epoch >> 32)) : "scc", "memory");
    tag = (u64)hi << 32 | lo;
  };
  asm volatile("" : "+s"(w));
  SB();

  const rsrc_t rws = mkr(ws, WS_END), rwd = mkr(wd, F * D * 2);
  const bool gemm = r < 30;
  const int idx = (xcd >> 1) * 30 + r, nt = idx >> 3, mt = (xcd & 1) * 8 + (idx & 7);
  const int n0 = nt < 14 ? nt * 96 : 1280;
  const int m2 = (xcd & 1) * 16 + (xcd >> 1) * 4 + (r >> 3), n2 = r & 7;
  const int j16 = lane & 15, g = lane >> 4;

  const rsrc_t rw = mkr((gemm ? nw : xcd >> 2) ? wu : wg, D * F * 2);
  unsigned vx[2], vw[3], lx = w4 * 4096, lw = (nw ? UOFF : GOFF) + mw * 2 * WBLK;
  asm volatile("" : "+s"(lx), "+s"(lw));
  auto addr_x = [&]() INL {
#pragma unroll
    for (int i = 0; i < 2; ++i) {
      const int row = i * 8 + (lane >> 3), id = (row >> 1) & 7, f = (id & 4) | ((id & 1) << 1) | ((id >> 1) & 1);
      vx[i] = (mt * 128 + w4 * 32 + row) * 1024 + (((lane & 7) ^ f) << 4);
    }
  };
  auto addr_w = [&]() INL {
    const int kr = (lane * 171) >> 11, c = lane - kr * 12;
    vw[0] = (mw * 32 + kr) * (F * 2) + c * 16 + n0 * 2;
    vw[1] = vw[0] + (c >= 8 ? 6 * F * 2 - 128 : 5 * F * 2 + 64);
    vw[2] = vw[0] + (c >= 4 ? 11 * F * 2 - 64 : 10 * F * 2 + 128);
  };
  auto issue = [&](int s, int i) INL {
    const int slot = (s % 3) * SLOT, j = i - 4;
    if (i < 4) dma16(lx, slot + i * 1024, vx[i & 1], rx, s * 128 + (i >> 1) * 16384);
    else dma16(lw, slot + (j / 3) * WBLK + (j % 3) * 1024, vw[j % 3], rw, s * (64 * F * 2) + (j / 3) * 16 * F * 2);
  };
  const unsigned wl = w4 * 8192, wl6 = (nw ? 0 : 2 * SLOT) + LEFT + mw * 4096;
  auto wdloc = [](int pos) INL { return ((pos / 2 + 2) % 3) * SLOT + (pos & 1) * 4096; };

  auto post = [&](int word, unsigned v) INL { fence(); *(volatile LDS unsigned*)(lds + word + w4 * 4) = v; fence(); };
  auto peek = [&](int word) INL { return *(volatile LDS unsigned*)(lds + word + w4 * 4); };
  auto await = [&](int word, unsigned seen, unsigned v) INL { if (__builtin_expect(rfl(seen) < v, 0)) lds_wait(lds + word + w4 * 4, v); fence(); };

  if (__builtin_expect(w < 4, 1)) {
    auto layernorm = [&](auto&& hook) INL {
      get_tag();
      STAMP(1);
      u32x2 stat;
#pragma unroll
      for (int i = 0; i < 2; ++i) {
        f32x2 v[4];
#pragma unroll
        for (int e = 0; e < 4; ++e) asm volatile("v_lshlrev_b32 %0, 16, %2\n\tv_and_b32 %1, 0xffff0000, %2" : "=&v"(v[e][0]), "=v"(v[e][1]) : "v"(xr[i][e]));
        hook(5 * i);
        const float x0 = u2f(rfl(f2u(v[0][0])));
#pragma unroll
        for (int e = 0; e < 4; ++e) v[e] -= dup(x0);
        const f32x2 s1 = (v[0] + v[1]) + (v[2] + v[3]), s2 = fma2(v[0], v[0], v[1] * v[1]) + fma2(v[2], v[2], v[3] * v[3]);
        float t1 = s1[0] + s1[1], t2 = s2[0] + s2[1];
        asm volatile("" : "+v"(t1), "+v"(t2));
        hook(5 * i + 1);
        const float md = wave_sum(t1) * (1.f / D);
        float var = __builtin_fmaf(-md, md, wave_sum(t2) * (1.f / D));
        asm volatile("" : "+v"(var));
        hook(5 * i + 2);
        const float m = rb(x0 + md), sd = rb(__builtin_amdgcn_sqrtf(rb(rb(__builtin_fmaxf(var, 0.f)) + 1e-5f))), rs = __builtin_amdgcn_rcpf(sd);
        float c = (x0 - m) * rs;
        asm volatile("" : "+v"(c));
        hook(5 * i + 3);
        u32x4 o;
#pragma unroll
        for (int e = 0; e < 4; ++e) { const f32x2 n = fma2(v[e], dup(rs), dup(c)); o[e] = pk2(n[0], n[1]); }
        *(LDS u32x4*)(lds + LNB + (2 * w4 + i) * LNROW + lane * 16) = o;
        hook(5 * i + 4);
        stat[i] = pk2(m, sd);
      }
      __builtin_amdgcn_raw_buffer_store_b64(stat, rws, lane ? 0x7f000000u : 0u, WS_ST + (row0 + 2 * w4) * 4, SC1);
      STAMP(21);
      bar();
      STAMP(22);
#pragma unroll
      for (int i = 0; i < 2; ++i) {
        LDS char* p = lds + LNB + (j16 >> 2) * LNROW + (i * 256 + w4 * 64 + g * 16 + (j16 & 3) * 4) * 2;
        st16(__builtin_bit_cast(u32x4, cat(trd(p), trd(p + 4 * LNROW))), rws, tid * 16, WS_XNT + (row0 >> 3) * XNP + i * 4096);
      }
    };
    unsigned ad[3][4], vh[4], vpoll, kh, kh10;
    s16x8 wf[11][4];
    auto wd_addr = [&](int i) INL {
      const int q = j16 >> 2, f = ((q >> 1) & 1) + 2 * (g & 1);
#pragma unroll
      for (int u = 0; u < 4; ++u) {
        ad[i][u] = (i == 0 ? wl : i == 1 ? wl + 2 * SLOT : wl6) + (8 * g + q) * 128 + ((u ^ f) << 5) + (j16 & 3) * 8;
        asm volatile("" : "+v"(ad[i][u]));
      }
    };
    auto rdwd = [&](int s, int u) INL {
      const int pos = s % 7;
      LDS char* p = lds + (pos == 6 ? ad[2][u] : pos < 2 ? ad[1][u] + pos * 4096 : ad[0][u] + wdloc(pos));
      wf[s][u] = cat(trd(p), trd(p + 512));
    };
    auto h_addr = [&]() INL {
      wd_addr(0); wd_addr(1); wd_addr(2);
#pragma unroll
      for (int m = 0; m < 4; ++m) { const int mm = (m + w4) & 3; vh[m] = (mm >> 1) * (NU * 1024) + (g >> 1) * 1024 + (32 * (g & 1) + 16 * (mm & 1) + j16) * 16; }
      const int f0 = (w4 * 22) / 3, f1 = min(w4 * 22 + 21, 85) / 3;
      vpoll = WS_HF + (m2 * 32 + min(f0 + j16, f1)) * HFS;
      asm volatile("" : "+v"(vh[0]), "+v"(vh[1]), "+v"(vh[2]), "+v"(vh[3]), "+v"(vpoll));
      kh = WS_H + m2 * 2 * NU * 1024 + w4 * (11 * 2048);
      kh10 = w4 == 3 ? 0x7f000000u : kh + 10 * 2048;
      asm volatile("" : "+s"(kh), "+s"(kh10));
    };

    if (gemm) {
      addr_x();
      addr_w();
      asm volatile("" : "+v"(vx[0]), "+v"(vx[1]), "+v"(vw[0]), "+v"(vw[1]), "+v"(vw[2]));
      unsigned ax[4], aw[2];
      {
        const int id = (j16 >> 1) & 7, f = (id & 4) | ((id & 1) << 1) | ((id >> 1) & 1), cg = 2 * (g & 1) + (g >> 1);
#pragma unroll
        for (int s = 0; s < 2; ++s) { ax[s] = (mw * 64 + j16) * 128 + (((4 * s + cg) ^ f) << 4); ax[s + 2] = ax[s] + 2 * SLOT; }
        aw[0] = GOFF + (g & 1) * WBLK + (8 * (g >> 1) + (j16 >> 2)) * 192 + nw * 96 + (j16 & 3) * 8;
        aw[1] = aw[0] + 2 * SLOT;
      }
      unsigned a_st = STO + (mw * 64 + j16) * 8, a_cs = CSO + (nw * 48 + g * 4) * 4, so_h[3];
#pragma unroll
      for (int u = 0; u < 3; ++u) {
        const int unit = (n0 >> 4) + nw * 3 + u;
        so_h[u] = nt < 14 || unit >= 84 ? WS_H + ((mt * 4 + mw * 2) * NU + unit) * 1024 : 0x7f000000u;
      }
      unsigned vflag = (lane == 0 && (nt < 14 || nw == 1)) ? WS_HF + ((mt * 2 + mw) * 32 + (nt < 14 ? nt * 2 + nw : 28)) * HFS : 0x7f000000u;
      asm volatile("" : "+v"(a_st), "+v"(a_cs), "+s"(so_h[0]), "+s"(so_h[1]), "+s"(so_h[2]), "+v"(vflag));
      asm volatile("" : "+v"(ax[0]), "+v"(ax[1]), "+v"(ax[2]), "+v"(ax[3]), "+v"(aw[0]), "+v"(aw[1]));
      h_addr();
      SB();
      STAMP(26);
#pragma unroll
      for (int i = 0; i < 10; ++i) issue(0, i);
      STAMP(20);
      waitv<11>();
      layernorm([&](int k) INL { issue(1, k); });
#pragma unroll
      for (int i = 10 - D2F; i < 10; ++i) issue(2, i);
      STAMP(23);
      f32x4 acc[4][6] = {};
      G1Frag fr[2];
      auto half = [&](G1Frag& c, bool domm, G1Frag& n, bool dord, int s, int jj) INL {
#pragma unroll
        for (int t = 0; t < 24; ++t) {
          if (domm) acc[t & 3][t >> 2] = mfma16(c.w[t >> 2], c.x[t & 3], acc[t & 3][t >> 2]);
          SB();
          if (dord && (t & 1) == 0) {
            const int k = t >> 1, hi = s % 3 == 2, so = s % 3 == 1 ? SLOT : 0;
            if (k < 2) {
              n.x[2 * k] = *(const LDS s16x8*)(lds + so + ax[jj + 2 * hi] + 2 * k * 2048);
              n.x[2 * k + 1] = *(const LDS s16x8*)(lds + so + ax[jj + 2 * hi] + (2 * k + 1) * 2048);
            } else if (k < 8) {
              const int u = k - 2;
              const unsigned o = so + jj * 2 * WBLK + (u / 3) * 4 * WBLK + (u % 3) * 32;
              n.w[u] = cat(trd(lds + aw[hi] + o), trd(lds + aw[hi] + o + 768));
            }
          }
          SB();
        }
      };
#pragma unroll
      for (int s = 0; s < 8; ++s) {
        if (s == 2) STAMP(27);
        if (s == 0) waitv<13 + D2F>(); else if (s == 1) waitv<D2F>(); else if (s == 2) waitv<0>();
        if (s == 3) STAMP(29);
        if (s == 0) STAMP(19);
        bar();
        STAMP(2 + s);
        half(fr[1], s > 0, fr[0], true, s, 0);
        half(fr[0], true, fr[1], true, s, 1);
      }
      bar();
      STAMP(10);
      f32x2 st[4]; f32x4 cs[6];
      float kg[4];
#pragma unroll
      for (int t = 0; t < 24; ++t) {
        acc[t & 3][t >> 2] = mfma16(fr[1].w[t >> 2], fr[1].x[t & 3], acc[t & 3][t >> 2]);
        SB();
        if (t < 4) st[t] = *(const LDS f32x2*)(lds + a_st + t * 128);
        else if (t < 10) cs[t - 4] = *(const LDS f32x4*)(lds + a_cs + ((t - 4) / 3) * 384 + ((t - 4) % 3) * 64);
        else if (t >= 16 && t < 20) { kg[t - 16] = -1.44269504f * __builtin_amdgcn_rcpf(st[t - 16][1]); asm volatile("" : "+v"(kg[t - 16])); }
        SB();
      }
      PRIO(1);
      STAMP(11);
      unsigned seen;
#pragma unroll
      for (int mp = 0; mp < 2; ++mp) {
#pragma unroll
        for (int u = 0; u < 3; ++u) {
          unsigned o[2][2];
          const int q = mp * 3 + u;
          if (q == RD0 - 1 || q == RD1 - 1) { seen = peek(LANDED); SB(); }
          if (q == RD0 || q == RD1) {
            await(LANDED, seen, q == RD0 ? 1 : 2);
#pragma unroll
            for (int k = 0; k < 8; ++k) rdwd((q == RD0 ? 0 : 2) + (k >> 2), k & 3);
            post(USED, q == RD0 ? 1 : 2);
          }
#pragma unroll
          for (int mi = 0; mi < 2; ++mi) {
            const int m = mp * 2 + mi;
            const float nm = st[m][0], sd = st[m][1];
#pragma unroll
            for (int e = 0; e < 2; ++e) {
              f32x2 hv;
#pragma unroll
              for (int k = 0; k < 2; ++k) {
                const float gg = one(one(__builtin_fmaf(nm, cs[u][2 * e + k], acc[m][u][2 * e + k])) * kg[m]);
                const float uu = one(__builtin_fmaf(nm, cs[u + 3][2 * e + k], acc[m][u + 3][2 * e + k]));
                hv[k] = one(uu * __builtin_amdgcn_rcpf(one(__builtin_fmaf(__builtin_amdgcn_exp2f(gg), sd, sd))));
              }
              o[mi][e] = pk2(hv[0], hv[1]);
            }
          }
          const auto r0 = __builtin_amdgcn_permlane16_swap(o[0][0], o[1][0], false, false);
          const auto r1 = __builtin_amdgcn_permlane16_swap(o[0][1], o[1][1], false, false);
          const u32x4 ov = {r0[0], r1[0], r0[1], r1[1]};
          st16(ov, rws, lane * 16 + mp * (NU * 1024), so_h[u]);
          SB();
        }
      }
      STAMP(12);
      waitv<0>();
      __builtin_amdgcn_raw_buffer_store_b64(u32x2{(unsigned)tag, (unsigned)(tag >> 32)}, rws, vflag, 0, SC1);
    } else {
      const int cj = (xcd & 3) * 2 + (r - 30), kg = (tid * 745) >> 14, cc = tid - kg * 22, col = cj * 176 + cc * 8;
      const unsigned vo = (kg < 11 && col < F) ? kg * (F * 2) + col * 2 : 0x7f000000u;
      u32x4 cv[2][8];
      auto ldcs = [&](int sl) INL {
#pragma unroll
        for (int i = 0; i < 8; ++i) cv[sl & 1][i] = __builtin_amdgcn_raw_buffer_load_b128(rw, vo + (sl * 8 + i) * (11 * F * 2), 0, 0);
      };
      ldcs(0);
      waitv<9>();
      layernorm([](int) INL {});
      f32x2 a[4] = {};
#pragma unroll
      for (int sl = 0; sl < 6; ++sl) {
        if (sl == 1) waitv<0>();
        if (sl < 5) ldcs(sl + 1);
        if (sl < 4) bar();
#pragma unroll
        for (int i = 0; i < 8; ++i)
#pragma unroll
          for (int e = 0; e < 4; ++e) { const unsigned q = cv[sl & 1][i][e]; a[e] += f32x2{blo(q), bhi(q)}; }
        asm volatile("" : "+v"(a[0]), "+v"(a[1]), "+v"(a[2]), "+v"(a[3]));
      }
      if (kg < 11) {
        *(LDS f32x4*)(lds + kg * 704 + cc * 32) = f32x4{a[0][0], a[0][1], a[1][0], a[1][1]};
        *(LDS f32x4*)(lds + kg * 704 + cc * 32 + 16) = f32x4{a[2][0], a[2][1], a[3][0], a[3][1]};
      }
      bar();
      if (tid < 176) {
        float s = 0.f;
#pragma unroll
        for (int k = 0; k < 11; ++k) s += *(const LDS float*)(lds + k * 704 + tid * 4);
        __builtin_amdgcn_raw_buffer_store_b64(u32x2{f2u(s), (unsigned)tag}, rws, tid * 8, WS_CS + ((xcd >> 2) * CSP + cj * 176) * 8, SC1);
      }
      h_addr();
      bar(); bar(); bar(); bar();
      await(LANDED, 0, 1);
#pragma unroll
      for (int k = 0; k < 8; ++k) rdwd(k >> 2, k & 3);
      post(USED, 1);
      await(LANDED, 0, 2);
#pragma unroll
      for (int k = 8; k < 16; ++k) rdwd(k >> 2, k & 3);
      post(USED, 2);
    }

    STAMP(13);
    poll<HNAP>(ws, vpoll, tag);
    STAMP(14);
    auto fs = [](int n) INL { return n < 32 ? n >> 2 : 8 + (n - 32) % 3; };
    auto fm = [](int n) INL { return ((n < 32 ? n : (n - 32) / 3) + 1) & 3; };
    s16x8 hf[44];
    auto ldh = [&](int n) INL {
      hf[n] = __builtin_bit_cast(s16x8, __builtin_amdgcn_raw_buffer_load_b128(rws, vh[fm(n)] + (fs(n) & 1) * 2048, fs(n) == 10 ? kh10 : kh + (fs(n) >> 1) * 4096, SC1));
    };
    f32x4 a2[4][4] = {};
#pragma unroll
    for (int n = 0; n < 4 * HD; ++n) { ldh(n); SB(); }
    unsigned landed;
    SB();
#pragma unroll
    for (int n = 0; n < 44; ++n) {
      if (n == 2 || n == 22) landed = peek(LANDED);
      if (n == 4 || n == 24) await(LANDED, landed, n == 4 ? 3 : 4);
#pragma unroll
      for (int u = 0; u < 4; ++u) {
        a2[fm(n)][u] = mfma16(wf[fs(n)][u], hf[n], a2[fm(n)][u]);
        SB();
        if (u == 1 && n >= 4 && n < 32) rdwd((n >> 2) + 3, n & 3);
        if (u == 3 && n + 4 * HD < 44) ldh(n + 4 * HD);
        if (n >= 35 && (n - 35) % 3 == 1) *(LDS f32x4*)(lds + wdloc((n - 35) / 3) + wl + u * 1024 + lane * 16) = a2[(n - 35) / 3 + 1][u];
        SB();
      }
      if (n == 3) STAMP(15);
    }
    STAMP(16);
    bar();
    STAMP(17);
    unsigned o[4][2];
#pragma unroll
    for (int u = 0; u < 4; ++u) {
      f32x4 s = a2[0][u];
#pragma unroll
      for (int m = 1; m < 4; ++m) s += *(const LDS f32x4*)(lds + wdloc(m - 1) + ((w4 - m) & 3) * 8192 + u * 1024 + lane * 16);
      s += *(const LDS f32x4*)(lds + SBASE + (u >> 1) * PAIR + (u & 1) * 4096 + w4 * 1024 + lane * 16);
      o[u][0] = pk2(s[0], s[1]); o[u][1] = pk2(s[2], s[3]);
    }
#pragma unroll
    for (int up = 0; up < 2; ++up) {
      const auto r0 = __builtin_amdgcn_permlane16_swap(o[2 * up][0], o[2 * up + 1][0], false, false);
      const auto r1 = __builtin_amdgcn_permlane16_swap(o[2 * up][1], o[2 * up + 1][1], false, false);
      const u32x4 ov = {r0[0], r1[0], r0[1], r1[1]};
      st16(ov, mkr(out, L * D * 2), j16 * 1024 + (g & 1) * 32 + (g >> 1) * 16 + up * 64, (m2 * 64 + w4 * 16) * 1024 + n2 * 128);
    }
    STAMP(18);
    STAMP_OUT(0);
    asm volatile(".Lk_code_end0:");
  } else {
    const int cl = nw, hh = mw;
    addr_x();
    addr_w();
    asm volatile("" : "+v"(vx[0]), "+v"(vx[1]), "+v"(vw[0]), "+v"(vw[1]), "+v"(vw[2]));
    auto part = [&](int s, int i0, int i1) INL {
      if (__builtin_expect(gemm, 1)) {
#pragma unroll
        for (int i = i0; i < i1; ++i) issue(s, i);
      }
    };
    auto stage = [&](int s) INL { part(s, 0, 10); };
    unsigned vd[4], vcs, pa[4], vxi, vzero, vimg, vxf, vtw[2], vtab[2];
#pragma unroll
    for (int i = 0; i < 4; ++i) { const int row = lane >> 3, f = ((row >> 1) & 1) + 2 * (i & 1); vd[i] = (w4 * 352 + i * 8 + row) * 1024 + n2 * 128 + (((lane & 7) ^ (2 * f)) << 4); }
    vcs = tid < 96 ? (n0 + tid) * 8 : (CSP + n0 + min(tid, 191) - 96) * 8;
#pragma unroll
    for (int i = 0; i < 4; ++i) pa[i] = SBASE + cl * PAIR + (j16 & 1 ? TX : TY) + (847 - 256 * i - hh * 64 - ((15 + j16 - 8 * g) >> 1)) * 4;
    vxi = (tid >> 1) * XNP + (tid & 1) * 16 + cp * 32;
    vimg = SBASE + (tid & 1) * PAIR + XIMG + (tid >> 1) * 16;
    vzero = lane * 16 + (hh ? SYNC : SBASE + cl * PAIR + (lane < 2 ? TY + 4096 : TX + 4064));
    vtab[0] = SBASE + cl * PAIR + (255 - (hh ? (lane >> 5 ? 64 : 128) + (lane & 31) : lane)) * 16;
    vtab[1] = vtab[0] - (hh ? 32 : 192) * 16;
    vxf = tid ? 0x7f000000u : WS_XNF + b * 8;
    vtw[0] = lane < 11 ? (b * 43 + w4 * 11 + lane) * 128 : 0x7f000000u;
    vtw[1] = lane < 11 ? (r * 43 + w4 * 11 + lane) * 1024 + ((384u - (unsigned)(u64)wd) & 896u) : 0x7f000000u;
    asm volatile("" : "+v"(vd[0]), "+v"(vd[1]), "+v"(vd[2]), "+v"(vd[3]), "+v"(vcs), "+v"(pa[0]), "+v"(pa[1]), "+v"(pa[2]), "+v"(pa[3]));
    asm volatile("" : "+v"(vxi), "+v"(vimg), "+v"(vzero), "+v"(vxf), "+v"(vtw[0]), "+v"(vtw[1]), "+v"(vtab[0]), "+v"(vtab[1]));
    auto issue_wd = [&](int s, int p) INL {
      const int pos = s % 7;
      dma16(pos == 6 ? wl6 : wl, (pos == 6 ? 0 : wdloc(pos)) + p * 1024, vd[p], rwd, s * 32768);
    };
    SPRIO(1);
    waitv<1>();
    get_tag();
    STAMP(1);
    u32x2 tg = {(unsigned)tag, (unsigned)(tag >> 32)};
    asm volatile("" : "+v"(tg));
    float prm[6];
#pragma unroll
    for (int i = 0; i < 6; ++i) asm volatile("v_lshlrev_b32 %0, 16, %1" : "=v"(prm[i]) : "v"(xr[i >> 2][i & 3]));
    u32x2 xt[4];
    auto ldxt = [&](int m) INL {
      asm volatile("buffer_load_dwordx2 %0, %1, %2, %3 offen" : "=v"(xt[m]) : "v"(j16 * 1024 + g * 8), "s"(rx), "s"((m2 * 64 + m * 16) * 1024 + n2 * 128 + w4 * 32) : "memory");
    };
    const float p_ldt = prm[0], p_lar = prm[1], p_aim = prm[2], p_cr = prm[3], p_ci = prm[4], skip = one(lane ? 0.f : prm[5]);
    const float dt = rb(exp_f(p_ldt));
    const float s2 = -rb(exp_f(p_lar)) * dt * 1.44269504f, th = p_aim * dt;
    const unsigned eb = min(max(wave_max(max(f2u(p_cr) << 1, f2u(p_ci) << 1)) >> 24, 1u), 253u);
    const float sc = u2f((254u - eb) << 23), ksc = 2.f * dt * u2f(eb << 23);
    f32x2 z1 = cpow2(s2, th, 1.f), z8c = cpow2(s2, -th, 8.f), zb = cpow2(s2, th, 256.f);
    asm volatile("" : "+v"(z1), "+v"(z8c), "+v"(zb));
    const cmulr z256 = mul_by(zb);
    STAMP(2);
    LDS char* pr = lds + SBASE + cl * PAIR;
    LDS char* ks = pr + hh * KSW;
    auto put = [&](int row, f32x2 v) INL { *(LDS unsigned*)(ks + row * KROW + lane * 4) = pkh(v[0], v[1]); };
    auto putu = [&](int reg, int h, f32x2 v) INL { put((reg & 3) + 4 * h + 8 * (reg >> 2), v); };
    f16x8 vf[8];
    {
      f32x2 v[4];
      const cmulr m1 = mul_by(z8c);
      v[0] = f32x2{1.f, 0.f}; v[1] = z8c; v[2] = cmul(z8c, m1); v[3] = cmul(v[2], m1);
      const cmulr m4 = mul_by(cmul(v[3], m1));
#pragma unroll
      for (int k = 0; k < 32; ++k) {
        if (k == 4) v[0] = m4.z; else if (k > 4) v[k & 3] = cmul(v[k & 3], m4);
        put(k, v[k & 3]);
      }
      fence();
#pragma unroll
      for (int s = 0; s < 8; ++s) vf[s] = *(const LDS f16x8*)(ks + (lane & 31) * KROW + s * 32 + (lane >> 5) * 16);
      fence();
    }
    STAMP(3);
    {
      f32x2 e[5];
      const cmulr m1 = mul_by(z1), m2_ = mul_by(cmul(z1, m1));
      e[0] = f32x2{p_cr, p_ci} * dup(sc);
      putu(0, 0, e[0]);
#pragma unroll
      for (int k = 0; k < 4; ++k) { putu(2 * k + 1, 0, cmul(e[k], m1)); e[k + 1] = cmul(e[k], m2_); putu(2 * k + 2, 0, e[k + 1]); if (XTP == 0) ldxt(k); }
#pragma unroll
      for (int k = 0; k < 5; ++k) e[k] = cmul(e[k], z256);
      putu(0, 1, e[0]); putu(2, 1, e[1]); putu(3, 1, e[2]); putu(4, 1, e[2]); putu(5, 1, e[2]); putu(6, 1, e[3]); putu(7, 1, e[4]); putu(8, 1, e[4]);
#pragma unroll
      for (int k = 0; k < 6; ++k) {
        const int reg = k < 2 ? 9 + 2 * k : k < 5 ? 5 + 2 * k : 13, h = k < 2 || k == 5;
        e[0] = cmul(e[0], z256); putu(reg, h, e[0]);
        e[4] = cmul(e[4], z256); putu(reg + 1, h, e[4]);
        if (k < 2) { e[2] = cmul(e[2], z256); putu(k ? 1 : 15, 1, e[2]); }
        if (XTP == 1 && k < 4) ldxt(k);
        if (TWP == 0 && k == 5) touch(DUMMY, vtw[0], rwd, 0);
      }
      fence();
    }
    STAMP(4);
    bar();
    f32x16 kd = {};
    {
      f16x8 uf[8];
#pragma unroll
      for (int s = 0; s < 8; ++s) uf[s] = *(const LDS f16x8*)(ks + (lane & 31) * KROW + s * 32 + (lane >> 5) * 16);
      SB();
#pragma unroll
      for (int s = 0; s < 8; ++s) kd = __builtin_amdgcn_mfma_f32_32x32x16_f16(uf[s], vf[s], kd, 0, 0, 0);
    }
    kd *= ksc;
    const bool h = lane >> 5, odd = lane & 1;
    if (hh) {
      *(LDS f32x4*)(pr + KEX1 + lane * 16) = f32x4{kd[0], kd[1], kd[2], kd[3]};
      *(LDS f32x4*)(pr + KEX1 + 1024 + lane * 16) = f32x4{kd[4], kd[5], kd[6], kd[7]};
      *(LDS f32x4*)(pr + KEX1 + 2048 + lane * 16) = f32x4{kd[8], kd[13], kd[14], kd[15]};
    } else {
      *(LDS f32x4*)(pr + KEX0 + lane * 16) = f32x4{kd[9], kd[10], kd[11], kd[12]};
      *(LDS f32x2*)(pr + KEX0 + 1024 + lane * 8) = f32x2{kd[15], kd[1]};
    }
    STAMP(5);
    if (PEND == 0) SPRIO(0);
    bar();
    STAMP(10);
    {
      u32x4 tb[5];
      auto tail = [&](float a, float c, float a3, float a4, float a5, int k) INL {
        const unsigned aa = pk2(a, a), cc = pk2(c, c);
        tb[k] = u32x4{cc, pk2(a5, a4), pk2(a3, a), aa};
        tb[k + 1] = u32x4{cc, pk2(c, a5), pk2(a4, a3), aa};
      };
      if (hh) {
        const f32x4 p = *(const LDS f32x4*)(pr + KEX0 + lane * 16);
        const f32x2 q = *(const LDS f32x2*)(pr + KEX0 + 1024 + lane * 8);
#pragma unroll
        for (int k = 0; k < 2; ++k) {
          const float a = one(kd[9 + 2 * k] + p[2 * k]), c = one(kd[10 + 2 * k] + p[2 * k + 1]), m = one(kd[k ? 1 : 15] + q[k]);
          tail(a, c, h ? m : a, h ? m : odd ? c : a, h ? m : c, 2 * k);
        }
        tb[4][0] = 0;
      } else {
        const f32x4 p0 = *(const LDS f32x4*)(pr + KEX1 + lane * 16), p1 = *(const LDS f32x4*)(pr + KEX1 + 1024 + lane * 16);
        const f32x4 p2 = *(const LDS f32x4*)(pr + KEX1 + 2048 + lane * 16);
        float t[9];
#pragma unroll
        for (int k = 0; k < 9; ++k) t[k] = one(kd[k] + (k < 4 ? p0[k] : k < 8 ? p1[k - 4] : p2[0]));
        t[0] += skip;
        t[1] = h ? t[0] : t[1];
        tb[0] = u32x4{pk2(t[7], t[6]), pk2(t[5], t[4]), pk2(t[3], t[2]), pk2(t[1], t[0])};
        tb[1] = u32x4{pk2(t[8], t[7]), pk2(t[6], t[5]), pk2(t[4], t[3]), pk2(t[2], t[1])};
        const float a = one(kd[13] + p2[1]), c = one(kd[14] + p2[2]);
        tail(a, c, a, odd ? c : a, c, 2);
        tb[4][0] = lane ? 0 : tb[0][3] >> 16;
      }
      tb[4][1] = tb[4][2] = tb[4][3] = 0;
      asm volatile("" : "+v"(tb[0]), "+v"(tb[1]), "+v"(tb[2]), "+v"(tb[3]), "+v"(tb[4]));
      STAMP(19);
#pragma unroll
      for (int k = 0; k < 4; ++k) *(LDS u32x4*)(lds + vtab[k >> 1] + (k & 1 ? TY : TX)) = tb[k];
      if (lane < (hh ? 3 : 36)) *(LDS u32x4*)(lds + vzero) = tb[4];
      part(2, 0, 10 - D2F);
    }
    if (XTP == 2) { ldxt(0); ldxt(1); ldxt(2); ldxt(3); }
    if (TWP == 2) touch(DUMMY, vtw[0], rwd, 0);
    STAMP(6);
    bar();
    __builtin_amdgcn_raw_buffer_store_b64(tg, rws, vxf, 0, SC1);
    stage(3);
    if (TWP == 1) touch(DUMMY, vtw[0], rwd, 0);
    waitv<10 + (TWP == 1)>();
    bar();
    stage(4);
    waitv<10>();
    bar();
    stage(5);
    waitv<10>();
    bar();
    stage(6);
    if (TW3) touch(DUMMY, vtw[1], rwd, 0);
    waitv<10 + (TW3 != 0)>();
    bar();
    {
      u64 fl;
      asm volatile("buffer_load_dwordx2 %0, %1, %2, %3 offen sc1" : "=v"(fl) : "v"(tid * 8), "s"(rws), "s"(WS_XNF) : "memory");
      stage(7);
      if (gemm) waitv<10>(); else waitv<0>();
      asm volatile("" : "+v"(fl));
      if (__builtin_expect(!wave_all(fl == tag), 0)) poll<4>(ws, WS_XNF + tid * 8, tag);
    }
    STAMP(7);
    bar();
    unsigned gst; u32x2 csg; u32x4 xi[2];
    asm volatile("buffer_load_dword %0, %1, %2, %3 offen sc1" : "=v"(gst) : "v"((tid & 127) * 4), "s"(rws), "s"(WS_ST + (mt & 15) * 512) : "memory");
    asm volatile("buffer_load_dwordx2 %0, %1, %2, %3 offen sc1" : "=v"(csg) : "v"(vcs), "s"(rws), "s"(WS_CS) : "memory");
#pragma unroll
    for (int i = 0; i < 2; ++i)
      asm volatile("buffer_load_dwordx4 %0, %1, %2, %3 offen sc1" : "=v"(xi[i]) : "v"(vxi), "s"(rws), "s"(WS_XNT + i * (128 * XNP)) : "memory");
#pragma unroll
    for (int i = 0; i < 4; ++i) issue_wd(0, i);
    waitv<8>();
    STAMP(8);
    bar();
    if (NW7 == 3) {
#pragma unroll
      for (int i = 0; i < 4; ++i) issue_wd(1, i);
    }
    waitv<4 * NW7 - 4>();
    asm volatile("" : "+v"(gst), "+v"(csg), "+v"(xi[0]), "+v"(xi[1]));
    if (tid < 128) *(LDS f32x2*)(lds + STO + tid * 8) = f32x2{-blo(gst), bhi(gst)};
#pragma unroll
    for (int i = 0; i < 2; ++i) *(LDS u32x4*)(lds + vimg + XPAD + i * 2048) = xi[i];
    if (__builtin_expect(gemm && !wave_all(csg[1] == (unsigned)tag), 0)) {
#pragma unroll 1
      for (unsigned spins = 0; spins < SPIN_CAP && !wave_all(csg[1] == (unsigned)tag); ++spins) {
        __builtin_amdgcn_s_sleep(2);
        asm volatile("buffer_load_dwordx2 %0, %1, %2, %3 offen sc1\n\ts_waitcnt vmcnt(0)" : "=&v"(csg) : "v"(vcs), "s"(rws), "s"(WS_CS) : "memory");
      }
    }
    *(LDS unsigned*)(lds + CSO + tid * 4) = csg[0];
#pragma unroll
    for (int i = 0; i < 4; ++i) issue_wd(NW7 - 1, i);
    STAMP(9);
    bar();
    LDS char* pb = pr + XIMG + XPAD + (16 * j16 + 8 * g - 16) * 2 - hh * 256 - 192;
    CvFrag cf[2];
    auto rdc = [&](CvFrag& f, int rr, int k) INL {
      if (k & 1) f.b[k >> 1] = *(const LDS s16x8*)(pb + (k >> 1) * 512 + (3 - rr) * 64);
      else {
        const LDS s16x4u* q = (const LDS s16x4u*)(lds + pa[k >> 2] + (k & 2 ? 0 : 512) + (3 - rr) * 64);
        f.a[k >> 1] = cat(q[0], q[1]);
      }
    };
#pragma unroll
    for (int k = 0; k < 16; ++k) { rdc(cf[0], 0, k); if (NW7 == 3 ? (k & 3) == 3 : (k & 1)) issue_wd(NW7 == 3 ? 3 : 2 + (k >> 3), NW7 == 3 ? k >> 2 : (k >> 1) & 3); }
    STAMP(11);
    if (PEND == 1 || PEND == 3) SPRIO(0);
    unsigned seen;
    f32x4 ca[8] = {};
#pragma unroll
    for (int rr = 0; rr < 4; ++rr) {
      int n = 0;
#pragma unroll
      for (int u = 0; u < 8; ++u)
#pragma unroll
        for (int t = u; t < 8; ++t) {
          ca[t] = mfma16(cf[rr & 1].a[u], cf[rr & 1].b[t - u], ca[t]);
          SB();
          if (rr < 3 && (n & 1) == 0 && (n >> 1) < 16) rdc(cf[(rr + 1) & 1], rr + 1, n >> 1);
          const int q = rr * 36 + n;
          if (q == 1) { waitv<8>(); post(LANDED, 1); }
          if (q == 8 * PACE + 1) { waitv<8>(); post(LANDED, 2); }
          if (q == 12 * PACE - 3 || q == 20 * PACE - 3) seen = peek(USED);
          if (q == 12 * PACE + 1 || q == 20 * PACE + 1) await(USED, seen, q < 20 * PACE ? 1 : 2);
          if (q % PACE == 3 && q < 28 * PACE) issue_wd(4 + q / PACE / 4, (q / PACE) & 3);
          SB();
          ++n;
        }
    }
    STAMP(12);
    if (PEND == 3) SPRIO(1);
    {
      LDS char* exa = pr + CEX + lane * 16;
      LDS char* exb = lds + SLOT + LEFT + cl * 4096 + lane * 16;
      f32x4 keep[4];
#pragma unroll
      for (int k = 0; k < 4; ++k) {
        *(LDS f32x4*)((hh ? exa : exb) + k * 1024) = hh ? ca[k] : ca[k + 4];
        keep[k] = hh ? ca[k + 4] : ca[k];
      }
      fence();
      post(SYNC, 1);
      waitv<8>();
      post(LANDED, 3);
      if (b == 0 && w4 == 0) setflag(ws, WS_E, tag, lane);
      lds_wait(lds + SYNC + (w4 ^ 1) * 4, 1);
      unsigned o[4][2];
#pragma unroll
      for (int k = 0; k < 4; ++k) {
        const f32x4 s = keep[k] + *(const LDS f32x4*)((hh ? exb : exa) + k * 1024);
        o[k][0] = pk2(s[0], s[1]); o[k][1] = pk2(s[2], s[3]);
      }
#pragma unroll
      for (int kp = 0; kp < 2; ++kp) {
        const auto r0 = __builtin_amdgcn_permlane16_swap(o[2 * kp][0], o[2 * kp + 1][0], false, false);
        const auto r1 = __builtin_amdgcn_permlane16_swap(o[2 * kp][1], o[2 * kp + 1][1], false, false);
        const u32x4 ov = {r0[0], r1[0], r0[1], r1[1]};
        st16(ov, rws, ((g & 1) * 256 + 16 * j16 + 8 * (g >> 1)) * 2, WS_YT + ch * 4096 + (hh * 4 + kp * 2) * 512);
      }
      STAMP(13);
      waitv<2>();
      post(LANDED, 4);
      waitv<0>();
      setflag(ws, WS_YF + (hh * 512 + ch) * YFS, tag, lane);
    }
    STAMP(14);
    __builtin_amdgcn_s_sleep(YSLEEP);
    poll<YNAP>(ws, WS_YF + ((m2 >> 4) * 512 + n2 * 64 + w4 * 16 + j16) * YFS, tag);
    STAMP(15);
    {
      LDS char* yb = pr + hh * 4096;
#pragma unroll
      for (int i = 0; i < 2; ++i) {
        const u32x4 y = __builtin_amdgcn_raw_buffer_load_b128(rws, (lane >> 3) * 4096 + (lane & 7) * 16, WS_YT + (n2 * 64 + w4 * 16 + i * 8) * 4096 + m2 * 128, SC1);
        *(LDS u32x4*)(yb + (i * 8 + (lane >> 3)) * 160 + (lane & 7) * 16) = y;
      }
      fence();
      asm volatile("" : "+v"(xt[0]), "+v"(xt[1]), "+v"(xt[2]), "+v"(xt[3]));
      u32x2 y[4];
#pragma unroll
      for (int m = 0; m < 4; ++m) y[m] = __builtin_bit_cast(u32x2, trd(yb + (4 * g + (j16 >> 2)) * 160 + m * 32 + (j16 & 3) * 8));
      waitl0();
#pragma unroll
      for (int m = 0; m < 4; ++m) {
        const unsigned y0 = y[m][0], y1 = y[m][1], q0 = xt[m][0], q1 = xt[m][1];
        *(LDS f32x4*)(yb + m * 1024 + lane * 16) = f32x4{blo(y0) + blo(q0), bhi(y0) + bhi(q0), blo(y1) + blo(q1), bhi(y1) + bhi(q1)};
      }
    }
    STAMP(16);
    bar();
    STAMP_OUT(1);
    asm volatile(".Lk_code_end1:");
  }
}

}

extern "C" void kernel_launch(void* const* d_in, const int* in_sizes, int n_in, void* d_out, int out_size, void* d_ws, size_t ws_size,
                              hipStream_t stream) {
  fused_kernel<<<256, 512, 0, stream>>>((const u16*)d_in[0], (const u16*)d_in[1], (const u16*)d_in[2], (const u16*)d_in[3], (const u16*)d_in[4],
                                        (const u16*)d_in[5], (const u16*)d_in[6], (const u16*)d_in[8], (const u16*)d_in[9], (const u16*)d_in[10],
                                        (u16*)d_out, (char*)d_ws);
}
```

```cpp
#include <hip/hip_runtime.h>
#include <hip/hip_bf16.h>

namespace {

typedef unsigned short u16;
typedef unsigned long long u64;
typedef __attribute__((ext_vector_type(4))) short s16x4;
typedef s16x4 __attribute__((aligned(4))) s16x4u;
typedef __attribute__((ext_vector_type(8))) short s16x8;
typedef __attribute__((ext_vector_type(8))) __bf16 bf16x8;
typedef __attribute__((ext_vector_type(2))) __bf16 bf16x2;
typedef __attribute__((ext_vector_type(8))) _Float16 f16x8;
typedef __attribute__((ext_vector_type(2))) _Float16 f16x2;
typedef __attribute__((ext_vector_type(2))) float f32x2;
typedef __attribute__((ext_vector_type(4))) float f32x4;
typedef __attribute__((ext_vector_type(16))) float f32x16;
typedef __attribute__((ext_vector_type(4))) unsigned u32x4;
typedef __attribute__((ext_vector_type(2))) unsigned u32x2;
typedef __amdgpu_buffer_rsrc_t rsrc_t;
#define LDS __attribute__((address_space(3)))
#define GLB __attribute__((address_space(1)))
#define INL __attribute__((always_inline))
#define SB() __builtin_amdgcn_sched_barrier(0)
#define RLX_AGENT __ATOMIC_RELAXED, __HIP_MEMORY_SCOPE_AGENT
#define RLX_WG __ATOMIC_RELAXED, __HIP_MEMORY_SCOPE_WORKGROUP

constexpr int L = 2048, D = 512, NM = 128, F = 1376, NU = F / 16;
constexpr int SC1 = 16;
#define HF 10
#define PRIO(x)
#define TW3 11
#define SPRIO(x) __builtin_amdgcn_s_setprio(x)
#define HNAP 2
#define YNAP 3
#define RD0 1
#define PACE 4
#define TWP 0
#define D2F 6
#define HSLEEP 8
#define CPRIO 0
#define TPRIO 0
#define NPRE 16
#define Q0 3
#define RD1 3
#define B9Q 5
#define PKQ 6
#define SPR 1
#define SPC 1
#define YSLEEP 4
static_assert(Q0 + (27 - NPRE) * PACE < 108 && RD0 < RD1 && NPRE >= 12 && NPRE <= 20 && RD0 < B9Q, "schedule");
constexpr unsigned SPIN_CAP = 1u << 18;

constexpr int WBLK = 3104, GOFF = 16384, UOFF = GOFF + 4 * WBLK, SLOT = GOFF + 8 * WBLK;
constexpr int LEFT = 32768;
constexpr int LNB = 2 * SLOT, LNROW = 1088;
constexpr int SBASE = 3 * SLOT;
constexpr int KROW = 272, KSW = 32 * KROW, PAIR = 2 * KSW;
constexpr int TX = 0, XIMG = 4160, XPAD = 480, CEX = 8736, TY = 13248, KEX0 = 7168, KEX1 = KSW;
constexpr int STO = SBASE + 2 * PAIR, CSO = STO + 1024, SYNC = CSO + 1536, LANDED = SYNC + 16, USED = SYNC + 32, DONE = SYNC + 48, DUMMY = SYNC + 128;
constexpr int LDS_BYTES = 163840;
static_assert(DUMMY + 256 <= LDS_BYTES && XIMG + XPAD + 4096 <= CEX && CEX + 4096 <= TY && TY + 4128 <= PAIR, "LDS map");

constexpr int WS_E = 0, WS_XNF = 4096, WS_HF = 65536, WS_YF = 196608, WS_ST = 32768, WS_CS = 40960;
constexpr int XNP = 8320, WS_XNT = 1 << 19, WS_YT = 3 << 20, WS_H = 5 << 20, WS_END = WS_H + 64 * NU * 1024;
constexpr int CSP = 1408;

__device__ __forceinline__ float u2f(unsigned v) { return __builtin_bit_cast(float, v); }
__device__ __forceinline__ unsigned f2u(float v) { return __builtin_bit_cast(unsigned, v); }
__device__ __forceinline__ bool wave_all(bool p) { return __builtin_amdgcn_ballot_w64(!p) == 0; }
__device__ __forceinline__ unsigned rfl(unsigned v) { return __builtin_amdgcn_readfirstlane(v); }
__device__ __forceinline__ rsrc_t mkr(const void* p, unsigned n) { return __builtin_amdgcn_make_buffer_rsrc((void*)p, 0, n, 0x00020000); }
__device__ __forceinline__ void dma16(unsigned base, int dst, unsigned voff, rsrc_t r, unsigned soff, int off = 0) {
  asm volatile("s_add_u32 m0, %0, %1\n\ts_nop 0\n\tbuffer_load_dwordx4 %2, %3, %4 offen offset:%5 lds"
               ::"s"(base), "n"(dst - off), "v"(voff), "s"(r), "s"(soff), "n"(off) : "scc", "memory");
}
__device__ __forceinline__ void touch(unsigned m0v, unsigned voff, rsrc_t r, unsigned soff) {
  asm volatile("s_mov_b32 m0, %0\n\ts_nop 0\n\tbuffer_load_dword %1, %2, %3 offen lds" ::"s"(m0v), "v"(voff), "s"(r), "s"(soff) : "memory");
}
__device__ __forceinline__ void st16(u32x4 v, rsrc_t r, unsigned voff, unsigned soff) {
  asm volatile("buffer_store_dwordx4 %0, %1, %2, %3 offen sc1\n\ts_nop 1" ::"v"(v), "v"(voff), "s"(r), "s"(soff) : "memory");
}
template <int W> __device__ __forceinline__ void waitv() { asm volatile("s_waitcnt vmcnt(%0)" ::"n"(W) : "memory"); }
__device__ __forceinline__ void waitl0() { asm volatile("" ::: "memory"); __builtin_amdgcn_s_waitcnt(0xc07f); asm volatile("" ::: "memory"); }
__device__ __forceinline__ void fence() { asm volatile("" ::: "memory"); }
__device__ __forceinline__ void bar() { waitl0(); __builtin_amdgcn_s_barrier(); fence(); }
__device__ __forceinline__ unsigned pk2(float a, float b) { f32x2 p = {a, b}; return __builtin_bit_cast(unsigned, __builtin_convertvector(p, bf16x2)); }
__device__ __forceinline__ unsigned pkh(float a, float b) { f32x2 p = {a, b}; return __builtin_bit_cast(unsigned, __builtin_convertvector(p, f16x2)); }
__device__ __forceinline__ float blo(unsigned v) { return u2f(v << 16); }
__device__ __forceinline__ float bhi(unsigned v) { return u2f(v & 0xffff0000u); }
__device__ __forceinline__ float rb(float a) { return blo(pk2(a, a)); }
template <int C, int RM> __device__ __forceinline__ float dppf(float v) {
  return __builtin_bit_cast(float, __builtin_amdgcn_update_dpp(0, __builtin_bit_cast(int, v), C, RM, 0xf, true));
}
__device__ __forceinline__ float lane63(float v) { return __builtin_bit_cast(float, __builtin_amdgcn_readlane(__builtin_bit_cast(int, v), 63)); }
__device__ __forceinline__ float wave_sum(float v) {
  v += dppf<0x111, 0xf>(v); v += dppf<0x112, 0xf>(v); v += dppf<0x114, 0xf>(v); v += dppf<0x118, 0xf>(v);
  v += dppf<0x142, 0xa>(v); v += dppf<0x143, 0xc>(v);
  return lane63(v);
}
template <int C, int RM> __device__ __forceinline__ unsigned dppmax(unsigned v) {
  const unsigned o = __builtin_amdgcn_update_dpp(0, v, C, RM, 0xf, true);
  return v > o ? v : o;
}
__device__ __forceinline__ unsigned wave_max(unsigned v) {
  v = dppmax<0x111, 0xf>(v); v = dppmax<0x112, 0xf>(v); v = dppmax<0x114, 0xf>(v); v = dppmax<0x118, 0xf>(v);
  v = dppmax<0x142, 0xa>(v); v = dppmax<0x143, 0xc>(v);
  return __builtin_amdgcn_readlane(v, 63);
}
__device__ __forceinline__ s16x8 cat(s16x4 a, s16x4 b) { return __builtin_shufflevector(a, b, 0, 1, 2, 3, 4, 5, 6, 7); }
__device__ __forceinline__ s16x4 trd(LDS char* p) { return __builtin_amdgcn_ds_read_tr16_b64_v4i16((LDS s16x4*)p); }
__device__ __forceinline__ f32x4 mfma16(s16x8 a, s16x8 b, f32x4 c) {
  return __builtin_amdgcn_mfma_f32_16x16x32_bf16(__builtin_bit_cast(bf16x8, a), __builtin_bit_cast(bf16x8, b), c, 0, 0, 0);
}
template <int NAP> __device__ __forceinline__ void poll(const char* ws, unsigned off, u64 tag) {
  GLB u64* p = (GLB u64*)(ws + off);
#pragma unroll 1
  for (unsigned spins = 0; spins < SPIN_CAP; ++spins) {
    if (wave_all(__hip_atomic_load(p, RLX_AGENT) == tag)) break;
    __builtin_amdgcn_s_sleep(NAP);
  }
  fence();
}
typedef __attribute__((ext_vector_type(8))) unsigned long long u64x8;
template <int NAP, int N> __device__ __forceinline__ bool watch(const char* p, u64 tag) {
#pragma unroll 1
  for (unsigned spins = 0; spins < 128; ++spins) {
    u64x8 f[2];
    u64 d = 0;
    if constexpr (N == 1) asm volatile("s_load_dwordx16 %0, %1, 0 glc\n\ts_waitcnt lgkmcnt(0)" : "=&s"(f[0]) : "s"(p) : "memory");
    else asm volatile("s_load_dwordx16 %0, %2, 0 glc\n\ts_load_dwordx16 %1, %2, 64 glc\n\ts_waitcnt lgkmcnt(0)" : "=&s"(f[0]), "=&s"(f[1]) : "s"(p) : "memory");
#pragma unroll
    for (int i = 0; i < 8 * N; ++i) { d |= f[i >> 3][i & 7] ^ tag; asm("" : "+s"(d)); }
    if (d == 0) return true;
    __builtin_amdgcn_s_sleep(NAP);
  }
  return false;
}
__device__ __forceinline__ void lds_wait(LDS char* p, unsigned v) {
#pragma unroll 1
  for (unsigned spins = 0; spins < SPIN_CAP; ++spins) { if (rfl(*(volatile LDS unsigned*)p) >= v) break; __builtin_amdgcn_s_sleep(1); }
  fence();
}
__device__ __forceinline__ void setflag(char* ws, unsigned off, u64 tag, int lane) {
  if (lane == 0) __hip_atomic_store((GLB u64*)(ws + off), tag, RLX_AGENT);
}
__device__ __forceinline__ f32x2 fma2(f32x2 a, f32x2 b, f32x2 c) { return __builtin_elementwise_fma(a, b, c); }
__device__ __forceinline__ float one(float v) { asm("" : "+v"(v)); return v; }
__device__ __forceinline__ f32x2 dup(float a) { return f32x2{a, a}; }
struct cmulr { f32x2 z, rot; };
__device__ __forceinline__ cmulr mul_by(f32x2 z) { return {z, f32x2{-z[1], z[0]}}; }
__device__ __forceinline__ f32x2 cmul(f32x2 a, cmulr m) { return fma2(dup(a[0]), m.z, dup(a[1]) * m.rot); }
__device__ __forceinline__ f32x2 cpow2(float s2, float th, float p) {
  const float t = th * p, n = __builtin_rintf(t * 0.15915494f);
  const float fr = __builtin_fmaf(t, 0.15915494f, -n) + t * 6.4206382e-9f;
  const float e = __builtin_amdgcn_exp2f(s2 * p);
  return f32x2{e * __builtin_amdgcn_cosf(fr), e * __builtin_amdgcn_sinf(fr)};
}
__device__ __forceinline__ float exp_f(float v) {
  const float t = v * 1.44269504f, e = __builtin_fmaf(v, 1.925963033e-8f, __builtin_fmaf(v, 1.44269504f, -t));
  const float y = __builtin_amdgcn_exp2f(t);
  return __builtin_fmaf(y, e * 0.69314718f, y);
}

#define STAMP(i)
#define STAMP_OUT(role)

struct G1Frag { s16x8 x[4]; s16x4 w[12]; };
struct CvFrag { s16x4 a[16]; s16x8 b[8]; };

__global__ __launch_bounds__(512, 2) void fused_kernel(const u16* x, const u16* lar, const u16* aim, const u16* cre, const u16* cim, const u16* ldt,
                                                       const u16* dsk, const u16* wg, const u16* wu, const u16* wd, u16* out, char* ws) {
  __shared__ __attribute__((aligned(16))) char lds_raw[LDS_BYTES];
  LDS char* lds = (LDS char*)lds_raw;
  int w = rfl(threadIdx.x >> 6);
  const int lane = threadIdx.x & 63, tid = threadIdx.x & 255;
  const int b = blockIdx.x, xcd = b & 7, r = b >> 3;
  STAMP(0);
  if (w == 7) {
    u64 pc; unsigned l0, l1;
    asm volatile("s_getpc_b64 %0\n.Lk_code_begin:\n\ts_mov_b32 %1, .Lk_code_end0-.Lk_code_begin\n\ts_mov_b32 %2, .Lk_code_end1-.Lk_code_begin" : "=s"(pc), "=s"(l0), "=s"(l1));
    touch(DUMMY, lane * 1024, mkr((const void*)pc, max(l0, l1) - 896), (r & 7) * 128);
  }
  const int row0 = (xcd & 1) * 1024 + (xcd >> 1) * 256 + r * 8;
  const unsigned vt = lane < 16 ? (lane >> 3) * (64 * F * 2) + (lane & 7) * 96 : 0x7f000000u;
  const unsigned so_t = ((w & 3) * 128 + (xcd & 1) * 32 + r) * (F * 2) + (xcd >> 1) * 688;
  asm volatile("" ::"s"(row0), "v"(vt), "s"(so_t));
  STAMP(28);
  asm volatile("" : "+s"(x), "+s"(lar), "+s"(aim), "+s"(cre), "+s"(cim), "+s"(ldt), "+s"(dsk), "+s"(wg), "+s"(wu), "+s"(wd), "+s"(out), "+s"(ws));
  STAMP(24);
  const rsrc_t rx = mkr(x, L * D * 2);
  const int w4 = w & 3, mw = w & 1, nw = w4 >> 1;
  const int cp = xcd * 32 + r, ch = cp * 2 + nw;
  u32x4 xr[2];
  u64 tag;
  if (w < 4) {
#pragma unroll
    for (int i = 0; i < 2; ++i)
      asm volatile("buffer_load_dwordx4 %0, %1, %2, %3 offen" : "=v"(xr[i]) : "v"(lane * 16), "s"(rx), "s"((row0 + 2 * w4 + i) * 1024) : "memory");
  } else {
    const unsigned pi = (ch * NM + mw * 64 + lane) * 2;
    asm volatile("global_load_ushort %0, %6, %8\n\tglobal_load_ushort %1, %7, %9\n\tglobal_load_ushort %2, %7, %10\n\tglobal_load_ushort %3, %7, %11\n\t"
                 "global_load_ushort %4, %7, %12\n\tglobal_load_ushort %5, %6, %13"
                 : "=&v"(xr[0][0]), "=&v"(xr[0][1]), "=&v"(xr[0][2]), "=&v"(xr[0][3]), "=&v"(xr[1][0]), "=&v"(xr[1][1])
                 : "v"(ch * 2), "v"(pi), "s"(ldt), "s"(lar), "s"(aim), "s"(cre), "s"(cim), "s"(dsk) : "memory");
  }
  u64 epoch;
  asm volatile("s_load_dwordx2 %0, %1, 0x0" : "=s"(epoch) : "s"(ws) : "memory");
  __builtin_amdgcn_s_barrier();
  STAMP(25);
  touch(DUMMY, vt, mkr(w >> 2 ? wu : wg, D * F * 2), so_t);
  auto get_tag = [&]() INL {
    unsigned lo, hi;
    asm volatile("s_waitcnt lgkmcnt(0)\n\ts_add_u32 %0, %2, 0x7F4A7C15\n\ts_addc_u32 %1, %3, 0x9E3779B9"
                 : "=&s"(lo), "=s"(hi) : "s"((unsigned)epoch), "s"((unsigned)(epoch >> 32)) : "scc", "memory");
    tag = (u64)hi << 32 | lo;
  };
  asm volatile("" : "+s"(w));
  SB();

  const rsrc_t rws = mkr(ws, WS_END), rwd = mkr(wd, F * D * 2);
  const bool gemm = r < 30;
  const int idx = (xcd >> 1) * 30 + r, nt = idx >> 3, mt = (xcd & 1) * 8 + (idx & 7);
  const int n0 = nt < 14 ? nt * 96 : 1280;
  const int m2 = (xcd & 1) * 16 + (xcd >> 1) * 4 + (r >> 3), n2 = r & 7;
  const int j16 = lane & 15, g = lane >> 4;

  const rsrc_t rw = mkr((gemm ? nw : xcd >> 2) ? wu : wg, D * F * 2);
  unsigned vx[2], vw[3], lx = w4 * 4096, lw = (nw ? UOFF : GOFF) + mw * 2 * WBLK;
  asm volatile("" : "+s"(lx), "+s"(lw));
  auto addr_x = [&]() INL {
#pragma unroll
    for (int i = 0; i < 2; ++i) {
      const int row = i * 8 + (lane >> 3), id = (row >> 1) & 7, f = (id & 4) | ((id & 1) << 1) | ((id >> 1) & 1);
      vx[i] = (mt * 128 + w4 * 32 + row) * 1024 + (((lane & 7) ^ f) << 4);
    }
  };
  auto addr_w = [&]() INL {
    const int kr = (lane * 171) >> 11, c = lane - kr * 12;
    vw[0] = (mw * 32 + kr) * (F * 2) + c * 16 + n0 * 2;
    vw[1] = vw[0] + (c >= 8 ? 6 * F * 2 - 128 : 5 * F * 2 + 64);
    vw[2] = vw[0] + (c >= 4 ? 11 * F * 2 - 64 : 10 * F * 2 + 128);
  };
  auto issue = [&](int s, int i) INL {
    const int slot = (s % 3) * SLOT, j = i - 4;
    if (i < 4) dma16(lx, slot + i * 1024, vx[i & 1], rx, s * 128 + (i >> 1) * 16384);
    else dma16(lw, slot + (j / 3) * WBLK + (j % 3) * 1024, vw[j % 3], rw, s * (64 * F * 2) + (j / 3) * 16 * F * 2);
  };
  const unsigned wl = w4 * 8192, wl6 = (nw ? 0 : 2 * SLOT) + LEFT + mw * 4096;
  auto wdloc = [](int pos) INL { return ((pos / 2 + 2) % 3) * SLOT + (pos & 1) * 4096; };

  auto post = [&](int word, unsigned v) INL { fence(); *(volatile LDS unsigned*)(lds + word + w4 * 4) = v; fence(); };
  auto peek = [&](int word) INL { return *(volatile LDS unsigned*)(lds + word + w4 * 4); };
  auto await = [&](int word, unsigned seen, unsigned v) INL { if (__builtin_expect(rfl(seen) < v, 0)) lds_wait(lds + word + w4 * 4, v); fence(); };

  if (__builtin_expect(w < 4, 1)) {
    auto layernorm = [&](auto&& hook) INL {
      get_tag();
      STAMP(1);
      u32x2 stat;
#pragma unroll
      for (int i = 0; i < 2; ++i) {
        f32x2 v[4];
#pragma unroll
        for (int e = 0; e < 4; ++e) asm volatile("v_lshlrev_b32 %0, 16, %2\n\tv_and_b32 %1, 0xffff0000, %2" : "=&v"(v[e][0]), "=v"(v[e][1]) : "v"(xr[i][e]));
        hook(5 * i);
        const float x0 = u2f(rfl(f2u(v[0][0])));
#pragma unroll
        for (int e = 0; e < 4; ++e) v[e] -= dup(x0);
        const f32x2 s1 = (v[0] + v[1]) + (v[2] + v[3]), s2 = fma2(v[0], v[0], v[1] * v[1]) + fma2(v[2], v[2], v[3] * v[3]);
        float t1 = s1[0] + s1[1], t2 = s2[0] + s2[1];
        asm volatile("" : "+v"(t1), "+v"(t2));
        hook(5 * i + 1);
        const float md = wave_sum(t1) * (1.f / D);
        float var = __builtin_fmaf(-md, md, wave_sum(t2) * (1.f / D));
        asm volatile("" : "+v"(var));
        hook(5 * i + 2);
        const float m = rb(x0 + md), sd = rb(__builtin_amdgcn_sqrtf(rb(rb(__builtin_fmaxf(var, 0.f)) + 1e-5f))), rs = __builtin_amdgcn_rcpf(sd);
        float c = (x0 - m) * rs;
        asm volatile("" : "+v"(c));
        hook(5 * i + 3);
        u32x4 o;
#pragma unroll
        for (int e = 0; e < 4; ++e) { const f32x2 n = fma2(v[e], dup(rs), dup(c)); o[e] = pk2(n[0], n[1]); }
        *(LDS u32x4*)(lds + LNB + (2 * w4 + i) * LNROW + lane * 16) = o;
        hook(5 * i + 4);
        stat[i] = pk2(m, sd);
      }
      __builtin_amdgcn_raw_buffer_store_b64(stat, rws, lane ? 0x7f000000u : 0u, WS_ST + (row0 + 2 * w4) * 4, SC1);
      STAMP(21);
      bar();
      STAMP(22);
#pragma unroll
      for (int i = 0; i < 2; ++i) {
        LDS char* p = lds + LNB + (j16 >> 2) * LNROW + (i * 256 + w4 * 64 + g * 16 + (j16 & 3) * 4) * 2;
        st16(__builtin_bit_cast(u32x4, cat(trd(p), trd(p + 4 * LNROW))), rws, tid * 16, WS_XNT + (row0 >> 3) * XNP + i * 4096);
      }
    };
    unsigned ad[3][4], vh[4], vpoll, kh, kh10, a_xt;
    u32x2 xt[4];
    auto rdxt = [&](int u) INL { xt[u] = *(const LDS u32x2*)(lds + SBASE + CEX - 32 * 1024 + (a_xt ^ (u * 32))); };
    s16x8 wf[11][4];
    auto wd_addr = [&](int i) INL {
      const int q = j16 >> 2, f = ((q >> 1) & 1) + 2 * (g & 1);
#pragma unroll
      for (int u = 0; u < 4; ++u) {
        ad[i][u] = (i == 0 ? wl : i == 1 ? wl + 2 * SLOT : wl6) + (8 * g + q) * 128 + ((u ^ f) << 5) + (j16 & 3) * 8;
        asm volatile("" : "+v"(ad[i][u]));
      }
    };
    auto rdwd = [&](int s, int u) INL {
      const int pos = s % 7;
      LDS char* p = lds + (pos == 6 ? ad[2][u] : pos < 2 ? ad[1][u] + pos * 4096 : ad[0][u] + wdloc(pos));
      wf[s][u] = cat(trd(p), trd(p + 512));
    };
    auto h_addr = [&]() INL {
      wd_addr(0); wd_addr(1); wd_addr(2);
#pragma unroll
      for (int m = 0; m < 4; ++m) { const int mm = (m + w4) & 3; vh[m] = (mm >> 1) * (NU * 1024) + (g >> 1) * 1024 + (32 * (g & 1) + 16 * (mm & 1) + j16) * 16; }
      vpoll = WS_HF + (m2 * 4 + w4) * 128;
      a_xt = 32 * 1024 + nw * PAIR + mw * 2048 + j16 * 128 + (((g >> 1) ^ (j16 >> 1)) << 4) + (g & 1) * 8;
      asm volatile("" : "+v"(a_xt), "+v"(vh[0]), "+v"(vh[1]), "+v"(vh[2]), "+v"(vh[3]));
      kh = WS_H + m2 * 2 * NU * 1024 + w4 * (11 * 2048);
      kh10 = w4 == 3 ? 0x7f000000u : kh + 10 * 2048;
      asm volatile("" : "+s"(kh), "+s"(kh10), "+s"(vpoll));
    };

    if (gemm) {
      addr_x();
      addr_w();
      asm volatile("" : "+v"(vx[0]), "+v"(vx[1]), "+v"(vw[0]), "+v"(vw[1]), "+v"(vw[2]));
      unsigned ax[4], aw[2];
      {
        const int id = (j16 >> 1) & 7, f = (id & 4) | ((id & 1) << 1) | ((id >> 1) & 1), cg = 2 * (g & 1) + (g >> 1);
#pragma unroll
        for (int s = 0; s < 2; ++s) { ax[s] = (mw * 64 + j16) * 128 + (((4 * s + cg) ^ f) << 4); ax[s + 2] = ax[s] + 2 * SLOT; }
        aw[0] = GOFF + (g & 1) * WBLK + (8 * (g >> 1) + (j16 >> 2)) * 192 + nw * 96 + (j16 & 3) * 8;
        aw[1] = aw[0] + 2 * SLOT;
      }
      unsigned a_st = STO + (mw * 64 + j16) * 8, a_cs = CSO + (nw * 48 + j16) * 16, so_h[3];
#pragma unroll
      for (int u = 0; u < 3; ++u) {
        const int unit = (n0 >> 4) + nw * 3 + u;
        so_h[u] = nt < 14 || unit >= 84 ? WS_H + ((mt * 4 + mw * 2) * NU + unit) * 1024 : 0x7f000000u;
      }
      const int fi = (nt < 14 ? nt * 2 + nw : 28) - 7 * lane;
      unsigned vflag = lane < 4 && fi >= 0 && fi < 8 && (nt < 14 || nw == 1) ? WS_HF + ((mt * 2 + mw) * 4 + lane) * 128 + fi * 8 : 0x7f000000u;
      asm volatile("" : "+v"(a_st), "+v"(a_cs), "+s"(so_h[0]), "+s"(so_h[1]), "+s"(so_h[2]), "+v"(vflag));
      asm volatile("" : "+v"(ax[0]), "+v"(ax[1]), "+v"(ax[2]), "+v"(ax[3]), "+v"(aw[0]), "+v"(aw[1]));
      h_addr();
      SB();
      STAMP(26);
#pragma unroll
      for (int i = 0; i < 10; ++i) issue(0, i);
      STAMP(20);
      waitv<11>();
      layernorm([&](int k) INL { issue(1, k); });
#pragma unroll
      for (int i = 10 - D2F; i < 10; ++i) issue(2, i);
      STAMP(23);
      f32x4 acc[4][6] = {};
      G1Frag fr[2];
      auto half = [&](G1Frag& c, bool domm, G1Frag& n, bool dord, int s, int jj) INL {
#pragma unroll
        for (int t = 0; t < 24; ++t) {
          if (domm) acc[t & 3][t >> 2] = mfma16(cat(c.w[t >> 2 << 1], c.w[(t >> 2 << 1) + 1]), c.x[t & 3], acc[t & 3][t >> 2]);
          SB();
          const int hi = s % 3 == 2, so = s % 3 == 1 ? SLOT : 0;
#pragma unroll
          for (int k = SPR ? t : (t & 1 ? 16 : t); k < 16 && k < (SPR ? t + 1 : t + 2); ++k) {
            const int u = (k - 4) >> 1;
            if (k < 4) n.x[k] = *(const LDS s16x8*)(lds + so + ax[jj + 2 * hi] + k * 2048);
            else n.w[k - 4] = trd(lds + aw[hi] + so + jj * 2 * WBLK + (u / 3) * 4 * WBLK + (u % 3) * 32 + (k & 1) * 768);
          }
          SB();
        }
      };
#pragma unroll
      for (int s = 0; s < 8; ++s) {
        if (s == 2) STAMP(27);
        if (s == 0) waitv<13 + D2F>(); else if (s == 1) waitv<D2F>(); else if (s == 2) waitv<0>();
        if (s == 3) STAMP(29);
        if (s == 0) STAMP(19);
        bar();
        STAMP(2 + s);
        half(fr[1], s > 0, fr[0], true, s, 0);
        half(fr[0], true, fr[1], true, s, 1);
      }
      bar();
      STAMP(10);
      u32x2 st[4], bm[4]; s16x8 cs[3];
      float kg[4];
#pragma unroll
      for (int t = 0; t < 24; ++t) {
        acc[t & 3][t >> 2] = mfma16(cat(fr[1].w[t >> 2 << 1], fr[1].w[(t >> 2 << 1) + 1]), fr[1].x[t & 3], acc[t & 3][t >> 2]);
        SB();
        if (t < 4) st[t] = *(const LDS u32x2*)(lds + a_st + t * 128);
        else if (t < 7) cs[t - 4] = *(const LDS s16x8*)(lds + a_cs + (t - 4) * 256);
        else if (t < 11) rdxt(t - 7);
        else if (t >= 12 && t < 16) { bm[t - 12] = u32x2{g ? 0 : st[t - 12][1], 0}; asm volatile("" : "+v"(bm[t - 12])); }
        else if (t >= 16 && t < 20) { kg[t - 16] = -1.44269504f * __builtin_amdgcn_rcpf(u2f(st[t - 16][0])); asm volatile("" : "+v"(kg[t - 16])); }
        SB();
      }
#pragma unroll
      for (int t = 0; t < 24; ++t) {
        const int m = (t & 1) + 2 * (t / 12), u = (t >> 2) % 3, up = (t >> 1) & 1;
        const s16x4 c = up ? __builtin_shufflevector(cs[u], cs[u], 4, 5, 6, 7) : __builtin_shufflevector(cs[u], cs[u], 0, 1, 2, 3);
        acc[m][u + 3 * up] = __builtin_amdgcn_mfma_f32_16x16x16bf16_1k(c, __builtin_bit_cast(s16x4, bm[m]), acc[m][u + 3 * up], 0, 0, 0);
        SB();
      }
      STAMP(11);
      unsigned seen;
#pragma unroll
      for (int mp = 0; mp < 2; ++mp) {
#pragma unroll
        for (int u = 0; u < 3; ++u) {
          unsigned o[2][2];
          const int q = mp * 3 + u;
          if (q == B9Q) bar();
          if (q == RD0 || q == RD1) {
            await(LANDED, seen, q == RD0 ? 1 : 2);
#pragma unroll
            for (int k = 0; k < 8; ++k) rdwd((q == RD0 ? 0 : 2) + (k >> 2), k & 3);
            post(USED, q == RD0 ? 1 : 2);
          }
          if (q == RD0 - 1 || q == RD1 - 1) { seen = peek(LANDED); SB(); }
#pragma unroll
          for (int mi = 0; mi < 2; ++mi) {
            const int m = mp * 2 + mi;
            const float sd = u2f(st[m][0]);
#pragma unroll
            for (int e = 0; e < 2; ++e) {
              f32x2 hv;
              if (q < PKQ) {
                const f32x2 gg = f32x2{acc[m][u][2 * e], acc[m][u][2 * e + 1]} * dup(kg[m]);
                const f32x2 dn = fma2(f32x2{__builtin_amdgcn_exp2f(gg[0]), __builtin_amdgcn_exp2f(gg[1])}, dup(sd), dup(sd));
                hv = f32x2{acc[m][u + 3][2 * e], acc[m][u + 3][2 * e + 1]} * f32x2{__builtin_amdgcn_rcpf(dn[0]), __builtin_amdgcn_rcpf(dn[1])};
              } else {
#pragma unroll
                for (int k = 0; k < 2; ++k) {
                  const float ex = __builtin_amdgcn_exp2f(one(acc[m][u][2 * e + k] * kg[m]));
                  hv[k] = one(acc[m][u + 3][2 * e + k] * __builtin_amdgcn_rcpf(one(__builtin_fmaf(ex, sd, sd))));
                }
              }
              o[mi][e] = pk2(hv[0], hv[1]);
            }
          }
          const auto r0 = __builtin_amdgcn_permlane16_swap(o[0][0], o[1][0], false, false);
          const auto r1 = __builtin_amdgcn_permlane16_swap(o[0][1], o[1][1], false, false);
          const u32x4 ov = {r0[0], r1[0], r0[1], r1[1]};
          st16(ov, rws, lane * 16 + mp * (NU * 1024), so_h[u]);
          SB();
        }
      }
      STAMP(12);
      if (B9Q == 6) bar();
      waitv<0>();
      __builtin_amdgcn_raw_buffer_store_b64(u32x2{(unsigned)tag, (unsigned)(tag >> 32)}, rws, vflag, 0, SC1);
    } else {
      const int cj = (xcd & 3) * 2 + (r - 30), kg = (tid * 745) >> 14, cc = tid - kg * 22, col = cj * 176 + cc * 8;
      const unsigned vo = (kg < 11 && col < F) ? kg * (F * 2) + col * 2 : 0x7f000000u;
      u32x4 cv[2][8];
      auto ldcs = [&](int sl) INL {
#pragma unroll
        for (int i = 0; i < 8; ++i) cv[sl & 1][i] = __builtin_amdgcn_raw_buffer_load_b128(rw, vo + (sl * 8 + i) * (11 * F * 2), 0, 0);
      };
      ldcs(0);
      waitv<9>();
      layernorm([](int) INL {});
      f32x2 a[4] = {};
#pragma unroll
      for (int sl = 0; sl < 6; ++sl) {
        if (sl == 1) waitv<0>();
        if (sl < 5) ldcs(sl + 1);
        if (sl < 4) bar();
#pragma unroll
        for (int i = 0; i < 8; ++i)
#pragma unroll
          for (int e = 0; e < 4; ++e) { const unsigned q = cv[sl & 1][i][e]; a[e] += f32x2{blo(q), bhi(q)}; }
        asm volatile("" : "+v"(a[0]), "+v"(a[1]), "+v"(a[2]), "+v"(a[3]));
      }
      if (kg < 11) {
        *(LDS f32x4*)(lds + kg * 704 + cc * 32) = f32x4{a[0][0], a[0][1], a[1][0], a[1][1]};
        *(LDS f32x4*)(lds + kg * 704 + cc * 32 + 16) = f32x4{a[2][0], a[2][1], a[3][0], a[3][1]};
      }
      bar();
      if (tid < 176) {
        float s = 0.f;
#pragma unroll
        for (int k = 0; k < 11; ++k) s += *(const LDS float*)(lds + k * 704 + tid * 4);
        __builtin_amdgcn_raw_buffer_store_b64(u32x2{pk2(-s, rb(s) - s), (unsigned)tag}, rws, tid * 8, WS_CS + ((xcd >> 2) * CSP + cj * 176) * 8, SC1);
      }
      h_addr();
      bar(); bar(); bar(); bar();
#pragma unroll
      for (int u = 0; u < 4; ++u) rdxt(u);
      await(LANDED, 0, 2);
#pragma unroll
      for (int k = 0; k < 16; ++k) rdwd(k >> 2, k & 3);
      post(USED, 2);
      bar();
    }

    STAMP(13);
    f32x4 a2[4][4] = {};
#pragma unroll
    for (int u = 0; u < 4; ++u) a2[0][u] = f32x4{blo(xt[u][0]), bhi(xt[u][0]), blo(xt[u][1]), bhi(xt[u][1])};
    unsigned a_p[3], a_y = SBASE + (4 * g + (j16 >> 2)) * 160 + w4 * 32 + (j16 & 3) * 8, a_o[2], v_o = (lane >> 3) * 1024 + (lane & 7) * 16;
#pragma unroll
    for (int m = 1; m < 4; ++m) a_p[m - 1] = wdloc(m - 1) + ((w4 - m) & 3) * 8192 + lane * 16;
    a_o[0] = wdloc(4) + wl + j16 * 144 + g * 8;
    a_o[1] = wdloc(4) + wl + (lane >> 3) * 144 + (lane & 7) * 16;
    asm volatile("" : "+v"(a2[0][0]), "+v"(a2[0][1]), "+v"(a2[0][2]), "+v"(a2[0][3]));
    asm volatile("" : "+v"(a_p[0]), "+v"(a_p[1]), "+v"(a_p[2]), "+v"(a_y), "+v"(a_o[0]), "+v"(a_o[1]), "+v"(v_o));
    if (HSLEEP) __builtin_amdgcn_s_sleep(HSLEEP);
    if (!watch<HNAP, 1>(ws + vpoll, tag)) poll<HNAP>(ws, vpoll + (lane & 7) * 8, tag);
    STAMP(14);
    auto fs = [](int n) INL { return n < 32 ? n >> 2 : 8 + (n - 32) % 3; };
    auto fm = [](int n) INL { return ((n < 32 ? n : (n - 32) / 3) + 1) & 3; };
    s16x8 hf[44];
    auto ldh = [&](int n) INL {
      hf[n] = __builtin_bit_cast(s16x8, __builtin_amdgcn_raw_buffer_load_b128(rws, vh[fm(n)] + (fs(n) & 1) * 2048, fs(n) == 10 ? kh10 : kh + (fs(n) >> 1) * 4096, SC1));
    };
#pragma unroll
    for (int n = 0; n < HF; ++n) { ldh(n); SB(); }
    unsigned landed;
    u32x4 dn[2];
    SB();
#pragma unroll
    for (int n = 0; n < 44; ++n) {
      if (n == 2 || n == 22) landed = peek(LANDED);
      if (n == 4 || n == 24) await(LANDED, landed, n == 4 ? 3 : 4);
#pragma unroll
      for (int u = 0; u < 4; ++u) {
        a2[fm(n)][u] = mfma16(wf[fs(n)][u], hf[n], a2[fm(n)][u]);
        SB();
        if (u == 1 && n >= 4 && n < 32) rdwd((n >> 2) + 3, n & 3);
        if (u == 3 && n + HF < 44) ldh(n + HF);
        if (n >= 35 && (n - 35) % 3 == 0) *(LDS f32x4*)(lds + wdloc((n - 35) / 3) + wl + u * 1024 + lane * 16) = a2[(n - 35) / 3 + 1][u];
        SB();
      }
      if (n == 41) post(DONE, 1);
      if (n == 42) { dn[0] = *(volatile LDS u32x4*)(lds + DONE); dn[1] = *(volatile LDS u32x4*)(lds + DONE + 16); SB(); }
      if (n == 3) STAMP(15);
    }
    STAMP(16);
    auto all4 = [](u32x4 d) INL { return rfl(d[0] & d[1] & d[2] & d[3]); };
#pragma unroll 1
    for (unsigned spins = 0; spins < SPIN_CAP && !all4(dn[0]); ++spins) {
      dn[0] = *(volatile LDS u32x4*)(lds + DONE);
      dn[1] = *(volatile LDS u32x4*)(lds + DONE + 16);
    }
    fence();
    STAMP(31);
    f32x4 pp[4][3];
    u32x2 y[4];
    auto rdy = [&]() INL {
#pragma unroll
      for (int u = 0; u < 4; ++u) y[u] = __builtin_bit_cast(u32x2, trd(lds + a_y + (u >> 1) * PAIR + (u & 1) * 4096));
    };
#pragma unroll
    for (int u = 0; u < 4; ++u)
#pragma unroll
      for (int m = 0; m < 3; ++m) pp[u][m] = *(const LDS f32x4*)(lds + a_p[m] + u * 1024);
    rdy();
    SB();
#pragma unroll
    for (int u = 0; u < 4; ++u) a2[0][u] += (pp[u][0] + pp[u][1]) + pp[u][2];
    asm volatile("" : "+v"(a2[0][0]), "+v"(a2[0][1]), "+v"(a2[0][2]), "+v"(a2[0][3]));
    if (__builtin_expect(!all4(dn[1]), 0)) {
#pragma unroll 1
      for (unsigned spins = 0; spins < SPIN_CAP && !all4(*(volatile LDS u32x4*)(lds + DONE + 16)); ++spins) {}
      fence();
      rdy();
    }
    STAMP(17);
#pragma unroll
    for (int u = 0; u < 4; ++u) {
      const f32x4 s = a2[0][u] + f32x4{blo(y[u][0]), bhi(y[u][0]), blo(y[u][1]), bhi(y[u][1])};
      *(LDS u32x2*)(lds + a_o[0] + u * 32) = u32x2{pk2(s[0], s[1]), pk2(s[2], s[3])};
    }
    fence();
    const u32x4 o0 = *(const LDS u32x4*)(lds + a_o[1]), o1 = *(const LDS u32x4*)(lds + a_o[1] + 8 * 144);
    st16(o0, mkr(out, L * D * 2), v_o, (m2 * 64 + w4 * 16) * 1024 + n2 * 128);
    st16(o1, mkr(out, L * D * 2), v_o, (m2 * 64 + w4 * 16 + 8) * 1024 + n2 * 128);
    STAMP(18);
    STAMP_OUT(0);
    asm volatile(".Lk_code_end0:");
  } else {
    const int cl = nw, hh = mw;
    STAMP(21);
    addr_x();
    addr_w();
    asm volatile("" : "+v"(vx[0]), "+v"(vx[1]), "+v"(vw[0]), "+v"(vw[1]), "+v"(vw[2]));
    auto part = [&](int s, int i0, int i1) INL {
      if (__builtin_expect(gemm, 1)) {
#pragma unroll
        for (int i = i0; i < i1; ++i) issue(s, i);
      }
    };
    auto stage = [&](int s) INL { part(s, 0, 10); };
    unsigned vd[4], vcs, pa[4], vxi, vzero, vimg, vxf, vtw[2], vtab[2], vex[2], vyf, vys, vco, vxt[2], vyl, vyw;
#pragma unroll
    for (int i = 0; i < 4; ++i) { const int row = lane >> 3, f = ((row >> 1) & 1) + 2 * (i & 1); vd[i] = (w4 * 352 + i * 8 + row) * 1024 + n2 * 128 + (((lane & 7) ^ (2 * f)) << 4); }
    vcs = tid < 96 ? (n0 + tid) * 8 : (CSP + n0 + min(tid, 191) - 96) * 8;
#pragma unroll
    for (int i = 0; i < 4; ++i) pa[i] = SBASE + cl * PAIR + (j16 & 1 ? TX : TY) + (847 - 256 * i - hh * 64 - ((15 + j16 - 8 * g) >> 1)) * 4;
    vxi = (tid >> 1) * XNP + (tid & 1) * 16 + cp * 32;
    vimg = SBASE + (tid & 1) * PAIR + XIMG + (tid >> 1) * 16;
    vzero = lane * 16 + (hh ? SYNC : SBASE + cl * PAIR + (lane < 2 ? TY + 4096 : TX + 4064));
    vtab[0] = SBASE + cl * PAIR + (255 - (hh ? (lane >> 5 ? 64 : 128) + (lane & 31) : lane)) * 16;
    vtab[1] = vtab[0] - (hh ? 32 : 192) * 16;
    vex[0] = (hh ? SBASE + cl * PAIR + CEX : SLOT + LEFT + cl * 4096) + lane * 16;
    vex[1] = (hh ? SLOT + LEFT + cl * 4096 : SBASE + cl * PAIR + CEX) + lane * 16;
    vyf = lane == 0 ? WS_YF + (hh * 256 + cp) * 16 + cl * 8 : lane == 1 && b == 0 && w4 == 0 ? WS_E : 0x7f000000u;
    vys = ch * 4096 + (hh ? 1 + (g & 1) : 3 * (g & 1)) * 512 + (j16 >> 2) * 128 + (g >> 1) * 64 + (j16 & 3) * 16;
    vco = CSO + (tid < 96 ? tid * 16 : (tid - 96) * 16 + 8);
    vxt[0] = (m2 * 64 + w4 * 16 + (lane >> 3)) * 1024 + n2 * 128 + (((lane & 7) ^ g) << 4);
    vxt[1] = vxt[0] ^ 64;
    vyl = (n2 * 64 + w4 * 16 + (lane >> 3)) * 4096 + m2 * 128 + (lane & 7) * 16;
    vyw = SBASE + cl * PAIR + hh * 4096 + (lane >> 3) * 160 + (lane & 3) * 32 + (lane & 4) * 4;
    asm volatile("" : "+v"(vxt[0]), "+v"(vxt[1]), "+v"(vyl), "+v"(vyw));
    asm volatile("" : "+v"(vco), "+v"(vex[0]), "+v"(vex[1]), "+v"(vyf), "+v"(vys));
    vxf = tid ? 0x7f000000u : WS_XNF + b * 8;
    vtw[0] = lane < 11 ? (b * 43 + w4 * 11 + lane) * 128 : 0x7f000000u;
    vtw[1] = lane < 11 ? (r * 43 + w4 * 11 + lane) * 1024 + ((384u - (unsigned)(u64)wd) & 896u) : 0x7f000000u;
    asm volatile("" : "+v"(vd[0]), "+v"(vd[1]), "+v"(vd[2]), "+v"(vd[3]), "+v"(vcs), "+v"(pa[0]), "+v"(pa[1]), "+v"(pa[2]), "+v"(pa[3]));
    asm volatile("" : "+v"(vxi), "+v"(vimg), "+v"(vzero), "+v"(vxf), "+v"(vtw[0]), "+v"(vtw[1]), "+v"(vtab[0]), "+v"(vtab[1]));
    auto issue_wd = [&](int s, int p) INL {
      const int pos = s % 7;
      dma16(pos == 6 ? wl6 : wl, (pos == 6 ? 0 : wdloc(pos)) + p * 1024, vd[p], rwd, s * 32768);
    };
    SPRIO(1);
    STAMP(22);
    waitv<1>();
    get_tag();
    STAMP(1);
    u32x2 tg = {(unsigned)tag, (unsigned)(tag >> 32)};
    asm volatile("" : "+v"(tg));
    float prm[6];
#pragma unroll
    for (int i = 0; i < 6; ++i) asm volatile("v_lshlrev_b32 %0, 16, %1" : "=v"(prm[i]) : "v"(xr[i >> 2][i & 3]));
    const float p_ldt = prm[0], p_lar = prm[1], p_aim = prm[2], p_cr = prm[3], p_ci = prm[4], skip = one(lane ? 0.f : prm[5]);
    const float dt = rb(exp_f(p_ldt));
    const float s2 = -rb(exp_f(p_lar)) * dt * 1.44269504f, th = p_aim * dt;
    const unsigned eb = min(max(wave_max(max(f2u(p_cr) << 1, f2u(p_ci) << 1)) >> 24, 1u), 253u);
    const float sc = u2f((254u - eb) << 23), ksc = 2.f * dt * u2f(eb << 23);
    f32x2 z1 = cpow2(s2, th, 1.f), z8c = cpow2(s2, -th, 8.f), zb = cpow2(s2, th, 256.f);
    asm volatile("" : "+v"(z1), "+v"(z8c), "+v"(zb));
    const cmulr z256 = mul_by(zb);
    STAMP(2);
    LDS char* pr = lds + SBASE + cl * PAIR;
    LDS char* ks = pr + hh * KSW;
    auto put = [&](int row, f32x2 v) INL { *(LDS unsigned*)(ks + row * KROW + lane * 4) = pkh(v[0], v[1]); };
    auto putu = [&](int reg, int h, f32x2 v) INL { put((reg & 3) + 4 * h + 8 * (reg >> 2), v); };
    f16x8 vf[8];
    {
      f32x2 v[4];
      const cmulr m1 = mul_by(z8c);
      v[0] = f32x2{1.f, 0.f}; v[1] = z8c; v[2] = cmul(z8c, m1); v[3] = cmul(v[2], m1);
      const cmulr m4 = mul_by(cmul(v[3], m1));
#pragma unroll
      for (int k = 0; k < 32; ++k) {
        if (k == 4) v[0] = m4.z; else if (k > 4) v[k & 3] = cmul(v[k & 3], m4);
        put(k, v[k & 3]);
      }
      fence();
#pragma unroll
      for (int s = 0; s < 8; ++s) vf[s] = *(const LDS f16x8*)(ks + (lane & 31) * KROW + s * 32 + (lane >> 5) * 16);
      fence();
    }
    STAMP(3);
    {
      f32x2 e[5];
      const cmulr m1 = mul_by(z1), m2_ = mul_by(cmul(z1, m1));
      e[0] = f32x2{p_cr, p_ci} * dup(sc);
      putu(0, 0, e[0]);
#pragma unroll
      for (int k = 0; k < 4; ++k) { putu(2 * k + 1, 0, cmul(e[k], m1)); e[k + 1] = cmul(e[k], m2_); putu(2 * k + 2, 0, e[k + 1]); }
#pragma unroll
      for (int k = 0; k < 5; ++k) e[k] = cmul(e[k], z256);
      putu(0, 1, e[0]); putu(2, 1, e[1]); putu(3, 1, e[2]); putu(4, 1, e[2]); putu(5, 1, e[2]); putu(6, 1, e[3]); putu(7, 1, e[4]); putu(8, 1, e[4]);
#pragma unroll
      for (int k = 0; k < 6; ++k) {
        const int reg = k < 2 ? 9 + 2 * k : k < 5 ? 5 + 2 * k : 13, h = k < 2 || k == 5;
        e[0] = cmul(e[0], z256); putu(reg, h, e[0]);
        e[4] = cmul(e[4], z256); putu(reg + 1, h, e[4]);
        if (k < 2) { e[2] = cmul(e[2], z256); putu(k ? 1 : 15, 1, e[2]); }
        if (TWP == 0 && k == 5) touch(DUMMY, vtw[0], rwd, 0);
      }
      fence();
    }
    STAMP(4);
    bar();
    f32x16 kd = {};
    {
      f16x8 uf[8];
#pragma unroll
      for (int s = 0; s < 8; ++s) uf[s] = *(const LDS f16x8*)(ks + (lane & 31) * KROW + s * 32 + (lane >> 5) * 16);
      SB();
#pragma unroll
      for (int s = 0; s < 8; ++s) kd = __builtin_amdgcn_mfma_f32_32x32x16_f16(uf[s], vf[s], kd, 0, 0, 0);
    }
    kd *= ksc;
    const bool h = lane >> 5, odd = lane & 1;
    if (hh) {
      *(LDS f32x4*)(pr + KEX1 + lane * 16) = f32x4{kd[0], kd[1], kd[2], kd[3]};
      *(LDS f32x4*)(pr + KEX1 + 1024 + lane * 16) = f32x4{kd[4], kd[5], kd[6], kd[7]};
      *(LDS f32x4*)(pr + KEX1 + 2048 + lane * 16) = f32x4{kd[8], kd[13], kd[14], kd[15]};
    } else {
      *(LDS f32x4*)(pr + KEX0 + lane * 16) = f32x4{kd[9], kd[10], kd[11], kd[12]};
      *(LDS f32x2*)(pr + KEX0 + 1024 + lane * 8) = f32x2{kd[15], kd[1]};
    }
    STAMP(5);
    SPRIO(0);
    bar();
    STAMP(10);
    {
      u32x4 tb[5];
      auto tail = [&](float a, float c, float a3, float a4, float a5, int k) INL {
        const unsigned aa = pk2(a, a), cc = pk2(c, c);
        tb[k] = u32x4{cc, pk2(a5, a4), pk2(a3, a), aa};
        tb[k + 1] = u32x4{cc, pk2(c, a5), pk2(a4, a3), aa};
      };
      if (hh) {
        const f32x4 p = *(const LDS f32x4*)(pr + KEX0 + lane * 16);
        const f32x2 q = *(const LDS f32x2*)(pr + KEX0 + 1024 + lane * 8);
#pragma unroll
        for (int k = 0; k < 2; ++k) {
          const float a = one(kd[9 + 2 * k] + p[2 * k]), c = one(kd[10 + 2 * k] + p[2 * k + 1]), m = one(kd[k ? 1 : 15] + q[k]);
          tail(a, c, h ? m : a, h ? m : odd ? c : a, h ? m : c, 2 * k);
        }
        tb[4][0] = 0;
      } else {
        const f32x4 p0 = *(const LDS f32x4*)(pr + KEX1 + lane * 16), p1 = *(const LDS f32x4*)(pr + KEX1 + 1024 + lane * 16);
        const f32x4 p2 = *(const LDS f32x4*)(pr + KEX1 + 2048 + lane * 16);
        float t[9];
#pragma unroll
        for (int k = 0; k < 9; ++k) t[k] = one(kd[k] + (k < 4 ? p0[k] : k < 8 ? p1[k - 4] : p2[0]));
        t[0] += skip;
        t[1] = h ? t[0] : t[1];
        tb[0] = u32x4{pk2(t[7], t[6]), pk2(t[5], t[4]), pk2(t[3], t[2]), pk2(t[1], t[0])};
        tb[1] = u32x4{pk2(t[8], t[7]), pk2(t[6], t[5]), pk2(t[4], t[3]), pk2(t[2], t[1])};
        const float a = one(kd[13] + p2[1]), c = one(kd[14] + p2[2]);
        tail(a, c, a, odd ? c : a, c, 2);
        tb[4][0] = lane ? 0 : tb[0][3] >> 16;
      }
      tb[4][1] = tb[4][2] = tb[4][3] = 0;
      asm volatile("" : "+v"(tb[0]), "+v"(tb[1]), "+v"(tb[2]), "+v"(tb[3]), "+v"(tb[4]));
      STAMP(19);
#pragma unroll
      for (int k = 0; k < 4; ++k) *(LDS u32x4*)(lds + vtab[k >> 1] + (k & 1 ? TY : TX)) = tb[k];
      if (lane < (hh ? 5 : 36)) *(LDS u32x4*)(lds + vzero) = tb[4];
      part(2, 0, 10 - D2F);
    }
    if (TWP == 2) touch(DUMMY, vtw[0], rwd, 0);
    STAMP(6);
    bar();
    __builtin_amdgcn_raw_buffer_store_b64(tg, rws, vxf, 0, SC1);
    stage(3);
    if (TWP == 1) touch(DUMMY, vtw[0], rwd, 0);
    waitv<10 + (TWP == 1)>();
    bar();
    stage(4);
    waitv<10>();
    bar();
    stage(5);
    waitv<10>();
    bar();
    stage(6);
    if (TW3) touch(DUMMY, vtw[1], rwd, 0);
    waitv<10 + (TW3 != 0)>();
    bar();
    {
      u64 fl;
      asm volatile("buffer_load_dwordx2 %0, %1, %2, %3 offen sc1" : "=v"(fl) : "v"(tid * 8), "s"(rws), "s"(WS_XNF) : "memory");
      stage(7);
      if (gemm) waitv<10>(); else waitv<0>();
      asm volatile("" : "+v"(fl));
      if (__builtin_expect(!wave_all(fl == tag), 0)) poll<4>(ws, WS_XNF + tid * 8, tag);
    }
    STAMP(7);
    bar();
#pragma unroll
    for (int i = 0; i < 2; ++i) dma16(cl * PAIR + hh * 2048, SBASE + CEX + i * 1024, vxt[i], rx, i * 8192);
    unsigned gst; u32x2 csg; u32x4 xi[2];
    asm volatile("buffer_load_dword %0, %1, %2, %3 offen sc1" : "=v"(gst) : "v"((tid & 127) * 4), "s"(rws), "s"(WS_ST + (mt & 15) * 512) : "memory");
    asm volatile("buffer_load_dwordx2 %0, %1, %2, %3 offen sc1" : "=v"(csg) : "v"(vcs), "s"(rws), "s"(WS_CS) : "memory");
#pragma unroll
    for (int i = 0; i < 2; ++i)
      asm volatile("buffer_load_dwordx4 %0, %1, %2, %3 offen sc1" : "=v"(xi[i]) : "v"(vxi), "s"(rws), "s"(WS_XNT + i * (128 * XNP)) : "memory");
#pragma unroll
    for (int i = 0; i < 4; ++i) issue_wd(0, i);
    waitv<10>();
    STAMP(8);
    bar();
    waitv<6>();
    asm volatile("" : "+v"(gst), "+v"(csg));
    if (tid < 128) *(LDS u32x2*)(lds + STO + tid * 8) = u32x2{gst & 0xffff0000u, __builtin_amdgcn_perm(gst, gst, 0x01000100)};
    if (__builtin_expect(gemm && !wave_all(csg[1] == (unsigned)tag), 0)) {
#pragma unroll 1
      for (unsigned spins = 0; spins < SPIN_CAP && !wave_all(csg[1] == (unsigned)tag); ++spins) {
        __builtin_amdgcn_s_sleep(2);
        asm volatile("buffer_load_dwordx2 %0, %1, %2, %3 offen sc1\n\ts_waitcnt vmcnt(0)" : "=&v"(csg) : "v"(vcs), "s"(rws), "s"(WS_CS) : "memory");
      }
    }
    if (tid < 192) *(LDS u32x2*)(lds + vco) = u32x2{csg[0], csg[0]};
#pragma unroll
    for (int i = 0; i < 4; ++i) issue_wd(1, i);
    STAMP(9);
    bar();
#pragma unroll
    for (int i = 8; i < 16; ++i) issue_wd(i >> 2, i & 3);
    waitv<8>();
    post(LANDED, 1);
    asm volatile("" : "+v"(xi[0]), "+v"(xi[1]));
#pragma unroll
    for (int i = 0; i < 2; ++i) *(LDS u32x4*)(lds + vimg + XPAD + i * 2048) = xi[i];
#pragma unroll
    for (int i = 16; i < 28; ++i) issue_wd(i >> 2, i & 3);
    waitv<12>();
    post(LANDED, 2);
    if (NPRE > 12) await(USED, 0, 1);
#pragma unroll
    for (int i = 28; i < 16 + NPRE; ++i) issue_wd(i >> 2, i & 3);
    LDS char* pb = pr + XIMG + XPAD + (16 * j16 + 8 * g - 16) * 2 - hh * 256 - 192;
    CvFrag cf[2];
    auto rdc = [&](CvFrag& f, int rr, int k) INL {
      const int i = k / 3, c = k % 3;
      if (c == 2) f.b[i] = *(const LDS s16x8*)(pb + i * 512 + (3 - rr) * 64);
      else f.a[2 * i + c] = *(const LDS s16x4u*)(lds + pa[i >> 1] + (i & 1 ? 0 : 512) + (3 - rr) * 64 + c * 8);
    };
#pragma unroll
    for (int k = 0; k < 24; ++k) if (k % 3 < 2) rdc(cf[0], 0, k);
    STAMP(18);
    bar();
#pragma unroll
    for (int k = 2; k < 24; k += 3) rdc(cf[0], 0, k);
    STAMP(11);
    SPRIO(CPRIO);
    unsigned seen = 0;
    f32x4 ca[8] = {};
    auto conv = [&](int rr) INL {
      int n = 0;
#pragma unroll
      for (int u = 0; u < 8; ++u)
#pragma unroll
        for (int t = u; t < 8; ++t) {
          ca[t] = mfma16(cat(cf[rr & 1].a[2 * u], cf[rr & 1].a[2 * u + 1]), cf[rr & 1].b[t - u], ca[t]);
          SB();
          if (SPC) { if (n < 32 && n % 4 < 3) rdc(cf[(rr + 1) & 1], rr + 1, n / 4 * 3 + n % 4); }
          else if (n < 32 && (n & 1) == 0) { rdc(cf[(rr + 1) & 1], rr + 1, n / 4 * 3 + n % 4); if (n % 4 == 0) rdc(cf[(rr + 1) & 1], rr + 1, n / 4 * 3 + 1); }
          const int q = rr * 36 + n;
          const int j = 16 + NPRE + (q - Q0) / PACE;
          if (q >= Q0 && (q - Q0) % PACE == 0 && j < 44) {
            if (j == 28 || j == 36) await(USED, seen, j == 28 ? 1 : 2);
            if (j == 27 || j == 35) seen = peek(USED);
            issue_wd(j >> 2, j & 3);
          }
          SB();
          ++n;
        }
    };
    conv(0); conv(1); conv(2);
    {
      unsigned o[4][2];
      auto last = [&](int h) INL {
        auto tile = [](int set, int k) INL { return (k >> 1) * 4 + (set ? 1 + (k & 1) : 3 * (k & 1)); };
        unsigned mate;
        f32x4 in[4];
#pragma unroll
        for (int ph = 0; ph < 2; ++ph) {
          int n = 0;
#pragma unroll
          for (int u = 0; u < 8; ++u)
#pragma unroll
            for (int k = 3; k >= 0; --k) {
              const int t = tile(ph ? h : 1 - h, k);
              if (t < u) continue;
              ca[t] = mfma16(cat(cf[1].a[2 * u], cf[1].a[2 * u + 1]), cf[1].b[t - u], ca[t]);
              SB();
              if (ph && n < 4) *(LDS f32x4*)(lds + vex[0] + n * 1024) = ca[tile(1 - h, n)];
              if (ph && n == 4) post(SYNC, 1);
              if (ph && n == 6) { waitv<8>(); post(LANDED, 3); }
              if (ph && n == 10) mate = *(volatile LDS unsigned*)(lds + SYNC + (w4 ^ 1) * 4);
              SB();
              ++n;
            }
        }
        STAMP(12);
        if (__builtin_expect(!rfl(mate), 0)) lds_wait(lds + SYNC + (w4 ^ 1) * 4, 1);
        fence();
        STAMP(20);
#pragma unroll
        for (int k = 0; k < 4; ++k) in[k] = *(const LDS f32x4*)(lds + vex[1] + k * 1024);
        SB();
#pragma unroll
        for (int k = 0; k < 4; ++k) {
          const f32x4 s = ca[tile(h, k)] + in[k];
          o[k][0] = pk2(s[0], s[1]); o[k][1] = pk2(s[2], s[3]);
        }
      };
      if (hh) last(1); else last(0);
      SPRIO(TPRIO);
#pragma unroll
      for (int kp = 0; kp < 2; ++kp) {
        const auto r0 = __builtin_amdgcn_permlane16_swap(o[2 * kp][0], o[2 * kp + 1][0], false, false);
        const auto r1 = __builtin_amdgcn_permlane16_swap(o[2 * kp][1], o[2 * kp + 1][1], false, false);
        const u32x4 ov = {r0[0], r1[0], r0[1], r1[1]};
        st16(ov, rws, vys, WS_YT + kp * 2048);
      }
      STAMP(13);
      waitv<2>();
      post(LANDED, 4);
      waitv<0>();
      STAMP(23);
      __builtin_amdgcn_raw_buffer_store_b64(tg, rws, vyf, 0, SC1);
    }
    STAMP(14);
    __builtin_amdgcn_s_sleep(YSLEEP);
    const unsigned yf = WS_YF + ((((m2 >> 2) + 1) >> 1 & 1) * 256 + n2 * 32 + w4 * 8) * 16;
    if (!watch<YNAP, 2>(ws + yf, tag)) poll<YNAP>(ws, yf + j16 * 8, tag);
    STAMP(15);
#pragma unroll
    for (int i = 0; i < 2; ++i) xi[i] = __builtin_amdgcn_raw_buffer_load_b128(rws, vyl, WS_YT + i * 32768, SC1);
#pragma unroll
    for (int i = 0; i < 2; ++i) *(LDS u32x4*)(lds + vyw + i * 1280) = xi[i];
    post(DONE + 16, 1);
    waitl0();
    STAMP(16);
    STAMP_OUT(1);
    asm volatile(".Lk_code_end1:");
  }
}

}

extern "C" void kernel_launch(void* const* d_in, const int* in_sizes, int n_in, void* d_out, int out_size, void* d_ws, size_t ws_size,
                              hipStream_t stream) {
  fused_kernel<<<256, 512, 0, stream>>>((const u16*)d_in[0], (const u16*)d_in[1], (const u16*)d_in[2], (const u16*)d_in[3], (const u16*)d_in[4],
                                        (const u16*)d_in[5], (const u16*)d_in[6], (const u16*)d_in[8], (const u16*)d_in[9], (const u16*)d_in[10],
                                        (u16*)d_out, (char*)d_ws);
}
```

```cpp
#include <hip/hip_runtime.h>
#include <hip/hip_bf16.h>

namespace {

typedef unsigned short u16;
typedef unsigned long long u64;
typedef __attribute__((ext_vector_type(4))) short s16x4;
typedef s16x4 __attribute__((aligned(4))) s16x4u;
typedef __attribute__((ext_vector_type(8))) short s16x8;
typedef __attribute__((ext_vector_type(8))) __bf16 bf16x8;
typedef __attribute__((ext_vector_type(2))) __bf16 bf16x2;
typedef __attribute__((ext_vector_type(8))) _Float16 f16x8;
typedef __attribute__((ext_vector_type(2))) _Float16 f16x2;
typedef __attribute__((ext_vector_type(2))) float f32x2;
typedef __attribute__((ext_vector_type(4))) float f32x4;
typedef __attribute__((ext_vector_type(16))) float f32x16;
typedef __attribute__((ext_vector_type(4))) unsigned u32x4;
typedef __attribute__((ext_vector_type(2))) unsigned u32x2;
typedef __amdgpu_buffer_rsrc_t rsrc_t;
#define LDS __attribute__((address_space(3)))
#define GLB __attribute__((address_space(1)))
#define INL __attribute__((always_inline))
#define SB() __builtin_amdgcn_sched_barrier(0)
#define RLX_AGENT __ATOMIC_RELAXED, __HIP_MEMORY_SCOPE_AGENT
#define RLX_WG __ATOMIC_RELAXED, __HIP_MEMORY_SCOPE_WORKGROUP

constexpr int L = 2048, D = 512, NM = 128, F = 1376, NU = F / 16;
constexpr int SC1 = 16;
#define HF 10
#define PRIO(x)
#define TW3 11
#define SPRIO(x) __builtin_amdgcn_s_setprio(x)
#define HNAP 2
#define YNAP 3
#define RD0 1
#define PACE 4
#define TWP 0
#define D2F 6
#define HSLEEP 8
#define CPRIO 0
#define TPRIO 0
#define NPRE 16
#define Q0 3
#define RD1 3
#define B9Q 5
#define PKQ 6
#define YSLEEP 4
static_assert(Q0 + (27 - NPRE) * PACE < 108 && RD0 < RD1 && NPRE >= 12 && NPRE <= 20 && RD0 < B9Q, "schedule");
constexpr unsigned SPIN_CAP = 1u << 18;

constexpr int WBLK = 3104, GOFF = 16384, UOFF = GOFF + 4 * WBLK, SLOT = GOFF + 8 * WBLK;
constexpr int LEFT = 32768;
constexpr int LNB = 2 * SLOT, LNROW = 1088;
constexpr int SBASE = 3 * SLOT;
constexpr int KROW = 272, KSW = 32 * KROW, PAIR = 2 * KSW;
constexpr int TX = 0, XIMG = 4160, XPAD = 480, CEX = 8736, TY = 13248, KEX0 = 7168, KEX1 = KSW;
constexpr int STO = SBASE + 2 * PAIR, CSO = STO + 1024, SYNC = CSO + 1536, LANDED = SYNC + 16, USED = SYNC + 32, DONE = SYNC + 48, DUMMY = SYNC + 128;
constexpr int LDS_BYTES = 163840;
static_assert(DUMMY + 256 <= LDS_BYTES && XIMG + XPAD + 4096 <= CEX && CEX + 4096 <= TY && TY + 4128 <= PAIR, "LDS map");

constexpr int WS_E = 0, WS_XNF = 4096, WS_HF = 65536, WS_YF = 196608, WS_ST = 32768, WS_CS = 40960;
constexpr int XNP = 8320, WS_XNT = 1 << 19, WS_YT = 3 << 20, WS_H = 5 << 20, WS_END = WS_H + 64 * NU * 1024;
constexpr int CSP = 1408;

__device__ __forceinline__ float u2f(unsigned v) { return __builtin_bit_cast(float, v); }
__device__ __forceinline__ unsigned f2u(float v) { return __builtin_bit_cast(unsigned, v); }
__device__ __forceinline__ bool wave_all(bool p) { return __builtin_amdgcn_ballot_w64(!p) == 0; }
__device__ __forceinline__ unsigned rfl(unsigned v) { return __builtin_amdgcn_readfirstlane(v); }
__device__ __forceinline__ rsrc_t mkr(const void* p, unsigned n) { return __builtin_amdgcn_make_buffer_rsrc((void*)p, 0, n, 0x00020000); }
__device__ __forceinline__ void dma16(unsigned base, int dst, unsigned voff, rsrc_t r, unsigned soff, int off = 0) {
  asm volatile("s_add_u32 m0, %0, %1\n\ts_nop 0\n\tbuffer_load_dwordx4 %2, %3, %4 offen offset:%5 lds"
               ::"s"(base), "n"(dst - off), "v"(voff), "s"(r), "s"(soff), "n"(off) : "scc", "memory");
}
__device__ __forceinline__ void touch(unsigned m0v, unsigned voff, rsrc_t r, unsigned soff) {
  asm volatile("s_mov_b32 m0, %0\n\ts_nop 0\n\tbuffer_load_dword %1, %2, %3 offen lds" ::"s"(m0v), "v"(voff), "s"(r), "s"(soff) : "memory");
}
__device__ __forceinline__ void st16(u32x4 v, rsrc_t r, unsigned voff, unsigned soff) {
  asm volatile("buffer_store_dwordx4 %0, %1, %2, %3 offen sc1\n\ts_nop 1" ::"v"(v), "v"(voff), "s"(r), "s"(soff) : "memory");
}
template <int W> __device__ __forceinline__ void waitv() { asm volatile("s_waitcnt vmcnt(%0)" ::"n"(W) : "memory"); }
__device__ __forceinline__ void waitl0() { asm volatile("" ::: "memory"); __builtin_amdgcn_s_waitcnt(0xc07f); asm volatile("" ::: "memory"); }
__device__ __forceinline__ void fence() { asm volatile("" ::: "memory"); }
__device__ __forceinline__ void bar() { waitl0(); __builtin_amdgcn_s_barrier(); fence(); }
__device__ __forceinline__ unsigned pk2(float a, float b) { f32x2 p = {a, b}; return __builtin_bit_cast(unsigned, __builtin_convertvector(p, bf16x2)); }
__device__ __forceinline__ unsigned pkh(float a, float b) { f32x2 p = {a, b}; return __builtin_bit_cast(unsigned, __builtin_convertvector(p, f16x2)); }
__device__ __forceinline__ float blo(unsigned v) { return u2f(v << 16); }
__device__ __forceinline__ float bhi(unsigned v) { return u2f(v & 0xffff0000u); }
__device__ __forceinline__ float rb(float a) { return blo(pk2(a, a)); }
template <int C, int RM> __device__ __forceinline__ float dppf(float v) {
  return __builtin_bit_cast(float, __builtin_amdgcn_update_dpp(0, __builtin_bit_cast(int, v), C, RM, 0xf, true));
}
__device__ __forceinline__ float lane63(float v) { return __builtin_bit_cast(float, __builtin_amdgcn_readlane(__builtin_bit_cast(int, v), 63)); }
__device__ __forceinline__ float wave_sum(float v) {
  v += dppf<0x111, 0xf>(v); v += dppf<0x112, 0xf>(v); v += dppf<0x114, 0xf>(v); v += dppf<0x118, 0xf>(v);
  v += dppf<0x142, 0xa>(v); v += dppf<0x143, 0xc>(v);
  return lane63(v);
}
template <int C, int RM> __device__ __forceinline__ unsigned dppmax(unsigned v) {
  const unsigned o = __builtin_amdgcn_update_dpp(0, v, C, RM, 0xf, true);
  return v > o ? v : o;
}
__device__ __forceinline__ unsigned wave_max(unsigned v) {
  v = dppmax<0x111, 0xf>(v); v = dppmax<0x112, 0xf>(v); v = dppmax<0x114, 0xf>(v); v = dppmax<0x118, 0xf>(v);
  v = dppmax<0x142, 0xa>(v); v = dppmax<0x143, 0xc>(v);
  return __builtin_amdgcn_readlane(v, 63);
}
__device__ __forceinline__ s16x8 cat(s16x4 a, s16x4 b) { return __builtin_shufflevector(a, b, 0, 1, 2, 3, 4, 5, 6, 7); }
__device__ __forceinline__ s16x4 trd(LDS char* p) { return __builtin_amdgcn_ds_read_tr16_b64_v4i16((LDS s16x4*)p); }
__device__ __forceinline__ f32x4 mfma16(s16x8 a, s16x8 b, f32x4 c) {
  return __builtin_amdgcn_mfma_f32_16x16x32_bf16(__builtin_bit_cast(bf16x8, a), __builtin_bit_cast(bf16x8, b), c, 0, 0, 0);
}
template <int NAP> __device__ __forceinline__ void poll(const char* ws, unsigned off, u64 tag) {
  GLB u64* p = (GLB u64*)(ws + off);
#pragma unroll 1
  for (unsigned spins = 0; spins < SPIN_CAP; ++spins) {
    if (wave_all(__hip_atomic_load(p, RLX_AGENT) == tag)) break;
    __builtin_amdgcn_s_sleep(NAP);
  }
  fence();
}
typedef __attribute__((ext_vector_type(8))) unsigned long long u64x8;
template <int NAP, int N> __device__ __forceinline__ bool watch(const char* p, u64 tag) {
#pragma unroll 1
  for (unsigned spins = 0; spins < 128; ++spins) {
    u64x8 f[2];
    u64 d = 0;
    if constexpr (N == 1) asm volatile("s_load_dwordx16 %0, %1, 0 glc\n\ts_waitcnt lgkmcnt(0)" : "=&s"(f[0]) : "s"(p) : "memory");
    else asm volatile("s_load_dwordx16 %0, %2, 0 glc\n\ts_load_dwordx16 %1, %2, 64 glc\n\ts_waitcnt lgkmcnt(0)" : "=&s"(f[0]), "=&s"(f[1]) : "s"(p) : "memory");
#pragma unroll
    for (int i = 0; i < 8 * N; ++i) { d |= f[i >> 3][i & 7] ^ tag; asm("" : "+s"(d)); }
    if (d == 0) return true;
    __builtin_amdgcn_s_sleep(NAP);
  }
  return false;
}
__device__ __forceinline__ void lds_wait(LDS char* p, unsigned v) {
#pragma unroll 1
  for (unsigned spins = 0; spins < SPIN_CAP; ++spins) { if (rfl(*(volatile LDS unsigned*)p) >= v) break; __builtin_amdgcn_s_sleep(1); }
  fence();
}
__device__ __forceinline__ void setflag(char* ws, unsigned off, u64 tag, int lane) {
  if (lane == 0) __hip_atomic_store((GLB u64*)(ws + off), tag, RLX_AGENT);
}
__device__ __forceinline__ f32x2 fma2(f32x2 a, f32x2 b, f32x2 c) { return __builtin_elementwise_fma(a, b, c); }
__device__ __forceinline__ float one(float v) { asm("" : "+v"(v)); return v; }
__device__ __forceinline__ f32x2 dup(float a) { return f32x2{a, a}; }
struct cmulr { f32x2 z, rot; };
__device__ __forceinline__ cmulr mul_by(f32x2 z) { return {z, f32x2{-z[1], z[0]}}; }
__device__ __forceinline__ f32x2 cmul(f32x2 a, cmulr m) { return fma2(dup(a[0]), m.z, dup(a[1]) * m.rot); }
__device__ __forceinline__ f32x2 cpow2(float s2, float th, float p) {
  const float t = th * p, n = __builtin_rintf(t * 0.15915494f);
  const float fr = __builtin_fmaf(t, 0.15915494f, -n) + t * 6.4206382e-9f;
  const float e = __builtin_amdgcn_exp2f(s2 * p);
  return f32x2{e * __builtin_amdgcn_cosf(fr), e * __builtin_amdgcn_sinf(fr)};
}
__device__ __forceinline__ float exp_f(float v) {
  const float t = v * 1.44269504f, e = __builtin_fmaf(v, 1.925963033e-8f, __builtin_fmaf(v, 1.44269504f, -t));
  const float y = __builtin_amdgcn_exp2f(t);
  return __builtin_fmaf(y, e * 0.69314718f, y);
}

#define STAMP(i)
#define STAMP_OUT(role)

struct G1Frag { s16x8 x[4]; s16x4 w[12]; };
struct CvFrag { s16x4 a[16]; s16x8 b[8]; };

__global__ __launch_bounds__(512, 2) void fused_kernel(const u16* x, const u16* lar, const u16* aim, const u16* cre, const u16* cim, const u16* ldt,
                                                       const u16* dsk, const u16* wg, const u16* wu, const u16* wd, u16* out, char* ws) {
  __shared__ __attribute__((aligned(16))) char lds_raw[LDS_BYTES];
  LDS char* lds = (LDS char*)lds_raw;
  int w = rfl(threadIdx.x >> 6);
  const int lane = threadIdx.x & 63, tid = threadIdx.x & 255;
  const int b = blockIdx.x, xcd = b & 7, r = b >> 3;
  STAMP(0);
  if (w == 7) {
    u64 pc; unsigned l0, l1;
    asm volatile("s_getpc_b64 %0\n.Lk_code_begin:\n\ts_mov_b32 %1, .Lk_code_end0-.Lk_code_begin\n\ts_mov_b32 %2, .Lk_code_end1-.Lk_code_begin" : "=s"(pc), "=s"(l0), "=s"(l1));
    touch(DUMMY, lane * 1024, mkr((const void*)pc, max(l0, l1) - 896), (r & 7) * 128);
  }
  const int row0 = (xcd & 1) * 1024 + (xcd >> 1) * 256 + r * 8;
  const unsigned vt = lane < 16 ? (lane >> 3) * (64 * F * 2) + (lane & 7) * 96 : 0x7f000000u;
  const unsigned so_t = ((w & 3) * 128 + (xcd & 1) * 32 + r) * (F * 2) + (xcd >> 1) * 688;
  asm volatile("" ::"s"(row0), "v"(vt), "s"(so_t));
  STAMP(28);
  asm volatile("" : "+s"(x), "+s"(lar), "+s"(aim), "+s"(cre), "+s"(cim), "+s"(ldt), "+s"(dsk), "+s"(wg), "+s"(wu), "+s"(wd), "+s"(out), "+s"(ws));
  STAMP(24);
  const rsrc_t rx = mkr(x, L * D * 2);
  const int w4 = w & 3, mw = w & 1, nw = w4 >> 1;
  const int cp = xcd * 32 + r, ch = cp * 2 + nw;
  u32x4 xr[2];
  u64 tag;
  if (w < 4) {
#pragma unroll
    for (int i = 0; i < 2; ++i)
      asm volatile("buffer_load_dwordx4 %0, %1, %2, %3 offen" : "=v"(xr[i]) : "v"(lane * 16), "s"(rx), "s"((row0 + 2 * w4 + i) * 1024) : "memory");
  } else {
    const unsigned pi = (ch * NM + mw * 64 + lane) * 2;
    asm volatile("global_load_ushort %0, %6, %8\n\tglobal_load_ushort %1, %7, %9\n\tglobal_load_ushort %2, %7, %10\n\tglobal_load_ushort %3, %7, %11\n\t"
                 "global_load_ushort %4, %7, %12\n\tglobal_load_ushort %5, %6, %13"
                 : "=&v"(xr[0][0]), "=&v"(xr[0][1]), "=&v"(xr[0][2]), "=&v"(xr[0][3]), "=&v"(xr[1][0]), "=&v"(xr[1][1])
                 : "v"(ch * 2), "v"(pi), "s"(ldt), "s"(lar), "s"(aim), "s"(cre), "s"(cim), "s"(dsk) : "memory");
  }
  u64 epoch;
  asm volatile("s_load_dwordx2 %0, %1, 0x0" : "=s"(epoch) : "s"(ws) : "memory");
  __builtin_amdgcn_s_barrier();
  STAMP(25);
  touch(DUMMY, vt, mkr(w >> 2 ? wu : wg, D * F * 2), so_t);
  auto get_tag = [&]() INL {
    unsigned lo, hi;
    asm volatile("s_waitcnt lgkmcnt(0)\n\ts_add_u32 %0, %2, 0x7F4A7C15\n\ts_addc_u32 %1, %3, 0x9E3779B9"
                 : "=&s"(lo), "=s"(hi) : "s"((unsigned)epoch), "s"((unsigned)(epoch >> 32)) : "scc", "memory");
    tag = (u64)hi << 32 | lo;
  };
  asm volatile("" : "+s"(w));
  SB();

  const rsrc_t rws = mkr(ws, WS_END), rwd = mkr(wd, F * D * 2);
  const bool gemm = r < 30;
  const int idx = (xcd >> 1) * 30 + r, nt = idx >> 3, mt = (xcd & 1) * 8 + (idx & 7);
  const int n0 = nt < 14 ? nt * 96 : 1280;
  const int m2 = (xcd & 1) * 16 + (xcd >> 1) * 4 + (r >> 3), n2 = r & 7;
  const int j16 = lane & 15, g = lane >> 4;
  int lv = lane;

  const rsrc_t rw = mkr((gemm ? nw : xcd >> 2) ? wu : wg, D * F * 2);
  unsigned vx[2], vw[3], lx = w4 * 4096, lw = (nw ? UOFF : GOFF) + mw * 2 * WBLK;
  asm volatile("" : "+s"(lx), "+s"(lw));
  auto addr_x = [&]() INL {
#pragma unroll
    for (int i = 0; i < 2; ++i) {
      const int row = i * 8 + (lane >> 3), id = (row >> 1) & 7, f = (id & 4) | ((id & 1) << 1) | ((id >> 1) & 1);
      vx[i] = (mt * 128 + w4 * 32 + row) * 1024 + (((lane & 7) ^ f) << 4);
    }
  };
  auto addr_w = [&]() INL {
    const int kr = (lane * 171) >> 11, c = lane - kr * 12;
    vw[0] = (mw * 32 + kr) * (F * 2) + c * 16 + n0 * 2;
    vw[1] = vw[0] + (c >= 8 ? 6 * F * 2 - 128 : 5 * F * 2 + 64);
    vw[2] = vw[0] + (c >= 4 ? 11 * F * 2 - 64 : 10 * F * 2 + 128);
  };
  auto issue = [&](int s, int i) INL {
    const int slot = (s % 3) * SLOT, j = i - 4;
    if (i < 4) dma16(lx, slot + i * 1024, vx[i & 1], rx, s * 128 + (i >> 1) * 16384);
    else dma16(lw, slot + (j / 3) * WBLK + (j % 3) * 1024, vw[j % 3], rw, s * (64 * F * 2) + (j / 3) * 16 * F * 2);
  };
  const unsigned wl = w4 * 8192, wl6 = (nw ? 0 : 2 * SLOT) + LEFT + mw * 4096;
  auto wdloc = [](int pos) INL { return ((pos / 2 + 2) % 3) * SLOT + (pos & 1) * 4096; };

  auto post = [&](int word, unsigned v) INL { fence(); *(volatile LDS unsigned*)(lds + word + w4 * 4) = v; fence(); };
  auto peek = [&](int word) INL { return *(volatile LDS unsigned*)(lds + word + w4 * 4); };
  auto await = [&](int word, unsigned seen, unsigned v) INL { if (__builtin_expect(rfl(seen) < v, 0)) lds_wait(lds + word + w4 * 4, v); fence(); };

  if (__builtin_expect(w < 4, 1)) {
    auto layernorm = [&](auto&& hook) INL {
      get_tag();
      STAMP(1);
      u32x2 stat;
#pragma unroll
      for (int i = 0; i < 2; ++i) {
        f32x2 v[4];
#pragma unroll
        for (int e = 0; e < 4; ++e) asm volatile("v_lshlrev_b32 %0, 16, %2\n\tv_and_b32 %1, 0xffff0000, %2" : "=&v"(v[e][0]), "=v"(v[e][1]) : "v"(xr[i][e]));
        hook(5 * i);
        const float x0 = u2f(rfl(f2u(v[0][0])));
#pragma unroll
        for (int e = 0; e < 4; ++e) v[e] -= dup(x0);
        const f32x2 s1 = (v[0] + v[1]) + (v[2] + v[3]), s2 = fma2(v[0], v[0], v[1] * v[1]) + fma2(v[2], v[2], v[3] * v[3]);
        float t1 = s1[0] + s1[1], t2 = s2[0] + s2[1];
        asm volatile("" : "+v"(t1), "+v"(t2));
        hook(5 * i + 1);
        const float md = wave_sum(t1) * (1.f / D);
        float var = __builtin_fmaf(-md, md, wave_sum(t2) * (1.f / D));
        asm volatile("" : "+v"(var));
        hook(5 * i + 2);
        const float m = rb(x0 + md), sd = rb(__builtin_amdgcn_sqrtf(rb(rb(__builtin_fmaxf(var, 0.f)) + 1e-5f))), rs = __builtin_amdgcn_rcpf(sd);
        float c = (x0 - m) * rs;
        asm volatile("" : "+v"(c));
        hook(5 * i + 3);
        u32x4 o;
#pragma unroll
        for (int e = 0; e < 4; ++e) { const f32x2 n = fma2(v[e], dup(rs), dup(c)); o[e] = pk2(n[0], n[1]); }
        *(LDS u32x4*)(lds + LNB + (2 * w4 + i) * LNROW + lane * 16) = o;
        hook(5 * i + 4);
        stat[i] = pk2(m, sd);
      }
      __builtin_amdgcn_raw_buffer_store_b64(stat, rws, lane ? 0x7f000000u : 0u, WS_ST + (row0 + 2 * w4) * 4, SC1);
      STAMP(21);
      bar();
      STAMP(22);
#pragma unroll
      for (int i = 0; i < 2; ++i) {
        LDS char* p = lds + LNB + (j16 >> 2) * LNROW + (i * 256 + w4 * 64 + g * 16 + (j16 & 3) * 4) * 2;
        st16(__builtin_bit_cast(u32x4, cat(trd(p), trd(p + 4 * LNROW))), rws, tid * 16, WS_XNT + (row0 >> 3) * XNP + i * 4096);
      }
    };
    unsigned ad[3][4], vh[4], vpoll, kh, kh10, a_xt;
    u32x2 xt[4];
    auto rdxt = [&](int u) INL { xt[u] = *(const LDS u32x2*)(lds + SBASE + CEX - 32 * 1024 + (a_xt ^ (u * 32))); };
    s16x8 wf[11][4];
    auto wd_addr = [&](int i) INL {
      const int j16 = lv & 15, g = lv >> 4, q = j16 >> 2, f = ((q >> 1) & 1) + 2 * (g & 1);
#pragma unroll
      for (int u = 0; u < 4; ++u) {
        ad[i][u] = (i == 0 ? wl : i == 1 ? wl + 2 * SLOT : wl6) + (8 * g + q) * 128 + ((u ^ f) << 5) + (j16 & 3) * 8;
        asm volatile("" : "+v"(ad[i][u]));
      }
    };
    auto rdwd = [&](int s, int u) INL {
      const int pos = s % 7;
      LDS char* p = lds + (pos == 6 ? ad[2][u] : pos < 2 ? ad[1][u] + pos * 4096 : ad[0][u] + wdloc(pos));
      wf[s][u] = cat(trd(p), trd(p + 512));
    };
    auto h_addr = [&]() INL {
      const int j16 = lv & 15, g = lv >> 4;
      wd_addr(0); wd_addr(1); wd_addr(2);
#pragma unroll
      for (int m = 0; m < 4; ++m) { const int mm = (m + w4) & 3; vh[m] = (mm >> 1) * (NU * 1024) + (g >> 1) * 1024 + (32 * (g & 1) + 16 * (mm & 1) + j16) * 16; }
      vpoll = WS_HF + (m2 * 4 + w4) * 128;
      a_xt = 32 * 1024 + nw * PAIR + mw * 2048 + j16 * 128 + (((g >> 1) ^ (j16 >> 1)) << 4) + (g & 1) * 8;
      asm volatile("" : "+v"(a_xt), "+v"(vh[0]), "+v"(vh[1]), "+v"(vh[2]), "+v"(vh[3]));
      kh = WS_H + m2 * 2 * NU * 1024 + w4 * (11 * 2048);
      kh10 = w4 == 3 ? 0x7f000000u : kh + 10 * 2048;
      asm volatile("" : "+s"(kh), "+s"(kh10), "+s"(vpoll));
    };

    if (gemm) {
      addr_x();
      addr_w();
      asm volatile("" : "+v"(vx[0]), "+v"(vx[1]), "+v"(vw[0]), "+v"(vw[1]), "+v"(vw[2]));
      unsigned ax[4], aw[2];
      {
        const int id = (j16 >> 1) & 7, f = (id & 4) | ((id & 1) << 1) | ((id >> 1) & 1), cg = 2 * (g & 1) + (g >> 1);
#pragma unroll
        for (int s = 0; s < 2; ++s) { ax[s] = (mw * 64 + j16) * 128 + (((4 * s + cg) ^ f) << 4); ax[s + 2] = ax[s] + 2 * SLOT; }
        aw[0] = GOFF + (g & 1) * WBLK + (8 * (g >> 1) + (j16 >> 2)) * 192 + nw * 96 + (j16 & 3) * 8;
        aw[1] = aw[0] + 2 * SLOT;
      }
      unsigned a_st, a_cs, so_h[3], vflag;
      auto late_addr = [&]() INL {
        asm volatile("" : "+v"(lv));
        a_st = STO + (mw * 64 + (lv & 15)) * 8; a_cs = CSO + (nw * 48 + (lv & 15)) * 16;
#pragma unroll
        for (int u = 0; u < 3; ++u) {
          const int unit = (n0 >> 4) + nw * 3 + u;
          so_h[u] = nt < 14 || unit >= 84 ? WS_H + ((mt * 4 + mw * 2) * NU + unit) * 1024 : 0x7f000000u;
        }
        const unsigned fi = (nt < 14 ? nt * 2 + nw : nw ? 28 : 63) - 7 * min(lv, 5);
        vflag = fi < 8 && lv < 4 ? WS_HF + (mt * 2 + mw) * 512 + lv * 128 + fi * 8 : 0x7f000000u;
        asm volatile("" : "+v"(a_st), "+v"(a_cs), "+v"(vflag));
        asm volatile("" : "+s"(so_h[0]), "+s"(so_h[1]), "+s"(so_h[2]));
        h_addr();
      };
      asm volatile("" : "+v"(ax[0]), "+v"(ax[1]), "+v"(ax[2]), "+v"(ax[3]), "+v"(aw[0]), "+v"(aw[1]));
      SB();
      STAMP(26);
#pragma unroll
      for (int i = 0; i < 10; ++i) issue(0, i);
      STAMP(20);
      waitv<11>();
      layernorm([&](int k) INL { issue(1, k); });
#pragma unroll
      for (int i = 10 - D2F; i < 10; ++i) issue(2, i);
      STAMP(23);
      f32x4 acc[4][6] = {};
      G1Frag fr[2];
      auto half = [&](G1Frag& c, bool domm, G1Frag& n, bool dord, int s, int jj) INL {
#pragma unroll
        for (int t = 0; t < 24; ++t) {
          if (domm) acc[t & 3][t >> 2] = mfma16(cat(c.w[t >> 2 << 1], c.w[(t >> 2 << 1) + 1]), c.x[t & 3], acc[t & 3][t >> 2]);
          SB();
          const int hi = s % 3 == 2, so = s % 3 == 1 ? SLOT : 0;
#pragma unroll
          for (int k = t; k < min(t + 1, 16); ++k) {
            const int u = (k - 4) >> 1;
            if (k < 4) n.x[k] = *(const LDS s16x8*)(lds + so + ax[jj + 2 * hi] + k * 2048);
            else n.w[k - 4] = trd(lds + aw[hi] + so + jj * 2 * WBLK + (u / 3) * 4 * WBLK + (u % 3) * 32 + (k & 1) * 768);
          }
          SB();
        }
      };
#pragma unroll
      for (int s = 0; s < 8; ++s) {
        if (s == 2) STAMP(27);
        if (s == 0) waitv<13 + D2F>(); else if (s == 1) waitv<D2F>(); else if (s == 2) waitv<0>();
        if (s == 3) STAMP(29);
        if (s == 0) STAMP(19);
        bar();
        STAMP(2 + s);
        half(fr[1], s > 0, fr[0], true, s, 0);
        half(fr[0], true, fr[1], true, s, 1);
        if (s == 0) { late_addr(); SB(); }
      }
      bar();
      STAMP(10);
      u32x2 st[4], bm[4]; s16x8 cs[3];
      float kg[4];
#pragma unroll
      for (int t = 0; t < 24; ++t) {
        acc[t & 3][t >> 2] = mfma16(cat(fr[1].w[t >> 2 << 1], fr[1].w[(t >> 2 << 1) + 1]), fr[1].x[t & 3], acc[t & 3][t >> 2]);
        SB();
        if (t < 4) st[t] = *(const LDS u32x2*)(lds + a_st + t * 128);
        else if (t < 7) cs[t - 4] = *(const LDS s16x8*)(lds + a_cs + (t - 4) * 256);
        else if (t < 11) rdxt(t - 7);
        else if (t >= 12 && t < 16) { bm[t - 12] = u32x2{g ? 0 : st[t - 12][1], 0}; asm volatile("" : "+v"(bm[t - 12])); }
        else if (t >= 16 && t < 20) { kg[t - 16] = -1.44269504f * __builtin_amdgcn_rcpf(u2f(st[t - 16][0])); asm volatile("" : "+v"(kg[t - 16])); }
        SB();
      }
#pragma unroll
      for (int t = 0; t < 24; ++t) {
        const int m = (t & 1) + 2 * (t / 12), u = (t >> 2) % 3, up = (t >> 1) & 1;
        const s16x4 c = up ? __builtin_shufflevector(cs[u], cs[u], 4, 5, 6, 7) : __builtin_shufflevector(cs[u], cs[u], 0, 1, 2, 3);
        acc[m][u + 3 * up] = __builtin_amdgcn_mfma_f32_16x16x16bf16_1k(c, __builtin_bit_cast(s16x4, bm[m]), acc[m][u + 3 * up], 0, 0, 0);
        SB();
      }
      STAMP(11);
      unsigned seen;
#pragma unroll
      for (int mp = 0; mp < 2; ++mp) {
#pragma unroll
        for (int u = 0; u < 3; ++u) {
          unsigned o[2][2];
          const int q = mp * 3 + u;
          if (q == B9Q) bar();
          if (q == RD0 || q == RD1) {
            await(LANDED, seen, q == RD0 ? 1 : 2);
#pragma unroll
            for (int k = 0; k < 8; ++k) rdwd((q == RD0 ? 0 : 2) + (k >> 2), k & 3);
            post(USED, q == RD0 ? 1 : 2);
          }
          if (q == RD0 - 1 || q == RD1 - 1) { seen = peek(LANDED); SB(); }
#pragma unroll
          for (int mi = 0; mi < 2; ++mi) {
            const int m = mp * 2 + mi;
            const float sd = u2f(st[m][0]);
#pragma unroll
            for (int e = 0; e < 2; ++e) {
              f32x2 hv;
              if (q < PKQ) {
                const f32x2 gg = f32x2{acc[m][u][2 * e], acc[m][u][2 * e + 1]} * dup(kg[m]);
                const f32x2 dn = fma2(f32x2{__builtin_amdgcn_exp2f(gg[0]), __builtin_amdgcn_exp2f(gg[1])}, dup(sd), dup(sd));
                hv = f32x2{acc[m][u + 3][2 * e], acc[m][u + 3][2 * e + 1]} * f32x2{__builtin_amdgcn_rcpf(dn[0]), __builtin_amdgcn_rcpf(dn[1])};
              } else {
#pragma unroll
                for (int k = 0; k < 2; ++k) {
                  const float ex = __builtin_amdgcn_exp2f(one(acc[m][u][2 * e + k] * kg[m]));
                  hv[k] = one(acc[m][u + 3][2 * e + k] * __builtin_amdgcn_rcpf(one(__builtin_fmaf(ex, sd, sd))));
                }
              }
              o[mi][e] = pk2(hv[0], hv[1]);
            }
          }
          const auto r0 = __builtin_amdgcn_permlane16_swap(o[0][0], o[1][0], false, false);
          const auto r1 = __builtin_amdgcn_permlane16_swap(o[0][1], o[1][1], false, false);
          const u32x4 ov = {r0[0], r1[0], r0[1], r1[1]};
          st16(ov, rws, lane * 16 + mp * (NU * 1024), so_h[u]);
          SB();
        }
      }
      STAMP(12);
      if (B9Q == 6) bar();
      waitv<0>();
      __builtin_amdgcn_raw_buffer_store_b64(u32x2{(unsigned)tag, (unsigned)(tag >> 32)}, rws, vflag, 0, SC1);
    } else {
      const int cj = (xcd & 3) * 2 + (r - 30), kg = (tid * 745) >> 14, cc = tid - kg * 22, col = cj * 176 + cc * 8;
      const unsigned vo = (kg < 11 && col < F) ? kg * (F * 2) + col * 2 : 0x7f000000u;
      u32x4 cv[2][8];
      auto ldcs = [&](int sl) INL {
#pragma unroll
        for (int i = 0; i < 8; ++i) cv[sl & 1][i] = __builtin_amdgcn_raw_buffer_load_b128(rw, vo + (sl * 8 + i) * (11 * F * 2), 0, 0);
      };
      ldcs(0);
      waitv<9>();
      layernorm([](int) INL {});
      f32x2 a[4] = {};
#pragma unroll
      for (int sl = 0; sl < 6; ++sl) {
        if (sl == 1) waitv<0>();
        if (sl < 5) ldcs(sl + 1);
        if (sl < 4) bar();
#pragma unroll
        for (int i = 0; i < 8; ++i)
#pragma unroll
          for (int e = 0; e < 4; ++e) { const unsigned q = cv[sl & 1][i][e]; a[e] += f32x2{blo(q), bhi(q)}; }
        asm volatile("" : "+v"(a[0]), "+v"(a[1]), "+v"(a[2]), "+v"(a[3]));
      }
      if (kg < 11) {
        *(LDS f32x4*)(lds + kg * 704 + cc * 32) = f32x4{a[0][0], a[0][1], a[1][0], a[1][1]};
        *(LDS f32x4*)(lds + kg * 704 + cc * 32 + 16) = f32x4{a[2][0], a[2][1], a[3][0], a[3][1]};
      }
      bar();
      if (tid < 176) {
        float s = 0.f;
#pragma unroll
        for (int k = 0; k < 11; ++k) s += *(const LDS float*)(lds + k * 704 + tid * 4);
        __builtin_amdgcn_raw_buffer_store_b64(u32x2{pk2(-s, rb(s) - s), (unsigned)tag}, rws, tid * 8, WS_CS + ((xcd >> 2) * CSP + cj * 176) * 8, SC1);
      }
      h_addr();
      bar(); bar(); bar(); bar();
#pragma unroll
      for (int u = 0; u < 4; ++u) rdxt(u);
      await(LANDED, 0, 2);
#pragma unroll
      for (int k = 0; k < 16; ++k) rdwd(k >> 2, k & 3);
      post(USED, 2);
      bar();
    }

    STAMP(13);
    f32x4 a2[4][4] = {};
#pragma unroll
    for (int u = 0; u < 4; ++u) a2[0][u] = f32x4{blo(xt[u][0]), bhi(xt[u][0]), blo(xt[u][1]), bhi(xt[u][1])};
    unsigned a_p[3], a_y = SBASE + (4 * g + (j16 >> 2)) * 160 + w4 * 32 + (j16 & 3) * 8, a_o[2], v_o = (lane >> 3) * 1024 + (lane & 7) * 16;
#pragma unroll
    for (int m = 1; m < 4; ++m) a_p[m - 1] = wdloc(m - 1) + ((w4 - m) & 3) * 8192 + lane * 16;
    a_o[0] = wdloc(4) + wl + j16 * 144 + g * 8;
    a_o[1] = wdloc(4) + wl + (lane >> 3) * 144 + (lane & 7) * 16;
    asm volatile("" : "+v"(a2[0][0]), "+v"(a2[0][1]), "+v"(a2[0][2]), "+v"(a2[0][3]));
    asm volatile("" : "+v"(a_p[0]), "+v"(a_p[1]), "+v"(a_p[2]), "+v"(a_y), "+v"(a_o[0]), "+v"(a_o[1]), "+v"(v_o));
    if (HSLEEP) __builtin_amdgcn_s_sleep(HSLEEP);
    if (!watch<HNAP, 1>(ws + vpoll, tag)) poll<HNAP>(ws, vpoll + (lane & 7) * 8, tag);
    STAMP(14);
    auto fs = [](int n) INL { return n < 32 ? n >> 2 : 8 + (n - 32) % 3; };
    auto fm = [](int n) INL { return ((n < 32 ? n : (n - 32) / 3) + 1) & 3; };
    s16x8 hf[44];
    auto ldh = [&](int n) INL {
      hf[n] = __builtin_bit_cast(s16x8, __builtin_amdgcn_raw_buffer_load_b128(rws, vh[fm(n)] + (fs(n) & 1) * 2048, fs(n) == 10 ? kh10 : kh + (fs(n) >> 1) * 4096, SC1));
    };
#pragma unroll
    for (int n = 0; n < HF; ++n) { ldh(n); SB(); }
    unsigned landed;
    u32x4 dn[2];
    SB();
#pragma unroll
    for (int n = 0; n < 44; ++n) {
      if (n == 2 || n == 22) landed = peek(LANDED);
      if (n == 4 || n == 24) await(LANDED, landed, n == 4 ? 3 : 4);
#pragma unroll
      for (int u = 0; u < 4; ++u) {
        a2[fm(n)][u] = mfma16(wf[fs(n)][u], hf[n], a2[fm(n)][u]);
        SB();
        if (u == 1 && n >= 4 && n < 32) rdwd((n >> 2) + 3, n & 3);
        if (u == 3 && n + HF < 44) ldh(n + HF);
        if (n >= 35 && (n - 35) % 3 == 0) *(LDS f32x4*)(lds + wdloc((n - 35) / 3) + wl + u * 1024 + lane * 16) = a2[(n - 35) / 3 + 1][u];
        SB();
      }
      if (n == 41) post(DONE, 1);
      if (n == 42) { dn[0] = *(volatile LDS u32x4*)(lds + DONE); dn[1] = *(volatile LDS u32x4*)(lds + DONE + 16); SB(); }
      if (n == 3) STAMP(15);
    }
    STAMP(16);
    auto all4 = [](u32x4 d) INL { return rfl(d[0] & d[1] & d[2] & d[3]); };
#pragma unroll 1
    for (unsigned spins = 0; spins < SPIN_CAP && !all4(dn[0]); ++spins) {
      dn[0] = *(volatile LDS u32x4*)(lds + DONE);
      dn[1] = *(volatile LDS u32x4*)(lds + DONE + 16);
    }
    fence();
    STAMP(31);
    f32x4 pp[4][3];
    u32x2 y[4];
    auto rdy = [&]() INL {
#pragma unroll
      for (int u = 0; u < 4; ++u) y[u] = __builtin_bit_cast(u32x2, trd(lds + a_y + (u >> 1) * PAIR + (u & 1) * 4096));
    };
#pragma unroll
    for (int u = 0; u < 4; ++u)
#pragma unroll
      for (int m = 0; m < 3; ++m) pp[u][m] = *(const LDS f32x4*)(lds + a_p[m] + u * 1024);
    rdy();
    SB();
#pragma unroll
    for (int u = 0; u < 4; ++u) a2[0][u] += (pp[u][0] + pp[u][1]) + pp[u][2];
    asm volatile("" : "+v"(a2[0][0]), "+v"(a2[0][1]), "+v"(a2[0][2]), "+v"(a2[0][3]));
    if (__builtin_expect(!all4(dn[1]), 0)) {
#pragma unroll 1
      for (unsigned spins = 0; spins < SPIN_CAP && !all4(*(volatile LDS u32x4*)(lds + DONE + 16)); ++spins) {}
      fence();
      rdy();
    }
    STAMP(17);
#pragma unroll
    for (int u = 0; u < 4; ++u) {
      const f32x4 s = a2[0][u] + f32x4{blo(y[u][0]), bhi(y[u][0]), blo(y[u][1]), bhi(y[u][1])};
      *(LDS u32x2*)(lds + a_o[0] + u * 32) = u32x2{pk2(s[0], s[1]), pk2(s[2], s[3])};
    }
    fence();
    const u32x4 o0 = *(const LDS u32x4*)(lds + a_o[1]), o1 = *(const LDS u32x4*)(lds + a_o[1] + 8 * 144);
    st16(o0, mkr(out, L * D * 2), v_o, (m2 * 64 + w4 * 16) * 1024 + n2 * 128);
    st16(o1, mkr(out, L * D * 2), v_o, (m2 * 64 + w4 * 16 + 8) * 1024 + n2 * 128);
    STAMP(18);
    STAMP_OUT(0);
    asm volatile(".Lk_code_end0:");
  } else {
    const int cl = nw, hh = mw;
    STAMP(21);
    addr_x();
    addr_w();
    asm volatile("" : "+v"(vx[0]), "+v"(vx[1]), "+v"(vw[0]), "+v"(vw[1]), "+v"(vw[2]));
    auto part = [&](int s, int i0, int i1) INL {
      if (__builtin_expect(gemm, 1)) {
#pragma unroll
        for (int i = i0; i < i1; ++i) issue(s, i);
      }
    };
    auto stage = [&](int s) INL { part(s, 0, 10); };
    const unsigned reg = SBASE + cl * PAIR;
    unsigned vd[2], vcs, vxi, vzero, vimg, vxf, vtw[2], vtab[2], vco, vxt[2];
    {
      const unsigned rw8 = (lane >> 3) * 1024 + n2 * 128, c16 = (lane & 7) * 16;
      vd[0] = rw8 + (c16 ^ ((lane & 16) * 2)) + w4 * (352 * 1024);
      vd[1] = vd[0] ^ 64;
      vxt[0] = rw8 + (c16 ^ (lane & 48)) + (m2 * 64 + w4 * 16) * 1024;
      vxt[1] = vxt[0] ^ 64;
      vcs = (min(tid, 191) + n0 + (tid < 96 ? 0 : CSP - 96)) * 8;
      vco = tid * 16 + (tid < 96 ? CSO : CSO - 96 * 16 + 8);
      vxi = (tid >> 1) * XNP + (tid & 1) * 16 + cp * 32;
      vimg = (tid & 1) * PAIR + (tid >> 1) * 16 + SBASE + XIMG;
      vzero = lane * 16 + (lane < 2 ? (hh ? SYNC : reg + TY + 4096) : (hh ? SYNC : reg + TX + 4064));
      vtab[0] = (lane < 32 ? reg + (hh ? 127 : 255) * 16 : reg + (hh ? 223 : 255) * 16) - lane * 16;
      vtab[1] = vtab[0] - (hh ? 32 : 192) * 16;
      vxf = tid ? 0x7f000000u : WS_XNF + b * 8;
      vtw[0] = lane < 11 ? lane * 128 : 0xfe00000u;
      vtw[1] = vtw[0] * 8;
    }
    asm volatile("" : "+v"(vd[0]), "+v"(vd[1]), "+v"(vcs), "+v"(vco), "+v"(vxt[0]), "+v"(vxt[1]), "+v"(vxi), "+v"(vimg), "+v"(vzero), "+v"(vxf));
    asm volatile("" : "+v"(vtw[0]), "+v"(vtw[1]), "+v"(vtab[0]), "+v"(vtab[1]));
    auto issue_wd = [&](int s, int p) INL {
      const int pos = s % 7;
      dma16(pos == 6 ? wl6 : wl, (pos == 6 ? 0 : wdloc(pos)) + p * 1024, vd[p & 1], rwd, s * 32768 + p * 8192);
    };
    SPRIO(1);
    STAMP(22);
    waitv<1>();
    get_tag();
    STAMP(1);
    u32x2 tg = {(unsigned)tag, (unsigned)(tag >> 32)};
    asm volatile("" : "+v"(tg));
    float prm[6];
#pragma unroll
    for (int i = 0; i < 6; ++i) asm volatile("v_lshlrev_b32 %0, 16, %1" : "=v"(prm[i]) : "v"(xr[i >> 2][i & 3]));
    const float p_ldt = prm[0], p_lar = prm[1], p_aim = prm[2], p_cr = prm[3], p_ci = prm[4], skip = one(lane ? 0.f : prm[5]);
    const float dt = rb(exp_f(p_ldt));
    const float s2 = -rb(exp_f(p_lar)) * dt * 1.44269504f, th = p_aim * dt;
    const unsigned eb = min(max(wave_max(max(f2u(p_cr) << 1, f2u(p_ci) << 1)) >> 24, 1u), 253u);
    const float sc = u2f((254u - eb) << 23), ksc = 2.f * dt * u2f(eb << 23);
    f32x2 z1 = cpow2(s2, th, 1.f), z8c = cpow2(s2, -th, 8.f), zb = cpow2(s2, th, 256.f);
    asm volatile("" : "+v"(z1), "+v"(z8c), "+v"(zb));
    const cmulr z256 = mul_by(zb);
    STAMP(2);
    LDS char* pr = lds + SBASE + cl * PAIR;
    LDS char* ks = pr + hh * KSW;
    auto put = [&](int row, f32x2 v) INL { *(LDS unsigned*)(ks + row * KROW + lane * 4) = pkh(v[0], v[1]); };
    auto putu = [&](int reg, int h, f32x2 v) INL { put((reg & 3) + 4 * h + 8 * (reg >> 2), v); };
    f16x8 vf[8];
    {
      f32x2 v[4];
      const cmulr m1 = mul_by(z8c);
      v[0] = f32x2{1.f, 0.f}; v[1] = z8c; v[2] = cmul(z8c, m1); v[3] = cmul(v[2], m1);
      const cmulr m4 = mul_by(cmul(v[3], m1));
#pragma unroll
      for (int k = 0; k < 32; ++k) {
        if (k == 4) v[0] = m4.z; else if (k > 4) v[k & 3] = cmul(v[k & 3], m4);
        put(k, v[k & 3]);
      }
      fence();
#pragma unroll
      for (int s = 0; s < 8; ++s) vf[s] = *(const LDS f16x8*)(ks + (lane & 31) * KROW + s * 32 + (lane >> 5) * 16);
      fence();
    }
    STAMP(3);
    {
      f32x2 e[5];
      const cmulr m1 = mul_by(z1), m2_ = mul_by(cmul(z1, m1));
      e[0] = f32x2{p_cr, p_ci} * dup(sc);
      putu(0, 0, e[0]);
#pragma unroll
      for (int k = 0; k < 4; ++k) { putu(2 * k + 1, 0, cmul(e[k], m1)); e[k + 1] = cmul(e[k], m2_); putu(2 * k + 2, 0, e[k + 1]); }
#pragma unroll
      for (int k = 0; k < 5; ++k) e[k] = cmul(e[k], z256);
      putu(0, 1, e[0]); putu(2, 1, e[1]); putu(3, 1, e[2]); putu(4, 1, e[2]); putu(5, 1, e[2]); putu(6, 1, e[3]); putu(7, 1, e[4]); putu(8, 1, e[4]);
#pragma unroll
      for (int k = 0; k < 6; ++k) {
        const int reg = k < 2 ? 9 + 2 * k : k < 5 ? 5 + 2 * k : 13, h = k < 2 || k == 5;
        e[0] = cmul(e[0], z256); putu(reg, h, e[0]);
        e[4] = cmul(e[4], z256); putu(reg + 1, h, e[4]);
        if (k < 2) { e[2] = cmul(e[2], z256); putu(k ? 1 : 15, 1, e[2]); }
        if (TWP == 0 && k == 5) touch(DUMMY, vtw[0], rwd, (b * 43 + w4 * 11) * 128);
      }
      fence();
    }
    STAMP(4);
    bar();
    f32x16 kd = {};
    {
      f16x8 uf[8];
#pragma unroll
      for (int s = 0; s < 8; ++s) uf[s] = *(const LDS f16x8*)(ks + (lane & 31) * KROW + s * 32 + (lane >> 5) * 16);
      SB();
#pragma unroll
      for (int s = 0; s < 8; ++s) kd = __builtin_amdgcn_mfma_f32_32x32x16_f16(uf[s], vf[s], kd, 0, 0, 0);
    }
    kd *= ksc;
    const bool h = lane >> 5, odd = lane & 1;
    if (hh) {
      *(LDS f32x4*)(pr + KEX1 + lane * 16) = f32x4{kd[0], kd[1], kd[2], kd[3]};
      *(LDS f32x4*)(pr + KEX1 + 1024 + lane * 16) = f32x4{kd[4], kd[5], kd[6], kd[7]};
      *(LDS f32x4*)(pr + KEX1 + 2048 + lane * 16) = f32x4{kd[8], kd[13], kd[14], kd[15]};
    } else {
      *(LDS f32x4*)(pr + KEX0 + lane * 16) = f32x4{kd[9], kd[10], kd[11], kd[12]};
      *(LDS f32x2*)(pr + KEX0 + 1024 + lane * 8) = f32x2{kd[15], kd[1]};
    }
    STAMP(5);
    SPRIO(0);
    bar();
    STAMP(10);
    {
      u32x4 tb[5];
      auto tail = [&](float a, float c, float a3, float a4, float a5, int k) INL {
        const unsigned aa = pk2(a, a), cc = pk2(c, c);
        tb[k] = u32x4{cc, pk2(a5, a4), pk2(a3, a), aa};
        tb[k + 1] = u32x4{cc, pk2(c, a5), pk2(a4, a3), aa};
      };
      if (hh) {
        const f32x4 p = *(const LDS f32x4*)(pr + KEX0 + lane * 16);
        const f32x2 q = *(const LDS f32x2*)(pr + KEX0 + 1024 + lane * 8);
#pragma unroll
        for (int k = 0; k < 2; ++k) {
          const float a = one(kd[9 + 2 * k] + p[2 * k]), c = one(kd[10 + 2 * k] + p[2 * k + 1]), m = one(kd[k ? 1 : 15] + q[k]);
          tail(a, c, h ? m : a, h ? m : odd ? c : a, h ? m : c, 2 * k);
        }
        tb[4][0] = 0;
      } else {
        const f32x4 p0 = *(const LDS f32x4*)(pr + KEX1 + lane * 16), p1 = *(const LDS f32x4*)(pr + KEX1 + 1024 + lane * 16);
        const f32x4 p2 = *(const LDS f32x4*)(pr + KEX1 + 2048 + lane * 16);
        float t[9];
#pragma unroll
        for (int k = 0; k < 9; ++k) t[k] = one(kd[k] + (k < 4 ? p0[k] : k < 8 ? p1[k - 4] : p2[0]));
        t[0] += skip;
        t[1] = h ? t[0] : t[1];
        tb[0] = u32x4{pk2(t[7], t[6]), pk2(t[5], t[4]), pk2(t[3], t[2]), pk2(t[1], t[0])};
        tb[1] = u32x4{pk2(t[8], t[7]), pk2(t[6], t[5]), pk2(t[4], t[3]), pk2(t[2], t[1])};
        const float a = one(kd[13] + p2[1]), c = one(kd[14] + p2[2]);
        tail(a, c, a, odd ? c : a, c, 2);
        tb[4][0] = lane ? 0 : tb[0][3] >> 16;
      }
      tb[4][1] = tb[4][2] = tb[4][3] = 0;
      asm volatile("" : "+v"(tb[0]), "+v"(tb[1]), "+v"(tb[2]), "+v"(tb[3]), "+v"(tb[4]));
      STAMP(19);
#pragma unroll
      for (int k = 0; k < 4; ++k) *(LDS u32x4*)(lds + vtab[k >> 1] + (k & 1 ? TY : TX)) = tb[k];
      if (lane < (hh ? 5 : 36)) *(LDS u32x4*)(lds + vzero) = tb[4];
      part(2, 0, 10 - D2F);
    }
    if (TWP == 2) touch(DUMMY, vtw[0], rwd, (b * 43 + w4 * 11) * 128);
    STAMP(6);
    bar();
    __builtin_amdgcn_raw_buffer_store_b64(tg, rws, vxf, 0, SC1);
    stage(3);
    if (TWP == 1) touch(DUMMY, vtw[0], rwd, (b * 43 + w4 * 11) * 128);
    waitv<10 + (TWP == 1)>();
    bar();
    stage(4);
    waitv<10>();
    bar();
    stage(5);
    waitv<10>();
    bar();
    stage(6);
    if (TW3) touch(DUMMY, vtw[1], rwd, (r * 43 + w4 * 11) * 1024 + ((384u - (unsigned)(u64)wd) & 896u));
    waitv<10 + (TW3 != 0)>();
    bar();
    {
      u64 fl;
      asm volatile("buffer_load_dwordx2 %0, %1, %2, %3 offen sc1" : "=v"(fl) : "v"(tid * 8), "s"(rws), "s"(WS_XNF) : "memory");
      stage(7);
      if (gemm) waitv<10>(); else waitv<0>();
      asm volatile("" : "+v"(fl));
      if (__builtin_expect(!wave_all(fl == tag), 0)) poll<4>(ws, WS_XNF + tid * 8, tag);
    }
    STAMP(7);
    bar();
#pragma unroll
    for (int i = 0; i < 2; ++i) dma16(cl * PAIR + hh * 2048, SBASE + CEX + i * 1024, vxt[i], rx, i * 8192);
    unsigned gst; u32x2 csg; u32x4 xi[2];
    asm volatile("buffer_load_dword %0, %1, %2, %3 offen sc1" : "=v"(gst) : "v"((tid & 127) * 4), "s"(rws), "s"(WS_ST + (mt & 15) * 512) : "memory");
    asm volatile("buffer_load_dwordx2 %0, %1, %2, %3 offen sc1" : "=v"(csg) : "v"(vcs), "s"(rws), "s"(WS_CS) : "memory");
#pragma unroll
    for (int i = 0; i < 2; ++i)
      asm volatile("buffer_load_dwordx4 %0, %1, %2, %3 offen sc1" : "=v"(xi[i]) : "v"(vxi), "s"(rws), "s"(WS_XNT + i * (128 * XNP)) : "memory");
#pragma unroll
    for (int i = 0; i < 4; ++i) issue_wd(0, i);
    waitv<10>();
    STAMP(8);
    bar();
    waitv<6>();
    asm volatile("" : "+v"(gst), "+v"(csg));
    if (tid < 128) *(LDS u32x2*)(lds + STO + tid * 8) = u32x2{gst & 0xffff0000u, __builtin_amdgcn_perm(gst, gst, 0x01000100)};
    if (__builtin_expect(gemm && !wave_all(csg[1] == (unsigned)tag), 0)) {
#pragma unroll 1
      for (unsigned spins = 0; spins < SPIN_CAP && !wave_all(csg[1] == (unsigned)tag); ++spins) {
        __builtin_amdgcn_s_sleep(2);
        asm volatile("buffer_load_dwordx2 %0, %1, %2, %3 offen sc1\n\ts_waitcnt vmcnt(0)" : "=&v"(csg) : "v"(vcs), "s"(rws), "s"(WS_CS) : "memory");
      }
    }
    if (tid < 192) *(LDS u32x2*)(lds + vco) = u32x2{csg[0], csg[0]};
#pragma unroll
    for (int i = 0; i < 4; ++i) issue_wd(1, i);
    STAMP(9);
    bar();
#pragma unroll
    for (int i = 8; i < 16; ++i) issue_wd(i >> 2, i & 3);
    waitv<8>();
    post(LANDED, 1);
    asm volatile("" : "+v"(xi[0]), "+v"(xi[1]));
#pragma unroll
    for (int i = 0; i < 2; ++i) *(LDS u32x4*)(lds + vimg + XPAD + i * 2048) = xi[i];
#pragma unroll
    for (int i = 16; i < 28; ++i) issue_wd(i >> 2, i & 3);
    unsigned pa[4], vex[2], vyf, vys;
    asm volatile("" : "+v"(lv));
    {
      const int j16 = lv & 15, g = lv >> 4;
#pragma unroll
      for (int i = 0; i < 4; ++i) pa[i] = reg + (j16 & 1 ? TX : TY) + (847 - 256 * i - hh * 64 - ((15 + j16 - 8 * g) >> 1)) * 4;
      vex[0] = (hh ? reg + CEX : SLOT + LEFT + cl * 4096) + lv * 16;
      vex[1] = (hh ? SLOT + LEFT + cl * 4096 : reg + CEX) + lv * 16;
      vyf = lv == 0 ? WS_YF + (hh * 256 + cp) * 16 + cl * 8 : lv == 1 && b == 0 && w4 == 0 ? WS_E : 0x7f000000u;
      vys = ch * 4096 + hh * 512 + (g & 1) * (hh ? 512 : 1536) + (j16 >> 2) * 128 + (g >> 1) * 64 + (j16 & 3) * 16;
    }
    asm volatile("" : "+v"(pa[0]), "+v"(pa[1]), "+v"(pa[2]), "+v"(pa[3]), "+v"(vex[0]), "+v"(vex[1]), "+v"(vyf), "+v"(vys));
    waitv<12>();
    post(LANDED, 2);
    if (NPRE > 12) await(USED, 0, 1);
#pragma unroll
    for (int i = 28; i < 16 + NPRE; ++i) issue_wd(i >> 2, i & 3);
    LDS char* pb = pr + XIMG + XPAD + (16 * (lv & 15) + 8 * (lv >> 4) - 16) * 2 - hh * 256 - 192;
    CvFrag cf[2];
    auto rdc = [&](CvFrag& f, int rr, int k) INL {
      const int i = k / 3, c = k % 3;
      if (c == 2) f.b[i] = *(const LDS s16x8*)(pb + i * 512 + (3 - rr) * 64);
      else f.a[2 * i + c] = *(const LDS s16x4u*)(lds + pa[i >> 1] + (i & 1 ? 0 : 512) + (3 - rr) * 64 + c * 8);
    };
#pragma unroll
    for (int k = 0; k < 24; ++k) if (k % 3 < 2) rdc(cf[0], 0, k);
    STAMP(18);
    bar();
#pragma unroll
    for (int k = 2; k < 24; k += 3) rdc(cf[0], 0, k);
    STAMP(11);
    SPRIO(CPRIO);
    unsigned seen = 0, vyl, vyw;
    f32x4 ca[8] = {};
    auto conv = [&](int rr) INL {
      int n = 0;
#pragma unroll
      for (int u = 0; u < 8; ++u)
#pragma unroll
        for (int t = u; t < 8; ++t) {
          ca[t] = mfma16(cat(cf[rr & 1].a[2 * u], cf[rr & 1].a[2 * u + 1]), cf[rr & 1].b[t - u], ca[t]);
          SB();
          if (n < 32 && n % 4 < 3) rdc(cf[(rr + 1) & 1], rr + 1, n / 4 * 3 + n % 4);
          const int q = rr * 36 + n;
          const int j = 16 + NPRE + (q - Q0) / PACE;
          if (q >= Q0 && (q - Q0) % PACE == 0 && j < 44) {
            if (j == 28 || j == 36) await(USED, seen, j == 28 ? 1 : 2);
            if (j == 27 || j == 35) seen = peek(USED);
            issue_wd(j >> 2, j & 3);
          }
          SB();
          ++n;
        }
    };
    conv(0); conv(1); conv(2);
    {
      unsigned o[4][2];
      auto last = [&](int h) INL {
        auto tile = [](int set, int k) INL { return (k >> 1) * 4 + (set ? 1 + (k & 1) : 3 * (k & 1)); };
        unsigned mate;
        f32x4 in[4];
#pragma unroll
        for (int ph = 0; ph < 2; ++ph) {
          int n = 0;
#pragma unroll
          for (int u = 0; u < 8; ++u)
#pragma unroll
            for (int k = 3; k >= 0; --k) {
              const int t = tile(ph ? h : 1 - h, k);
              if (t < u) continue;
              ca[t] = mfma16(cat(cf[1].a[2 * u], cf[1].a[2 * u + 1]), cf[1].b[t - u], ca[t]);
              SB();
              if (ph && n < 4) *(LDS f32x4*)(lds + vex[0] + n * 1024) = ca[tile(1 - h, n)];
              if (ph && n == 4) post(SYNC, 1);
              if (ph && n == 6) { waitv<8>(); post(LANDED, 3); }
              if (ph && n == 10) mate = *(volatile LDS unsigned*)(lds + SYNC + (w4 ^ 1) * 4);
              SB();
              ++n;
            }
        }
        STAMP(12);
        if (__builtin_expect(!rfl(mate), 0)) lds_wait(lds + SYNC + (w4 ^ 1) * 4, 1);
        fence();
        STAMP(20);
#pragma unroll
        for (int k = 0; k < 4; ++k) in[k] = *(const LDS f32x4*)(lds + vex[1] + k * 1024);
        SB();
#pragma unroll
        for (int k = 0; k < 4; ++k) {
          const f32x4 s = ca[tile(h, k)] + in[k];
          o[k][0] = pk2(s[0], s[1]); o[k][1] = pk2(s[2], s[3]);
        }
      };
      if (hh) last(1); else last(0);
      SPRIO(TPRIO);
#pragma unroll
      for (int kp = 0; kp < 2; ++kp) {
        const auto r0 = __builtin_amdgcn_permlane16_swap(o[2 * kp][0], o[2 * kp + 1][0], false, false);
        const auto r1 = __builtin_amdgcn_permlane16_swap(o[2 * kp][1], o[2 * kp + 1][1], false, false);
        const u32x4 ov = {r0[0], r1[0], r0[1], r1[1]};
        st16(ov, rws, vys, WS_YT + kp * 2048);
      }
      STAMP(13);
      asm volatile("" : "+v"(lv));
      vyl = (n2 * 64 + w4 * 16 + (lv >> 3)) * 4096 + m2 * 128 + (lv & 7) * 16;
      vyw = reg + hh * 4096 + (lv >> 3) * 160 + (lv & 3) * 32 + (lv & 4) * 4;
      asm volatile("" : "+v"(vyl), "+v"(vyw));
      waitv<2>();
      post(LANDED, 4);
      waitv<0>();
      STAMP(23);
      __builtin_amdgcn_raw_buffer_store_b64(tg, rws, vyf, 0, SC1);
    }
    STAMP(14);
    __builtin_amdgcn_s_sleep(YSLEEP);
    const unsigned yf = WS_YF + ((((m2 >> 2) + 1) >> 1 & 1) * 256 + n2 * 32 + w4 * 8) * 16;
    if (!watch<YNAP, 2>(ws + yf, tag)) poll<YNAP>(ws, yf + j16 * 8, tag);
    STAMP(15);
#pragma unroll
    for (int i = 0; i < 2; ++i) xi[i] = __builtin_amdgcn_raw_buffer_load_b128(rws, vyl, WS_YT + i * 32768, SC1);
#pragma unroll
    for (int i = 0; i < 2; ++i) *(LDS u32x4*)(lds + vyw + i * 1280) = xi[i];
    post(DONE + 16, 1);
    waitl0();
    STAMP(16);
    STAMP_OUT(1);
    asm volatile(".Lk_code_end1:");
  }
}

}

extern "C" void kernel_launch(void* const* d_in, const int* in_sizes, int n_in, void* d_out, int out_size, void* d_ws, size_t ws_size,
                              hipStream_t stream) {
  fused_kernel<<<256, 512, 0, stream>>>((const u16*)d_in[0], (const u16*)d_in[1], (const u16*)d_in[2], (const u16*)d_in[3], (const u16*)d_in[4],
                                        (const u16*)d_in[5], (const u16*)d_in[6], (const u16*)d_in[8], (const u16*)d_in[9], (const u16*)d_in[10],
                                        (u16*)d_out, (char*)d_ws);
}
```

```cpp
#include <hip/hip_runtime.h>
#include <hip/hip_bf16.h>

namespace {

typedef unsigned short u16;
typedef unsigned long long u64;
typedef __attribute__((ext_vector_type(4))) short s16x4;
typedef s16x4 __attribute__((aligned(4))) s16x4u;
typedef __attribute__((ext_vector_type(8))) short s16x8;
typedef __attribute__((ext_vector_type(8))) __bf16 bf16x8;
typedef __attribute__((ext_vector_type(2))) __bf16 bf16x2;
typedef __attribute__((ext_vector_type(8))) _Float16 f16x8;
typedef __attribute__((ext_vector_type(2))) _Float16 f16x2;
typedef __attribute__((ext_vector_type(2))) float f32x2;
typedef __attribute__((ext_vector_type(4))) float f32x4;
typedef __attribute__((ext_vector_type(16))) float f32x16;
typedef __attribute__((ext_vector_type(4))) unsigned u32x4;
typedef __attribute__((ext_vector_type(2))) unsigned u32x2;
typedef __amdgpu_buffer_rsrc_t rsrc_t;
#define LDS __attribute__((address_space(3)))
#define GLB __attribute__((address_space(1)))
#define INL __attribute__((always_inline))
#define SB() __builtin_amdgcn_sched_barrier(0)
#define RLX_AGENT __ATOMIC_RELAXED, __HIP_MEMORY_SCOPE_AGENT
#define RLX_WG __ATOMIC_RELAXED, __HIP_MEMORY_SCOPE_WORKGROUP

constexpr int L = 2048, D = 512, NM = 128, F = 1376, NU = F / 16;
constexpr int SC1 = 16;
#define HF 10
#define PRIO(x)
#define TW3 11
#define SPRIO(x) __builtin_amdgcn_s_setprio(x)
#define HNAP 2
#define YNAP 2
#define RD0 1
#define PACE 4
#define TWP 0
#define D2F 6
#define HSLEEP 8
#define CPRIO 1
#define TPRIO 1
#define NPRE 16
#define Q0 3
#define RD1 3
#define B9Q 5
#define PKQ 6
#define FOLD 24
#define YREP 1
#define YST 128
#define HREP 1
#define YMARK 0
#define YSLEEP 14
static_assert(Q0 + (27 - NPRE) * PACE < 108 && RD0 < RD1 && NPRE >= 12 && NPRE <= 20 && RD0 < B9Q, "schedule");
constexpr unsigned SPIN_CAP = 1u << 18;

constexpr int WBLK = 3104, GOFF = 16384, UOFF = GOFF + 4 * WBLK, SLOT = GOFF + 8 * WBLK;
constexpr int LEFT = 32768;
constexpr int LNB = 2 * SLOT, LNROW = 1088;
constexpr int SBASE = 3 * SLOT;
constexpr int KROW = 272, KSW = 32 * KROW, PAIR = 2 * KSW;
constexpr int TX = 0, XIMG = 4160, XPAD = 480, CEX = 8736, TY = 13248, KEX0 = 7168, KEX1 = KSW;
constexpr int STO = SBASE + 2 * PAIR, CSO = STO + 1024, SYNC = CSO + 1536, LANDED = SYNC + 16, USED = SYNC + 32, DONE = SYNC + 48, DUMMY = SYNC + 128;
constexpr int LDS_BYTES = 163840;
static_assert(DUMMY + 256 <= LDS_BYTES && XIMG + XPAD + 4096 <= CEX && CEX + 4096 <= TY && TY + 4128 <= PAIR, "LDS map");

constexpr int WS_E = 0, WS_XNF = 4096, WS_HF = 65536, WS_YF = 196608, WS_ST = 32768, WS_CS = 40960;
constexpr int XNP = 8320, WS_XNT = 1 << 19, WS_YT = 3 << 20, WS_H = 5 << 20, WS_END = WS_H + 64 * NU * 1024;
constexpr int CSP = 1408;

__device__ __forceinline__ float u2f(unsigned v) { return __builtin_bit_cast(float, v); }
__device__ __forceinline__ unsigned f2u(float v) { return __builtin_bit_cast(unsigned, v); }
__device__ __forceinline__ bool wave_all(bool p) { return __builtin_amdgcn_ballot_w64(!p) == 0; }
__device__ __forceinline__ unsigned rfl(unsigned v) { return __builtin_amdgcn_readfirstlane(v); }
__device__ __forceinline__ rsrc_t mkr(const void* p, unsigned n) { return __builtin_amdgcn_make_buffer_rsrc((void*)p, 0, n, 0x00020000); }
__device__ __forceinline__ void dma16(unsigned base, int dst, unsigned voff, rsrc_t r, unsigned soff, int off = 0) {
  asm volatile("s_add_u32 m0, %0, %1\n\ts_nop 0\n\tbuffer_load_dwordx4 %2, %3, %4 offen offset:%5 lds"
               ::"s"(base), "n"(dst - off), "v"(voff), "s"(r), "s"(soff), "n"(off) : "scc", "memory");
}
__device__ __forceinline__ void touch(unsigned m0v, unsigned voff, rsrc_t r, unsigned soff) {
  asm volatile("s_mov_b32 m0, %0\n\ts_nop 0\n\tbuffer_load_dword %1, %2, %3 offen lds" ::"s"(m0v), "v"(voff), "s"(r), "s"(soff) : "memory");
}
__device__ __forceinline__ void st16(u32x4 v, rsrc_t r, unsigned voff, unsigned soff) {
  asm volatile("buffer_store_dwordx4 %0, %1, %2, %3 offen sc1\n\ts_nop 1" ::"v"(v), "v"(voff), "s"(r), "s"(soff) : "memory");
}
template <int W> __device__ __forceinline__ void waitv() { asm volatile("s_waitcnt vmcnt(%0)" ::"n"(W) : "memory"); }
__device__ __forceinline__ void waitl0() { asm volatile("" ::: "memory"); __builtin_amdgcn_s_waitcnt(0xc07f); asm volatile("" ::: "memory"); }
__device__ __forceinline__ void fence() { asm volatile("" ::: "memory"); }
__device__ __forceinline__ void bar() { waitl0(); __builtin_amdgcn_s_barrier(); fence(); }
__device__ __forceinline__ unsigned pk2(float a, float b) { f32x2 p = {a, b}; return __builtin_bit_cast(unsigned, __builtin_convertvector(p, bf16x2)); }
__device__ __forceinline__ unsigned pkh(float a, float b) { f32x2 p = {a, b}; return __builtin_bit_cast(unsigned, __builtin_convertvector(p, f16x2)); }
__device__ __forceinline__ float blo(unsigned v) { return u2f(v << 16); }
__device__ __forceinline__ float bhi(unsigned v) { return u2f(v & 0xffff0000u); }
__device__ __forceinline__ float rb(float a) { return blo(pk2(a, a)); }
template <int C, int RM> __device__ __forceinline__ float dppf(float v) {
  return __builtin_bit_cast(float, __builtin_amdgcn_update_dpp(0, __builtin_bit_cast(int, v), C, RM, 0xf, true));
}
__device__ __forceinline__ float lane63(float v) { return __builtin_bit_cast(float, __builtin_amdgcn_readlane(__builtin_bit_cast(int, v), 63)); }
__device__ __forceinline__ float wave_sum(float v) {
  v += dppf<0x111, 0xf>(v); v += dppf<0x112, 0xf>(v); v += dppf<0x114, 0xf>(v); v += dppf<0x118, 0xf>(v);
  v += dppf<0x142, 0xa>(v); v += dppf<0x143, 0xc>(v);
  return lane63(v);
}
template <int C, int RM> __device__ __forceinline__ unsigned dppmax(unsigned v) {
  const unsigned o = __builtin_amdgcn_update_dpp(0, v, C, RM, 0xf, true);
  return v > o ? v : o;
}
__device__ __forceinline__ unsigned wave_max(unsigned v) {
  v = dppmax<0x111, 0xf>(v); v = dppmax<0x112, 0xf>(v); v = dppmax<0x114, 0xf>(v); v = dppmax<0x118, 0xf>(v);
  v = dppmax<0x142, 0xa>(v); v = dppmax<0x143, 0xc>(v);
  return __builtin_amdgcn_readlane(v, 63);
}
__device__ __forceinline__ s16x8 cat(s16x4 a, s16x4 b) { return __builtin_shufflevector(a, b, 0, 1, 2, 3, 4, 5, 6, 7); }
__device__ __forceinline__ s16x4 trd(LDS char* p) { return __builtin_amdgcn_ds_read_tr16_b64_v4i16((LDS s16x4*)p); }
__device__ __forceinline__ f32x4 mfma16(s16x8 a, s16x8 b, f32x4 c) {
  return __builtin_amdgcn_mfma_f32_16x16x32_bf16(__builtin_bit_cast(bf16x8, a), __builtin_bit_cast(bf16x8, b), c, 0, 0, 0);
}
template <int NAP> __device__ __forceinline__ void poll(const char* ws, unsigned off, u64 tag) {
  GLB u64* p = (GLB u64*)(ws + off);
#pragma unroll 1
  for (unsigned spins = 0; spins < SPIN_CAP; ++spins) {
    if (wave_all(__hip_atomic_load(p, RLX_AGENT) == tag)) break;
    __builtin_amdgcn_s_sleep(NAP);
  }
  fence();
}
typedef __attribute__((ext_vector_type(8))) unsigned long long u64x8;
template <int NAP, int N, int TRIES> __device__ __forceinline__ bool watch(const char* p, u64 tag) {
#pragma unroll 1
  for (unsigned spins = 0; spins < TRIES; ++spins) {
    u64x8 f[2];
    u64 d = 0;
    if constexpr (N == 1) asm volatile("s_load_dwordx16 %0, %1, 0 glc\n\ts_waitcnt lgkmcnt(0)" : "=&s"(f[0]) : "s"(p) : "memory");
    else asm volatile("s_load_dwordx16 %0, %2, 0 glc\n\ts_load_dwordx16 %1, %2, %3 glc\n\ts_waitcnt lgkmcnt(0)" : "=&s"(f[0]), "=&s"(f[1]) : "s"(p), "n"(YST) : "memory");
#pragma unroll
    for (int i = 0; i < 8 * N; ++i) { d |= f[i >> 3][i & 7] ^ tag; asm("" : "+s"(d)); }
    if (d == 0) return true;
    if (NAP) __builtin_amdgcn_s_sleep(NAP);
  }
  return false;
}
__device__ __forceinline__ void lds_wait(LDS char* p, unsigned v) {
#pragma unroll 1
  for (unsigned spins = 0; spins < SPIN_CAP; ++spins) { if (rfl(*(volatile LDS unsigned*)p) >= v) break; __builtin_amdgcn_s_sleep(1); }
  fence();
}
__device__ __forceinline__ void setflag(char* ws, unsigned off, u64 tag, int lane) {
  if (lane == 0) __hip_atomic_store((GLB u64*)(ws + off), tag, RLX_AGENT);
}
__device__ __forceinline__ f32x2 fma2(f32x2 a, f32x2 b, f32x2 c) { return __builtin_elementwise_fma(a, b, c); }
__device__ __forceinline__ float one(float v) { asm("" : "+v"(v)); return v; }
__device__ __forceinline__ f32x2 dup(float a) { return f32x2{a, a}; }
struct cmulr { f32x2 z, rot; };
__device__ __forceinline__ cmulr mul_by(f32x2 z) { return {z, f32x2{-z[1], z[0]}}; }
__device__ __forceinline__ f32x2 cmul(f32x2 a, cmulr m) { return fma2(dup(a[0]), m.z, dup(a[1]) * m.rot); }
__device__ __forceinline__ f32x2 cpow2(float s2, float th, float p) {
  const float t = th * p, n = __builtin_rintf(t * 0.15915494f);
  const float fr = __builtin_fmaf(t, 0.15915494f, -n) + t * 6.4206382e-9f;
  const float e = __builtin_amdgcn_exp2f(s2 * p);
  return f32x2{e * __builtin_amdgcn_cosf(fr), e * __builtin_amdgcn_sinf(fr)};
}
__device__ __forceinline__ float exp_f(float v) {
  const float t = v * 1.44269504f, e = __builtin_fmaf(v, 1.925963033e-8f, __builtin_fmaf(v, 1.44269504f, -t));
  const float y = __builtin_amdgcn_exp2f(t);
  return __builtin_fmaf(y, e * 0.69314718f, y);
}

#define STAMP(i)
#define STAMP_OUT(role)

struct G1Frag { s16x8 x[4]; s16x4 w[12]; };
struct CvFrag { s16x4 a[16]; s16x8 b[8]; };

__global__ __launch_bounds__(512, 2) void fused_kernel(const u16* x, const u16* lar, const u16* aim, const u16* cre, const u16* cim, const u16* ldt,
                                                       const u16* dsk, const u16* wg, const u16* wu, const u16* wd, u16* out, char* ws) {
  __shared__ __attribute__((aligned(16))) char lds_raw[LDS_BYTES];
  LDS char* lds = (LDS char*)lds_raw;
  int w = rfl(threadIdx.x >> 6);
  const int lane = threadIdx.x & 63, tid = threadIdx.x & 255;
  const int b = blockIdx.x, xcd = b & 7, r = b >> 3;
  STAMP(0);
  if (w == 7) {
    u64 pc; unsigned l0, l1;
    asm volatile("s_getpc_b64 %0\n.Lk_code_begin:\n\ts_mov_b32 %1, .Lk_code_end0-.Lk_code_begin\n\ts_mov_b32 %2, .Lk_code_end1-.Lk_code_begin" : "=s"(pc), "=s"(l0), "=s"(l1));
    touch(DUMMY, lane * 1024, mkr((const void*)pc, max(l0, l1) - 896), (r & 7) * 128);
  }
  const int row0 = (xcd & 1) * 1024 + (xcd >> 1) * 256 + r * 8;
  const unsigned vt = lane < 16 ? (lane >> 3) * (64 * F * 2) + (lane & 7) * 96 : 0x7f000000u;
  const unsigned so_t = ((w & 3) * 128 + (xcd & 1) * 32 + r) * (F * 2) + (xcd >> 1) * 688;
  asm volatile("" ::"s"(row0), "v"(vt), "s"(so_t));
  STAMP(28);
  asm volatile("" : "+s"(x), "+s"(lar), "+s"(aim), "+s"(cre), "+s"(cim), "+s"(ldt), "+s"(dsk), "+s"(wg), "+s"(wu), "+s"(wd), "+s"(out), "+s"(ws));
  STAMP(24);
  const rsrc_t rx = mkr(x, L * D * 2);
  const int w4 = w & 3, mw = w & 1, nw = w4 >> 1;
  const int cp = xcd * 32 + r, ch = cp * 2 + nw;
  u32x4 xr[2];
  u64 tag;
  if (w < 4) {
#pragma unroll
    for (int i = 0; i < 2; ++i)
      asm volatile("buffer_load_dwordx4 %0, %1, %2, %3 offen" : "=v"(xr[i]) : "v"(lane * 16), "s"(rx), "s"((row0 + 2 * w4 + i) * 1024) : "memory");
  } else {
    const unsigned pi = (ch * NM + mw * 64 + lane) * 2;
    asm volatile("global_load_ushort %0, %6, %8\n\tglobal_load_ushort %1, %7, %9\n\tglobal_load_ushort %2, %7, %10\n\tglobal_load_ushort %3, %7, %11\n\t"
                 "global_load_ushort %4, %7, %12\n\tglobal_load_ushort %5, %6, %13"
                 : "=&v"(xr[0][0]), "=&v"(xr[0][1]), "=&v"(xr[0][2]), "=&v"(xr[0][3]), "=&v"(xr[1][0]), "=&v"(xr[1][1])
                 : "v"(ch * 2), "v"(pi), "s"(ldt), "s"(lar), "s"(aim), "s"(cre), "s"(cim), "s"(dsk) : "memory");
  }
  u64 epoch;
  asm volatile("s_load_dwordx2 %0, %1, 0x0" : "=s"(epoch) : "s"(ws) : "memory");
  __builtin_amdgcn_s_barrier();
  STAMP(25);
  touch(DUMMY, vt, mkr(w >> 2 ? wu : wg, D * F * 2), so_t);
  auto get_tag = [&]() INL {
    unsigned lo, hi;
    asm volatile("s_waitcnt lgkmcnt(0)\n\ts_add_u32 %0, %2, 0x7F4A7C15\n\ts_addc_u32 %1, %3, 0x9E3779B9"
                 : "=&s"(lo), "=s"(hi) : "s"((unsigned)epoch), "s"((unsigned)(epoch >> 32)) : "scc", "memory");
    tag = (u64)hi << 32 | lo;
  };
  asm volatile("" : "+s"(w));
  SB();

  const rsrc_t rws = mkr(ws, WS_END), rwd = mkr(wd, F * D * 2);
  const bool gemm = r < 30;
  const int idx = (xcd >> 1) * 30 + r, nt = idx >> 3, mt = (xcd & 1) * 8 + (idx & 7);
  const int n0 = nt < 14 ? nt * 96 : 1280;
  const int m2 = (xcd & 1) * 16 + (xcd >> 1) * 4 + (r >> 3), n2 = r & 7;
  const int j16 = lane & 15, g = lane >> 4;

  const rsrc_t rw = mkr((gemm ? nw : xcd >> 2) ? wu : wg, D * F * 2);
  unsigned vx[2], vw[3], lx = w4 * 4096, lw = (nw ? UOFF : GOFF) + mw * 2 * WBLK;
  asm volatile("" : "+s"(lx), "+s"(lw));
  auto addr_x = [&]() INL {
#pragma unroll
    for (int i = 0; i < 2; ++i) {
      const int row = i * 8 + (lane >> 3), id = (row >> 1) & 7, f = (id & 4) | ((id & 1) << 1) | ((id >> 1) & 1);
      vx[i] = (mt * 128 + w4 * 32 + row) * 1024 + (((lane & 7) ^ f) << 4);
    }
  };
  auto addr_w = [&]() INL {
    const int kr = (lane * 171) >> 11, c = lane - kr * 12;
    vw[0] = (mw * 32 + kr) * (F * 2) + c * 16 + n0 * 2;
    vw[1] = vw[0] + (c >= 8 ? 6 * F * 2 - 128 : 5 * F * 2 + 64);
    vw[2] = vw[0] + (c >= 4 ? 11 * F * 2 - 64 : 10 * F * 2 + 128);
  };
  auto issue = [&](int s, int i) INL {
    const int slot = (s % 3) * SLOT, j = i - 4;
    if (i < 4) dma16(lx, slot + i * 1024, vx[i & 1], rx, s * 128 + (i >> 1) * 16384);
    else dma16(lw, slot + (j / 3) * WBLK + (j % 3) * 1024, vw[j % 3], rw, s * (64 * F * 2) + (j / 3) * 16 * F * 2);
  };
  const unsigned wl = w4 * 8192, wl6 = (nw ? 0 : 2 * SLOT) + LEFT + mw * 4096;
  auto wdloc = [](int pos) INL { return ((pos / 2 + 2) % 3) * SLOT + (pos & 1) * 4096; };

  auto post = [&](int word, unsigned v) INL { fence(); *(volatile LDS unsigned*)(lds + word + w4 * 4) = v; fence(); };
  auto peek = [&](int word) INL { return *(volatile LDS unsigned*)(lds + word + w4 * 4); };
  auto await = [&](int word, unsigned seen, unsigned v) INL { if (__builtin_expect(rfl(seen) < v, 0)) lds_wait(lds + word + w4 * 4, v); fence(); };

  if (__builtin_expect(w < 4, 1)) {
    auto layernorm = [&](auto&& hook) INL {
      get_tag();
      STAMP(1);
      u32x2 stat;
#pragma unroll
      for (int i = 0; i < 2; ++i) {
        f32x2 v[4];
#pragma unroll
        for (int e = 0; e < 4; ++e) asm volatile("v_lshlrev_b32 %0, 16, %2\n\tv_and_b32 %1, 0xffff0000, %2" : "=&v"(v[e][0]), "=v"(v[e][1]) : "v"(xr[i][e]));
        hook(5 * i);
        const float x0 = u2f(rfl(f2u(v[0][0])));
#pragma unroll
        for (int e = 0; e < 4; ++e) v[e] -= dup(x0);
        const f32x2 s1 = (v[0] + v[1]) + (v[2] + v[3]), s2 = fma2(v[0], v[0], v[1] * v[1]) + fma2(v[2], v[2], v[3] * v[3]);
        float t1 = s1[0] + s1[1], t2 = s2[0] + s2[1];
        asm volatile("" : "+v"(t1), "+v"(t2));
        hook(5 * i + 1);
        const float md = wave_sum(t1) * (1.f / D);
        float var = __builtin_fmaf(-md, md, wave_sum(t2) * (1.f / D));
        asm volatile("" : "+v"(var));
        hook(5 * i + 2);
        const float m = rb(x0 + md), sd = rb(__builtin_amdgcn_sqrtf(rb(rb(__builtin_fmaxf(var, 0.f)) + 1e-5f))), rs = __builtin_amdgcn_rcpf(sd);
        float c = (x0 - m) * rs;
        asm volatile("" : "+v"(c));
        hook(5 * i + 3);
        u32x4 o;
#pragma unroll
        for (int e = 0; e < 4; ++e) { const f32x2 n = fma2(v[e], dup(rs), dup(c)); o[e] = pk2(n[0], n[1]); }
        *(LDS u32x4*)(lds + LNB + (2 * w4 + i) * LNROW + lane * 16) = o;
        hook(5 * i + 4);
        stat[i] = pk2(m, sd);
      }
      __builtin_amdgcn_raw_buffer_store_b64(stat, rws, lane ? 0x7f000000u : 0u, WS_ST + (row0 + 2 * w4) * 4, SC1);
      STAMP(21);
      bar();
      STAMP(22);
#pragma unroll
      for (int i = 0; i < 2; ++i) {
        LDS char* p = lds + LNB + (j16 >> 2) * LNROW + (i * 256 + w4 * 64 + g * 16 + (j16 & 3) * 4) * 2;
        st16(__builtin_bit_cast(u32x4, cat(trd(p), trd(p + 4 * LNROW))), rws, tid * 16, WS_XNT + (row0 >> 3) * XNP + i * 4096);
      }
    };
    unsigned ad[3][4], vh[4], vpoll, kh, kh10, a_xt, vflag = 0x7f000000u;
    u32x2 xt[4];
    auto rdxt = [&](int u) INL { xt[u] = *(const LDS u32x2*)(lds + SBASE + CEX - 32 * 1024 + (a_xt ^ (u * 32))); };
    s16x4 wf[11][8];
    auto wd_addr = [&](int i) INL {
      const int q = j16 >> 2, f = ((q >> 1) & 1) + 2 * (g & 1);
#pragma unroll
      for (int u = 0; u < 4; ++u) {
        ad[i][u] = (i == 0 ? wl : i == 1 ? wl + 2 * SLOT : wl6) + (8 * g + q) * 128 + ((u ^ f) << 5) + (j16 & 3) * 8;
        asm volatile("" : "+v"(ad[i][u]));
      }
    };
    auto rdwh = [&](int s, int u, int h) INL {
      const int pos = s % 7;
      LDS char* p = lds + (pos == 6 ? ad[2][u] : pos < 2 ? ad[1][u] + pos * 4096 : ad[0][u] + wdloc(pos));
      wf[s][2 * u + h] = trd(p + h * 512);
    };
    auto rdwd = [&](int s, int u) INL { rdwh(s, u, 0); rdwh(s, u, 1); };
    auto h_addr = [&]() INL {
      wd_addr(0); wd_addr(1); wd_addr(2);
#pragma unroll
      for (int m = 0; m < 4; ++m) { const int mm = (m + w4) & 3; vh[m] = (mm >> 1) * (NU * 1024) + (g >> 1) * 1024 + (32 * (g & 1) + 16 * (mm & 1) + j16) * 16; }
      vpoll = WS_HF + ((m2 * 4 + w4) * 8 + (n2 & (HREP - 1))) * 128;
      a_xt = 32 * 1024 + nw * PAIR + mw * 2048 + j16 * 128 + (((g >> 1) ^ (j16 >> 1)) << 4) + (g & 1) * 8;
      asm volatile("" : "+v"(a_xt), "+v"(vh[0]), "+v"(vh[1]), "+v"(vh[2]), "+v"(vh[3]));
      kh = WS_H + m2 * 2 * NU * 1024 + w4 * (11 * 2048);
      kh10 = w4 == 3 ? 0x7f000000u : kh + 10 * 2048;
      asm volatile("" : "+s"(kh), "+s"(kh10), "+s"(vpoll));
    };

    if (gemm) {
      addr_x();
      addr_w();
      asm volatile("" : "+v"(vx[0]), "+v"(vx[1]), "+v"(vw[0]), "+v"(vw[1]), "+v"(vw[2]));
      unsigned ax[4], aw[2];
      {
        const int id = (j16 >> 1) & 7, f = (id & 4) | ((id & 1) << 1) | ((id >> 1) & 1), cg = 2 * (g & 1) + (g >> 1);
#pragma unroll
        for (int s = 0; s < 2; ++s) { ax[s] = (mw * 64 + j16) * 128 + (((4 * s + cg) ^ f) << 4); ax[s + 2] = ax[s] + 2 * SLOT; }
        aw[0] = GOFF + (g & 1) * WBLK + (8 * (g >> 1) + (j16 >> 2)) * 192 + nw * 96 + (j16 & 3) * 8;
        aw[1] = aw[0] + 2 * SLOT;
      }
      unsigned a_st = STO + (mw * 64 + j16) * 8, a_cs = CSO + (nw * 48 + j16) * 16, so_h[3];
#pragma unroll
      for (int u = 0; u < 3; ++u) {
        const int unit = (n0 >> 4) + nw * 3 + u;
        so_h[u] = nt < 14 || unit >= 84 ? WS_H + ((mt * 4 + mw * 2) * NU + unit) * 1024 : 0x7f000000u;
      }
      const unsigned fi = (nt < 14 ? nt * 2 + nw : nw ? 28 : 63) - 7 * (lane >> 3);
      vflag = lane < 32 && (lane & 7) < HREP && fi < 8 ? WS_HF + (mt * 2 + mw) * 4096 + lane * 128 + fi * 8 : 0x7f000000u;
      asm volatile("" : "+v"(a_st), "+v"(a_cs), "+s"(so_h[0]), "+s"(so_h[1]), "+s"(so_h[2]), "+v"(vflag));
      asm volatile("" : "+v"(ax[0]), "+v"(ax[1]), "+v"(ax[2]), "+v"(ax[3]), "+v"(aw[0]), "+v"(aw[1]));
      h_addr();
      SB();
      STAMP(26);
#pragma unroll
      for (int i = 0; i < 10; ++i) issue(0, i);
      STAMP(20);
      waitv<11>();
      layernorm([&](int k) INL { issue(1, k); });
#pragma unroll
      for (int i = 10 - D2F; i < 10; ++i) issue(2, i);
      STAMP(23);
      f32x4 acc[4][6] = {};
      G1Frag fr[2];
      auto half = [&](G1Frag& c, bool domm, G1Frag& n, bool dord, int s, int jj) INL {
#pragma unroll
        for (int t = 0; t < 24; ++t) {
          if (domm) acc[t & 3][t >> 2] = mfma16(cat(c.w[t >> 2 << 1], c.w[(t >> 2 << 1) + 1]), c.x[t & 3], acc[t & 3][t >> 2]);
          SB();
          const int hi = s % 3 == 2, so = s % 3 == 1 ? SLOT : 0;
#pragma unroll
          for (int k = t; k < min(t + 1, 16); ++k) {
            const int u = (k - 4) >> 1;
            if (k < 4) n.x[k] = *(const LDS s16x8*)(lds + so + ax[jj + 2 * hi] + k * 2048);
            else n.w[k - 4] = trd(lds + aw[hi] + so + jj * 2 * WBLK + (u / 3) * 4 * WBLK + (u % 3) * 32 + (k & 1) * 768);
          }
          SB();
        }
      };
#pragma unroll
      for (int s = 0; s < 8; ++s) {
        if (s == 2) STAMP(27);
        if (s == 0) waitv<13 + D2F>(); else if (s == 1) waitv<D2F>(); else if (s == 2) waitv<0>();
        if (s == 3) STAMP(29);
        if (s == 0) STAMP(19);
        bar();
        STAMP(2 + s);
        half(fr[1], s > 0, fr[0], true, s, 0);
        half(fr[0], true, fr[1], true, s, 1);
      }
      bar();
      STAMP(10);
      u32x2 st[4], bm[4]; s16x8 cs[3];
      float kg[4];
#pragma unroll
      for (int t = 0; t < 24; ++t) {
        acc[t & 3][t >> 2] = mfma16(cat(fr[1].w[t >> 2 << 1], fr[1].w[(t >> 2 << 1) + 1]), fr[1].x[t & 3], acc[t & 3][t >> 2]);
        SB();
        if (t < 4) st[t] = *(const LDS u32x2*)(lds + a_st + t * 128);
        else if (t < 7) cs[t - 4] = *(const LDS s16x8*)(lds + a_cs + (t - 4) * 256);
        else if (t < 11) rdxt(t - 7);
        else if (t >= 12 && t < 16) { bm[t - 12] = u32x2{g ? 0 : st[t - 12][1], 0}; asm volatile("" : "+v"(bm[t - 12])); }
        else if (t >= 16 && t < 20) { kg[t - 16] = -1.44269504f * __builtin_amdgcn_rcpf(u2f(st[t - 16][0])); asm volatile("" : "+v"(kg[t - 16])); }
        SB();
      }
      auto mean = [&](int t) INL {
        const int m = (t & 1) + 2 * (t / 12), u = (t >> 2) % 3, up = (t >> 1) & 1;
        const s16x4 c = up ? __builtin_shufflevector(cs[u], cs[u], 4, 5, 6, 7) : __builtin_shufflevector(cs[u], cs[u], 0, 1, 2, 3);
        acc[m][u + 3 * up] = __builtin_amdgcn_mfma_f32_16x16x16bf16_1k(c, __builtin_bit_cast(s16x4, bm[m]), acc[m][u + 3 * up], 0, 0, 0);
      };
#pragma unroll
      for (int t = 0; t < FOLD; ++t) { mean(t); SB(); }
      STAMP(11);
      unsigned seen;
#pragma unroll
      for (int mp = 0; mp < 2; ++mp) {
#pragma unroll
        for (int u = 0; u < 3; ++u) {
          unsigned o[2][2];
          const int q = mp * 3 + u;
          if (q == B9Q) bar();
          if (q == RD0 || q == RD1) {
            await(LANDED, seen, q == RD0 ? 1 : 2);
#pragma unroll
            for (int k = 0; k < 8; ++k) rdwd((q == RD0 ? 0 : 2) + (k >> 2), k & 3);
            post(USED, q == RD0 ? 1 : 2);
          }
          if (q == RD0 - 1 || q == RD1 - 1) { seen = peek(LANDED); SB(); }
#pragma unroll
          for (int mi = 0; mi < 2; ++mi) {
            const int m = mp * 2 + mi;
            const float sd = u2f(st[m][0]);
#pragma unroll
            for (int e = 0; e < 2; ++e) {
              f32x2 hv;
              if (FOLD + q * 4 + mi * 2 + e < 24) mean(FOLD + q * 4 + mi * 2 + e);
              if (q < PKQ) {
                const f32x2 gg = f32x2{acc[m][u][2 * e], acc[m][u][2 * e + 1]} * dup(kg[m]);
                const f32x2 dn = fma2(f32x2{__builtin_amdgcn_exp2f(gg[0]), __builtin_amdgcn_exp2f(gg[1])}, dup(sd), dup(sd));
                hv = f32x2{acc[m][u + 3][2 * e], acc[m][u + 3][2 * e + 1]} * f32x2{__builtin_amdgcn_rcpf(dn[0]), __builtin_amdgcn_rcpf(dn[1])};
              } else {
#pragma unroll
                for (int k = 0; k < 2; ++k) {
                  const float ex = __builtin_amdgcn_exp2f(one(acc[m][u][2 * e + k] * kg[m]));
                  hv[k] = one(acc[m][u + 3][2 * e + k] * __builtin_amdgcn_rcpf(one(__builtin_fmaf(ex, sd, sd))));
                }
              }
              o[mi][e] = pk2(hv[0], hv[1]);
            }
          }
          const auto r0 = __builtin_amdgcn_permlane16_swap(o[0][0], o[1][0], false, false);
          const auto r1 = __builtin_amdgcn_permlane16_swap(o[0][1], o[1][1], false, false);
          const u32x4 ov = {r0[0], r1[0], r0[1], r1[1]};
          st16(ov, rws, lane * 16 + mp * (NU * 1024), so_h[u]);
          SB();
        }
      }
      STAMP(12);
      if (B9Q == 6) bar();
    } else {
      const int cj = (xcd & 3) * 2 + (r - 30), kg = (tid * 745) >> 14, cc = tid - kg * 22, col = cj * 176 + cc * 8;
      const unsigned vo = (kg < 11 && col < F) ? kg * (F * 2) + col * 2 : 0x7f000000u;
      u32x4 cv[2][8];
      auto ldcs = [&](int sl) INL {
#pragma unroll
        for (int i = 0; i < 8; ++i) cv[sl & 1][i] = __builtin_amdgcn_raw_buffer_load_b128(rw, vo + (sl * 8 + i) * (11 * F * 2), 0, 0);
      };
      ldcs(0);
      waitv<9>();
      layernorm([](int) INL {});
      f32x2 a[4] = {};
#pragma unroll
      for (int sl = 0; sl < 6; ++sl) {
        if (sl == 1) waitv<0>();
        if (sl < 5) ldcs(sl + 1);
        if (sl < 4) bar();
#pragma unroll
        for (int i = 0; i < 8; ++i)
#pragma unroll
          for (int e = 0; e < 4; ++e) { const unsigned q = cv[sl & 1][i][e]; a[e] += f32x2{blo(q), bhi(q)}; }
        asm volatile("" : "+v"(a[0]), "+v"(a[1]), "+v"(a[2]), "+v"(a[3]));
      }
      if (kg < 11) {
        *(LDS f32x4*)(lds + kg * 704 + cc * 32) = f32x4{a[0][0], a[0][1], a[1][0], a[1][1]};
        *(LDS f32x4*)(lds + kg * 704 + cc * 32 + 16) = f32x4{a[2][0], a[2][1], a[3][0], a[3][1]};
      }
      bar();
      if (tid < 176) {
        float s = 0.f;
#pragma unroll
        for (int k = 0; k < 11; ++k) s += *(const LDS float*)(lds + k * 704 + tid * 4);
        __builtin_amdgcn_raw_buffer_store_b64(u32x2{pk2(-s, rb(s) - s), (unsigned)tag}, rws, tid * 8, WS_CS + ((xcd >> 2) * CSP + cj * 176) * 8, SC1);
      }
      h_addr();
      bar(); bar(); bar(); bar();
#pragma unroll
      for (int u = 0; u < 4; ++u) rdxt(u);
      await(LANDED, 0, 2);
#pragma unroll
      for (int k = 0; k < 16; ++k) rdwd(k >> 2, k & 3);
      post(USED, 2);
      bar();
    }

    STAMP(13);
    f32x4 a2[4][4] = {};
#pragma unroll
    for (int u = 0; u < 4; ++u) a2[0][u] = f32x4{blo(xt[u][0]), bhi(xt[u][0]), blo(xt[u][1]), bhi(xt[u][1])};
    unsigned a_p[3], a_y = SBASE + (4 * g + (j16 >> 2)) * 160 + w4 * 32 + (j16 & 3) * 8, a_o[2], v_o = (lane >> 3) * 1024 + (lane & 7) * 16;
#pragma unroll
    for (int m = 1; m < 4; ++m) a_p[m - 1] = wdloc(m - 1) + ((w4 - m) & 3) * 8192 + lane * 16;
    a_o[0] = wdloc(4) + wl + j16 * 144 + g * 8;
    a_o[1] = wdloc(4) + wl + (lane >> 3) * 144 + (lane & 7) * 16;
    asm volatile("" : "+v"(a2[0][0]), "+v"(a2[0][1]), "+v"(a2[0][2]), "+v"(a2[0][3]));
    asm volatile("" : "+v"(a_p[0]), "+v"(a_p[1]), "+v"(a_p[2]), "+v"(a_y), "+v"(a_o[0]), "+v"(a_o[1]), "+v"(v_o));
    waitv<0>();
    __builtin_amdgcn_raw_buffer_store_b64(u32x2{(unsigned)tag, (unsigned)(tag >> 32)}, rws, vflag, 0, SC1);
    __builtin_amdgcn_s_sleep(HSLEEP);
    if (!watch<HNAP, 1, 128>(ws + vpoll, tag)) poll<HNAP>(ws, vpoll + (lane & 7) * 8, tag);
    STAMP(14);
    auto fs = [](int n) INL { return n < 32 ? n >> 2 : 8 + (n - 32) % 3; };
    auto fm = [](int n) INL { return ((n < 32 ? n : (n - 32) / 3) + 1) & 3; };
    s16x8 hf[44];
    auto ldh = [&](int n) INL {
      hf[n] = __builtin_bit_cast(s16x8, __builtin_amdgcn_raw_buffer_load_b128(rws, vh[fm(n)] + (fs(n) & 1) * 2048, fs(n) == 10 ? kh10 : kh + (fs(n) >> 1) * 4096, SC1));
    };
#pragma unroll
    for (int n = 0; n < HF; ++n) { ldh(n); SB(); }
    unsigned landed;
    u32x4 dn[2];
    SB();
#pragma unroll
    for (int n = 0; n < 44; ++n) {
      if (n == 2 || n == 22) landed = peek(LANDED);
      if (n == 4 || n == 24) await(LANDED, landed, n == 4 ? 3 : 4);
#pragma unroll
      for (int u = 0; u < 4; ++u) {
        a2[fm(n)][u] = mfma16(cat(wf[fs(n)][2 * u], wf[fs(n)][2 * u + 1]), hf[n], a2[fm(n)][u]);
        SB();
        if (u < 2 && n >= 4 && n < 32) rdwh((n >> 2) + 3, n & 3, u);
        if (u == 3 && n + HF < 44) ldh(n + HF);
        if (n >= 35 && (n - 35) % 3 == 0) *(LDS f32x4*)(lds + wdloc((n - 35) / 3) + wl + u * 1024 + lane * 16) = a2[(n - 35) / 3 + 1][u];
        SB();
      }
      if (n == 41) post(DONE, 1);
      if (n == 42) { dn[0] = *(volatile LDS u32x4*)(lds + DONE); dn[1] = *(volatile LDS u32x4*)(lds + DONE + 16); SB(); }
      if (n == 3) STAMP(15);
    }
    STAMP(16);
    auto all4 = [](u32x4 d) INL { return rfl(d[0] & d[1] & d[2] & d[3]); };
#pragma unroll 1
    for (unsigned spins = 0; spins < SPIN_CAP && !all4(dn[0]); ++spins) {
      dn[0] = *(volatile LDS u32x4*)(lds + DONE);
      dn[1] = *(volatile LDS u32x4*)(lds + DONE + 16);
    }
    fence();
    STAMP(31);
    f32x4 pp[4][3];
    u32x2 y[4];
    auto rdy = [&]() INL {
#pragma unroll
      for (int u = 0; u < 4; ++u) y[u] = __builtin_bit_cast(u32x2, trd(lds + a_y + (u >> 1) * PAIR + (u & 1) * 4096));
    };
#pragma unroll
    for (int u = 0; u < 4; ++u)
#pragma unroll
      for (int m = 0; m < 3; ++m) pp[u][m] = *(const LDS f32x4*)(lds + a_p[m] + u * 1024);
    rdy();
    SB();
#pragma unroll
    for (int u = 0; u < 4; ++u) a2[0][u] += (pp[u][0] + pp[u][1]) + pp[u][2];
    asm volatile("" : "+v"(a2[0][0]), "+v"(a2[0][1]), "+v"(a2[0][2]), "+v"(a2[0][3]));
    if (__builtin_expect(!all4(dn[1]), 0)) {
#pragma unroll 1
      for (unsigned spins = 0; spins < SPIN_CAP && !all4(*(volatile LDS u32x4*)(lds + DONE + 16)); ++spins) {}
      fence();
      rdy();
    }
    STAMP(17);
#pragma unroll
    for (int u = 0; u < 4; ++u) {
      const f32x4 s = a2[0][u] + f32x4{blo(y[u][0]), bhi(y[u][0]), blo(y[u][1]), bhi(y[u][1])};
      *(LDS u32x2*)(lds + a_o[0] + u * 32) = u32x2{pk2(s[0], s[1]), pk2(s[2], s[3])};
    }
    fence();
    const u32x4 o0 = *(const LDS u32x4*)(lds + a_o[1]), o1 = *(const LDS u32x4*)(lds + a_o[1] + 8 * 144);
    st16(o0, mkr(out, L * D * 2), v_o, (m2 * 64 + w4 * 16) * 1024 + n2 * 128);
    st16(o1, mkr(out, L * D * 2), v_o, (m2 * 64 + w4 * 16 + 8) * 1024 + n2 * 128);
    STAMP(18);
    STAMP_OUT(0);
    asm volatile(".Lk_code_end0:");
  } else {
    const int cl = nw, hh = mw;
    STAMP(21);
    addr_x();
    addr_w();
    asm volatile("" : "+v"(vx[0]), "+v"(vx[1]), "+v"(vw[0]), "+v"(vw[1]), "+v"(vw[2]));
    auto part = [&](int s, int i0, int i1) INL {
      if (__builtin_expect(gemm, 1)) {
#pragma unroll
        for (int i = i0; i < i1; ++i) issue(s, i);
      }
    };
    auto stage = [&](int s) INL { part(s, 0, 10); };
    unsigned vd[4], vcs, pa[4], vxi, vzero, vimg, vxf, vtw[2], vtab[2], vex[2], vyf, vys, vco, vxt[2], vyl, vyw;
#pragma unroll
    for (int i = 0; i < 4; ++i) { const int row = lane >> 3, f = ((row >> 1) & 1) + 2 * (i & 1); vd[i] = (w4 * 352 + i * 8 + row) * 1024 + n2 * 128 + (((lane & 7) ^ (2 * f)) << 4); }
    vcs = tid < 96 ? (n0 + tid) * 8 : (CSP + n0 + min(tid, 191) - 96) * 8;
#pragma unroll
    for (int i = 0; i < 4; ++i) pa[i] = SBASE + cl * PAIR + (j16 & 1 ? TX : TY) + (847 - 256 * i - hh * 64 - ((15 + j16 - 8 * g) >> 1)) * 4;
    vxi = (tid >> 1) * XNP + (tid & 1) * 16 + cp * 32;
    vimg = SBASE + (tid & 1) * PAIR + XIMG + (tid >> 1) * 16;
    vzero = lane * 16 + (hh ? SYNC : SBASE + cl * PAIR + (lane < 2 ? TY + 4096 : TX + 4064));
    vtab[0] = SBASE + cl * PAIR + (255 - (hh ? (lane >> 5 ? 64 : 128) + (lane & 31) : lane)) * 16;
    vtab[1] = vtab[0] - (hh ? 32 : 192) * 16;
    vex[0] = (hh ? SBASE + cl * PAIR + CEX : SLOT + LEFT + cl * 4096) + lane * 16;
    vex[1] = (hh ? SLOT + LEFT + cl * 4096 : SBASE + cl * PAIR + CEX) + lane * 16;
    vyf = lane < 4 * YREP ? WS_YF + (((lane & 2) * 2 + (hh ? 1 + (lane & 1) : 3 * (lane & 1))) * 4 + (lane >> 2)) * (64 * YST) + (ch >> 3) * YST + (ch & 7) * 8
                          : lane == 63 && b == 0 && w4 == 0 ? WS_E : 0x7f000000u;
    vys = ch * 4096 + (hh ? 1 + (g & 1) : 3 * (g & 1)) * 512 + (j16 >> 2) * 128 + (g >> 1) * 64 + (j16 & 3) * 16;
    vco = CSO + (tid < 96 ? tid * 16 : (tid - 96) * 16 + 8);
    vxt[0] = (m2 * 64 + w4 * 16 + (lane >> 3)) * 1024 + n2 * 128 + (((lane & 7) ^ g) << 4);
    vxt[1] = vxt[0] ^ 64;
    vyl = (n2 * 64 + w4 * 16 + (lane >> 3)) * 4096 + m2 * 128 + (lane & 7) * 16;
    vyw = SBASE + cl * PAIR + hh * 4096 + (lane >> 3) * 160 + (lane & 3) * 32 + (lane & 4) * 4;
    asm volatile("" : "+v"(vxt[0]), "+v"(vxt[1]), "+v"(vyl), "+v"(vyw));
    asm volatile("" : "+v"(vco), "+v"(vex[0]), "+v"(vex[1]), "+v"(vyf), "+v"(vys));
    vxf = tid ? 0x7f000000u : WS_XNF + b * 8;
    vtw[0] = lane < 11 ? (b * 43 + w4 * 11 + lane) * 128 : 0x7f000000u;
    vtw[1] = lane < 11 ? (r * 43 + w4 * 11 + lane) * 1024 + ((384u - (unsigned)(u64)wd) & 896u) : 0x7f000000u;
    asm volatile("" : "+v"(vd[0]), "+v"(vd[1]), "+v"(vd[2]), "+v"(vd[3]), "+v"(vcs), "+v"(pa[0]), "+v"(pa[1]), "+v"(pa[2]), "+v"(pa[3]));
    asm volatile("" : "+v"(vxi), "+v"(vimg), "+v"(vzero), "+v"(vxf), "+v"(vtw[0]), "+v"(vtw[1]), "+v"(vtab[0]), "+v"(vtab[1]));
    auto issue_wd = [&](int s, int p) INL {
      const int pos = s % 7;
      dma16(pos == 6 ? wl6 : wl, (pos == 6 ? 0 : wdloc(pos)) + p * 1024, vd[p], rwd, s * 32768);
    };
    SPRIO(1);
    STAMP(22);
    waitv<1>();
    get_tag();
    STAMP(1);
    u32x2 tg = {(unsigned)tag, (unsigned)(tag >> 32)};
    asm volatile("" : "+v"(tg));
    float prm[6];
#pragma unroll
    for (int i = 0; i < 6; ++i) asm volatile("v_lshlrev_b32 %0, 16, %1" : "=v"(prm[i]) : "v"(xr[i >> 2][i & 3]));
    const float p_ldt = prm[0], p_lar = prm[1], p_aim = prm[2], p_cr = prm[3], p_ci = prm[4], skip = one(lane ? 0.f : prm[5]);
    const float dt = rb(exp_f(p_ldt));
    const float s2 = -rb(exp_f(p_lar)) * dt * 1.44269504f, th = p_aim * dt;
    const unsigned eb = min(max(wave_max(max(f2u(p_cr) << 1, f2u(p_ci) << 1)) >> 24, 1u), 253u);
    const float sc = u2f((254u - eb) << 23), ksc = 2.f * dt * u2f(eb << 23);
    f32x2 z1 = cpow2(s2, th, 1.f), z8c = cpow2(s2, -th, 8.f), zb = cpow2(s2, th, 256.f);
    asm volatile("" : "+v"(z1), "+v"(z8c), "+v"(zb));
    const cmulr z256 = mul_by(zb);
    STAMP(2);
    LDS char* pr = lds + SBASE + cl * PAIR;
    LDS char* ks = pr + hh * KSW;
    auto put = [&](int row, f32x2 v) INL { *(LDS unsigned*)(ks + row * KROW + lane * 4) = pkh(v[0], v[1]); };
    auto putu = [&](int reg, int h, f32x2 v) INL { put((reg & 3) + 4 * h + 8 * (reg >> 2), v); };
    f16x8 vf[8];
    {
      f32x2 v[4];
      const cmulr m1 = mul_by(z8c);
      v[0] = f32x2{1.f, 0.f}; v[1] = z8c; v[2] = cmul(z8c, m1); v[3] = cmul(v[2], m1);
      const cmulr m4 = mul_by(cmul(v[3], m1));
#pragma unroll
      for (int k = 0; k < 32; ++k) {
        if (k == 4) v[0] = m4.z; else if (k > 4) v[k & 3] = cmul(v[k & 3], m4);
        put(k, v[k & 3]);
      }
      fence();
#pragma unroll
      for (int s = 0; s < 8; ++s) vf[s] = *(const LDS f16x8*)(ks + (lane & 31) * KROW + s * 32 + (lane >> 5) * 16);
      fence();
    }
    STAMP(3);
    {
      f32x2 e[5];
      const cmulr m1 = mul_by(z1), m2_ = mul_by(cmul(z1, m1));
      e[0] = f32x2{p_cr, p_ci} * dup(sc);
      putu(0, 0, e[0]);
#pragma unroll
      for (int k = 0; k < 4; ++k) { putu(2 * k + 1, 0, cmul(e[k], m1)); e[k + 1] = cmul(e[k], m2_); putu(2 * k + 2, 0, e[k + 1]); }
#pragma unroll
      for (int k = 0; k < 5; ++k) e[k] = cmul(e[k], z256);
      putu(0, 1, e[0]); putu(2, 1, e[1]); putu(3, 1, e[2]); putu(4, 1, e[2]); putu(5, 1, e[2]); putu(6, 1, e[3]); putu(7, 1, e[4]); putu(8, 1, e[4]);
#pragma unroll
      for (int k = 0; k < 6; ++k) {
        const int reg = k < 2 ? 9 + 2 * k : k < 5 ? 5 + 2 * k : 13, h = k < 2 || k == 5;
        e[0] = cmul(e[0], z256); putu(reg, h, e[0]);
        e[4] = cmul(e[4], z256); putu(reg + 1, h, e[4]);
        if (k < 2) { e[2] = cmul(e[2], z256); putu(k ? 1 : 15, 1, e[2]); }
        if (TWP == 0 && k == 5) touch(DUMMY, vtw[0], rwd, 0);
      }
      fence();
    }
    STAMP(4);
    bar();
    f32x16 kd = {};
    {
      f16x8 uf[8];
#pragma unroll
      for (int s = 0; s < 8; ++s) uf[s] = *(const LDS f16x8*)(ks + (lane & 31) * KROW + s * 32 + (lane >> 5) * 16);
      SB();
#pragma unroll
      for (int s = 0; s < 8; ++s) kd = __builtin_amdgcn_mfma_f32_32x32x16_f16(uf[s], vf[s], kd, 0, 0, 0);
    }
    kd *= ksc;
    const bool h = lane >> 5, odd = lane & 1;
    if (hh) {
      *(LDS f32x4*)(pr + KEX1 + lane * 16) = f32x4{kd[0], kd[1], kd[2], kd[3]};
      *(LDS f32x4*)(pr + KEX1 + 1024 + lane * 16) = f32x4{kd[4], kd[5], kd[6], kd[7]};
      *(LDS f32x4*)(pr + KEX1 + 2048 + lane * 16) = f32x4{kd[8], kd[13], kd[14], kd[15]};
    } else {
      *(LDS f32x4*)(pr + KEX0 + lane * 16) = f32x4{kd[9], kd[10], kd[11], kd[12]};
      *(LDS f32x2*)(pr + KEX0 + 1024 + lane * 8) = f32x2{kd[15], kd[1]};
    }
    STAMP(5);
    SPRIO(0);
    bar();
    STAMP(10);
    if (YMARK) {
#pragma unroll
      for (int kp = 0; kp < 2; ++kp) st16(u32x4{~0u, ~0u, ~0u, ~0u}, rws, vys, WS_YT + kp * 2048);
    }
    {
      u32x4 tb[5];
      auto tail = [&](float a, float c, float a3, float a4, float a5, int k) INL {
        const unsigned aa = pk2(a, a), cc = pk2(c, c);
        tb[k] = u32x4{cc, pk2(a5, a4), pk2(a3, a), aa};
        tb[k + 1] = u32x4{cc, pk2(c, a5), pk2(a4, a3), aa};
      };
      if (hh) {
        const f32x4 p = *(const LDS f32x4*)(pr + KEX0 + lane * 16);
        const f32x2 q = *(const LDS f32x2*)(pr + KEX0 + 1024 + lane * 8);
#pragma unroll
        for (int k = 0; k < 2; ++k) {
          const float a = one(kd[9 + 2 * k] + p[2 * k]), c = one(kd[10 + 2 * k] + p[2 * k + 1]), m = one(kd[k ? 1 : 15] + q[k]);
          tail(a, c, h ? m : a, h ? m : odd ? c : a, h ? m : c, 2 * k);
        }
        tb[4][0] = 0;
      } else {
        const f32x4 p0 = *(const LDS f32x4*)(pr + KEX1 + lane * 16), p1 = *(const LDS f32x4*)(pr + KEX1 + 1024 + lane * 16);
        const f32x4 p2 = *(const LDS f32x4*)(pr + KEX1 + 2048 + lane * 16);
        float t[9];
#pragma unroll
        for (int k = 0; k < 9; ++k) t[k] = one(kd[k] + (k < 4 ? p0[k] : k < 8 ? p1[k - 4] : p2[0]));
        t[0] += skip;
        t[1] = h ? t[0] : t[1];
        tb[0] = u32x4{pk2(t[7], t[6]), pk2(t[5], t[4]), pk2(t[3], t[2]), pk2(t[1], t[0])};
        tb[1] = u32x4{pk2(t[8], t[7]), pk2(t[6], t[5]), pk2(t[4], t[3]), pk2(t[2], t[1])};
        const float a = one(kd[13] + p2[1]), c = one(kd[14] + p2[2]);
        tail(a, c, a, odd ? c : a, c, 2);
        tb[4][0] = lane ? 0 : tb[0][3] >> 16;
      }
      tb[4][1] = tb[4][2] = tb[4][3] = 0;
      asm volatile("" : "+v"(tb[0]), "+v"(tb[1]), "+v"(tb[2]), "+v"(tb[3]), "+v"(tb[4]));
      STAMP(19);
#pragma unroll
      for (int k = 0; k < 4; ++k) *(LDS u32x4*)(lds + vtab[k >> 1] + (k & 1 ? TY : TX)) = tb[k];
      if (lane < (hh ? 5 : 36)) *(LDS u32x4*)(lds + vzero) = tb[4];
      part(2, 0, 10 - D2F);
    }
    if (TWP == 2) touch(DUMMY, vtw[0], rwd, 0);
    if (YMARK) { if (gemm) waitv<10 - D2F>(); else waitv<0>(); }
    STAMP(6);
    bar();
    __builtin_amdgcn_raw_buffer_store_b64(tg, rws, vxf, 0, SC1);
    stage(3);
    if (TWP == 1) touch(DUMMY, vtw[0], rwd, 0);
    waitv<10 + (TWP == 1)>();
    bar();
    stage(4);
    waitv<10>();
    bar();
    stage(5);
    waitv<10>();
    bar();
    stage(6);
    if (TW3) touch(DUMMY, vtw[1], rwd, 0);
    waitv<10 + (TW3 != 0)>();
    bar();
    {
      u64 fl;
      asm volatile("buffer_load_dwordx2 %0, %1, %2, %3 offen sc1" : "=v"(fl) : "v"(tid * 8), "s"(rws), "s"(WS_XNF) : "memory");
      stage(7);
      if (gemm) waitv<10>(); else waitv<0>();
      asm volatile("" : "+v"(fl));
      if (__builtin_expect(!wave_all(fl == tag), 0)) poll<4>(ws, WS_XNF + tid * 8, tag);
    }
    STAMP(7);
    bar();
#pragma unroll
    for (int i = 0; i < 2; ++i) dma16(cl * PAIR + hh * 2048, SBASE + CEX + i * 1024, vxt[i], rx, i * 8192);
    unsigned gst; u32x2 csg; u32x4 xi[2];
    asm volatile("buffer_load_dword %0, %1, %2, %3 offen sc1" : "=v"(gst) : "v"((tid & 127) * 4), "s"(rws), "s"(WS_ST + (mt & 15) * 512) : "memory");
    asm volatile("buffer_load_dwordx2 %0, %1, %2, %3 offen sc1" : "=v"(csg) : "v"(vcs), "s"(rws), "s"(WS_CS) : "memory");
#pragma unroll
    for (int i = 0; i < 2; ++i)
      asm volatile("buffer_load_dwordx4 %0, %1, %2, %3 offen sc1" : "=v"(xi[i]) : "v"(vxi), "s"(rws), "s"(WS_XNT + i * (128 * XNP)) : "memory");
#pragma unroll
    for (int i = 0; i < 4; ++i) issue_wd(0, i);
    waitv<10>();
    STAMP(8);
    bar();
    waitv<6>();
    asm volatile("" : "+v"(gst), "+v"(csg));
    if (tid < 128) *(LDS u32x2*)(lds + STO + tid * 8) = u32x2{gst & 0xffff0000u, __builtin_amdgcn_perm(gst, gst, 0x01000100)};
    if (__builtin_expect(gemm && !wave_all(csg[1] == (unsigned)tag), 0)) {
#pragma unroll 1
      for (unsigned spins = 0; spins < SPIN_CAP && !wave_all(csg[1] == (unsigned)tag); ++spins) {
        __builtin_amdgcn_s_sleep(2);
        asm volatile("buffer_load_dwordx2 %0, %1, %2, %3 offen sc1\n\ts_waitcnt vmcnt(0)" : "=&v"(csg) : "v"(vcs), "s"(rws), "s"(WS_CS) : "memory");
      }
    }
    if (tid < 192) *(LDS u32x2*)(lds + vco) = u32x2{csg[0], csg[0]};
#pragma unroll
    for (int i = 0; i < 4; ++i) issue_wd(1, i);
    STAMP(9);
    bar();
#pragma unroll
    for (int i = 8; i < 16; ++i) issue_wd(i >> 2, i & 3);
    waitv<8>();
    post(LANDED, 1);
    asm volatile("" : "+v"(xi[0]), "+v"(xi[1]));
#pragma unroll
    for (int i = 0; i < 2; ++i) *(LDS u32x4*)(lds + vimg + XPAD + i * 2048) = xi[i];
#pragma unroll
    for (int i = 16; i < 28; ++i) issue_wd(i >> 2, i & 3);
    waitv<12>();
    post(LANDED, 2);
    if (NPRE > 12) await(USED, 0, 1);
#pragma unroll
    for (int i = 28; i < 16 + NPRE; ++i) issue_wd(i >> 2, i & 3);
    LDS char* pb = pr + XIMG + XPAD + (16 * j16 + 8 * g - 16) * 2 - hh * 256 - 192;
    CvFrag cf[2];
    auto rdc = [&](CvFrag& f, int rr, int k) INL {
      const int i = k / 3, c = k % 3;
      if (c == 2) f.b[i] = *(const LDS s16x8*)(pb + i * 512 + (3 - rr) * 64);
      else f.a[2 * i + c] = *(const LDS s16x4u*)(lds + pa[i >> 1] + (i & 1 ? 0 : 512) + (3 - rr) * 64 + c * 8);
    };
#pragma unroll
    for (int k = 0; k < 24; ++k) if (k % 3 < 2) rdc(cf[0], 0, k);
    STAMP(18);
    bar();
#pragma unroll
    for (int k = 2; k < 24; k += 3) rdc(cf[0], 0, k);
    STAMP(11);
    SPRIO(CPRIO);
    unsigned seen = 0;
    f32x4 ca[8] = {};
    auto conv = [&](int rr) INL {
      int n = 0;
#pragma unroll
      for (int u = 0; u < 8; ++u)
#pragma unroll
        for (int t = u; t < 8; ++t) {
          ca[t] = mfma16(cat(cf[rr & 1].a[2 * u], cf[rr & 1].a[2 * u + 1]), cf[rr & 1].b[t - u], ca[t]);
          SB();
          if (n < 32 && n % 4 < 3) rdc(cf[(rr + 1) & 1], rr + 1, n / 4 * 3 + n % 4);
          const int q = rr * 36 + n;
          const int j = 16 + NPRE + (q - Q0) / PACE;
          if (q >= Q0 && (q - Q0) % PACE == 0 && j < 44) {
            if (j == 28 || j == 36) await(USED, seen, j == 28 ? 1 : 2);
            if (j == 27 || j == 35) seen = peek(USED);
            issue_wd(j >> 2, j & 3);
          }
          SB();
          ++n;
        }
    };
    conv(0); conv(1); conv(2);
    {
      unsigned o[4][2];
      auto last = [&](int h) INL {
        auto tile = [](int set, int k) INL { return (k >> 1) * 4 + (set ? 1 + (k & 1) : 3 * (k & 1)); };
        unsigned mate;
        f32x4 in[4];
#pragma unroll
        for (int ph = 0; ph < 2; ++ph) {
          int n = 0;
#pragma unroll
          for (int u = 0; u < 8; ++u)
#pragma unroll
            for (int k = 3; k >= 0; --k) {
              const int t = tile(ph ? h : 1 - h, k);
              if (t < u) continue;
              ca[t] = mfma16(cat(cf[1].a[2 * u], cf[1].a[2 * u + 1]), cf[1].b[t - u], ca[t]);
              SB();
              if (ph && n < 4) *(LDS f32x4*)(lds + vex[0] + n * 1024) = ca[tile(1 - h, n)];
              if (ph && n == 4) post(SYNC, 1);
              if (ph && n == 6) { waitv<8>(); post(LANDED, 3); }
              if (ph && n == 10) mate = *(volatile LDS unsigned*)(lds + SYNC + (w4 ^ 1) * 4);
              SB();
              ++n;
            }
        }
        STAMP(12);
        if (__builtin_expect(!rfl(mate), 0)) lds_wait(lds + SYNC + (w4 ^ 1) * 4, 1);
        fence();
        STAMP(20);
#pragma unroll
        for (int k = 0; k < 4; ++k) in[k] = *(const LDS f32x4*)(lds + vex[1] + k * 1024);
        SB();
#pragma unroll
        for (int k = 0; k < 4; ++k) {
          const f32x4 s = ca[tile(h, k)] + in[k];
          o[k][0] = min(pk2(s[0], s[1]), YMARK ? ~1u : ~0u); o[k][1] = min(pk2(s[2], s[3]), YMARK ? ~1u : ~0u);
        }
      };
      if (hh) last(1); else last(0);
      SPRIO(TPRIO);
#pragma unroll
      for (int kp = 0; kp < 2; ++kp) {
        const auto r0 = __builtin_amdgcn_permlane16_swap(o[2 * kp][0], o[2 * kp + 1][0], false, false);
        const auto r1 = __builtin_amdgcn_permlane16_swap(o[2 * kp][1], o[2 * kp + 1][1], false, false);
        const u32x4 ov = {r0[0], r1[0], r0[1], r1[1]};
        st16(ov, rws, vys, WS_YT + kp * 2048);
      }
      STAMP(13);
      if (!YMARK) { waitv<2>(); post(LANDED, 4); waitv<0>(); }
      STAMP(23);
      __builtin_amdgcn_raw_buffer_store_b64(tg, rws, vyf, 0, SC1);
      if (YMARK) { waitv<3>(); post(LANDED, 4); }
    }
    STAMP(14);
    __builtin_amdgcn_s_sleep(YSLEEP);
    const unsigned yf = WS_YF + ((m2 & (YREP - 5)) * 64 + n2 * 8 + w4 * 2) * YST;
    if (YMARK < 2 && !watch<YNAP, 2, 128>(ws + yf, tag)) poll<YNAP>(ws, yf + (j16 >> 3) * YST + (j16 & 7) * 8, tag);
    STAMP(15);
#pragma unroll 1
    for (unsigned spins = 0; spins < SPIN_CAP; ++spins) {
#pragma unroll
      for (int i = 0; i < 2; ++i) xi[i] = __builtin_amdgcn_raw_buffer_load_b128(rws, vyl, WS_YT + i * 32768, SC1);
      const u32x4 mx = __builtin_elementwise_max(xi[0], xi[1]);
      if (!YMARK || wave_all(max(max(mx[0], mx[1]), max(mx[2], mx[3])) != ~0u)) break;
      fence();
    }
#pragma unroll
    for (int i = 0; i < 2; ++i) *(LDS u32x4*)(lds + vyw + i * 1280) = xi[i];
    post(DONE + 16, 1);
    waitl0();
    STAMP(16);
    STAMP_OUT(1);
    asm volatile(".Lk_code_end1:");
  }
}

}

extern "C" void kernel_launch(void* const* d_in, const int* in_sizes, int n_in, void* d_out, int out_size, void* d_ws, size_t ws_size,
                              hipStream_t stream) {
  fused_kernel<<<256, 512, 0, stream>>>((const u16*)d_in[0], (const u16*)d_in[1], (const u16*)d_in[2], (const u16*)d_in[3], (const u16*)d_in[4],
                                        (const u16*)d_in[5], (const u16*)d_in[6], (const u16*)d_in[8], (const u16*)d_in[9], (const u16*)d_in[10],
                                        (u16*)d_out, (char*)d_ws);
}
```

```cpp
#include <hip/hip_runtime.h>
#include <hip/hip_bf16.h>

namespace {

typedef unsigned short u16;
typedef unsigned long long u64;
typedef __attribute__((ext_vector_type(4))) short s16x4;
typedef s16x4 __attribute__((aligned(4))) s16x4u;
typedef __attribute__((ext_vector_type(8))) short s16x8;
typedef __attribute__((ext_vector_type(8))) __bf16 bf16x8;
typedef __attribute__((ext_vector_type(2))) __bf16 bf16x2;
typedef __attribute__((ext_vector_type(8))) _Float16 f16x8;
typedef __attribute__((ext_vector_type(2))) _Float16 f16x2;
typedef __attribute__((ext_vector_type(2))) float f32x2;
typedef __attribute__((ext_vector_type(4))) float f32x4;
typedef __attribute__((ext_vector_type(16))) float f32x16;
typedef __attribute__((ext_vector_type(4))) unsigned u32x4;
typedef __attribute__((ext_vector_type(2))) unsigned u32x2;
typedef __amdgpu_buffer_rsrc_t rsrc_t;
#define LDS __attribute__((address_space(3)))
#define GLB __attribute__((address_space(1)))
#define INL __attribute__((always_inline))
#define SB() __builtin_amdgcn_sched_barrier(0)
#define RLX_AGENT __ATOMIC_RELAXED, __HIP_MEMORY_SCOPE_AGENT
#define RLX_WG __ATOMIC_RELAXED, __HIP_MEMORY_SCOPE_WORKGROUP

constexpr int L = 2048, D = 512, NM = 128, F = 1376, NU = F / 16;
constexpr int SC1 = 16;
constexpr int HF = 10;
constexpr int D2F = 6;
constexpr int RD0 = 1, RD1 = 3, B9Q = 5;
constexpr int NPRE = 16, Q0 = 3, PACE = 4;
constexpr int HSLEEP = 8, YSLEEP = 14, NAP = 2;
constexpr int YST = 128;
static_assert(Q0 + (27 - NPRE) * PACE < 108 && RD0 < RD1 && RD1 < B9Q && B9Q < 6, "schedule");
constexpr unsigned SPIN_CAP = 1u << 18;

constexpr int WBLK = 3104, GOFF = 16384, UOFF = GOFF + 4 * WBLK, SLOT = GOFF + 8 * WBLK;
constexpr int LEFT = 32768;
constexpr int LNB = 2 * SLOT, LNROW = 1088;
constexpr int SBASE = 3 * SLOT;
constexpr int KROW = 272, KSW = 32 * KROW, PAIR = 2 * KSW;
constexpr int TX = 0, XIMG = 4160, XPAD = 480, CEX = 8736, TY = 13248, KEX0 = 7168, KEX1 = KSW;
constexpr int STO = SBASE + 2 * PAIR, CSO = STO + 1024, SYNC = CSO + 1536, LANDED = SYNC + 16, USED = SYNC + 32, DONE = SYNC + 48, DUMMY = SYNC + 128;
constexpr int LDS_BYTES = 163840;
static_assert(DUMMY + 256 <= LDS_BYTES && XIMG + XPAD + 4096 <= CEX && CEX + 4096 <= TY && TY + 4128 <= PAIR, "LDS map");

constexpr int WS_E = 0, WS_XNF = 4096, WS_HF = 65536, WS_YF = 196608, WS_ST = 32768, WS_CS = 40960;
constexpr int XNP = 8320, WS_XNT = 1 << 19, WS_YT = 3 << 20, WS_H = 5 << 20, WS_END = WS_H + 64 * NU * 1024;
constexpr int CSP = 1408;

__device__ __forceinline__ float u2f(unsigned v) { return __builtin_bit_cast(float, v); }
__device__ __forceinline__ unsigned f2u(float v) { return __builtin_bit_cast(unsigned, v); }
__device__ __forceinline__ bool wave_all(bool p) { return __builtin_amdgcn_ballot_w64(!p) == 0; }
__device__ __forceinline__ unsigned rfl(unsigned v) { return __builtin_amdgcn_readfirstlane(v); }
__device__ __forceinline__ rsrc_t mkr(const void* p, unsigned n) { return __builtin_amdgcn_make_buffer_rsrc((void*)p, 0, n, 0x00020000); }
__device__ __forceinline__ void dma16(unsigned base, int dst, unsigned voff, rsrc_t r, unsigned soff, int off = 0) {
  asm volatile("s_add_u32 m0, %0, %1\n\ts_nop 0\n\tbuffer_load_dwordx4 %2, %3, %4 offen offset:%5 lds"
               ::"s"(base), "n"(dst - off), "v"(voff), "s"(r), "s"(soff), "n"(off) : "scc", "memory");
}
__device__ __forceinline__ void touch(unsigned m0v, unsigned voff, rsrc_t r, unsigned soff) {
  asm volatile("s_mov_b32 m0, %0\n\ts_nop 0\n\tbuffer_load_dword %1, %2, %3 offen lds" ::"s"(m0v), "v"(voff), "s"(r), "s"(soff) : "memory");
}
__device__ __forceinline__ void st16(u32x4 v, rsrc_t r, unsigned voff, unsigned soff) {
  asm volatile("buffer_store_dwordx4 %0, %1, %2, %3 offen sc1\n\ts_nop 1" ::"v"(v), "v"(voff), "s"(r), "s"(soff) : "memory");
}
template <int W> __device__ __forceinline__ void waitv() { asm volatile("s_waitcnt vmcnt(%0)" ::"n"(W) : "memory"); }
__device__ __forceinline__ void waitl0() { asm volatile("" ::: "memory"); __builtin_amdgcn_s_waitcnt(0xc07f); asm volatile("" ::: "memory"); }
__device__ __forceinline__ void fence() { asm volatile("" ::: "memory"); }
__device__ __forceinline__ void bar() { waitl0(); __builtin_amdgcn_s_barrier(); fence(); }
__device__ __forceinline__ unsigned pk2(float a, float b) { f32x2 p = {a, b}; return __builtin_bit_cast(unsigned, __builtin_convertvector(p, bf16x2)); }
__device__ __forceinline__ unsigned pkh(float a, float b) { f32x2 p = {a, b}; return __builtin_bit_cast(unsigned, __builtin_convertvector(p, f16x2)); }
__device__ __forceinline__ float blo(unsigned v) { return u2f(v << 16); }
__device__ __forceinline__ float bhi(unsigned v) { return u2f(v & 0xffff0000u); }
__device__ __forceinline__ float rb(float a) { return blo(pk2(a, a)); }
template <int C, int RM> __device__ __forceinline__ float dppf(float v) {
  return __builtin_bit_cast(float, __builtin_amdgcn_update_dpp(0, __builtin_bit_cast(int, v), C, RM, 0xf, true));
}
__device__ __forceinline__ float lane63(float v) { return __builtin_bit_cast(float, __builtin_amdgcn_readlane(__builtin_bit_cast(int, v), 63)); }
__device__ __forceinline__ float wave_sum(float v) {
  v += dppf<0x111, 0xf>(v); v += dppf<0x112, 0xf>(v); v += dppf<0x114, 0xf>(v); v += dppf<0x118, 0xf>(v);
  v += dppf<0x142, 0xa>(v); v += dppf<0x143, 0xc>(v);
  return lane63(v);
}
template <int C, int RM> __device__ __forceinline__ unsigned dppmax(unsigned v) {
  const unsigned o = __builtin_amdgcn_update_dpp(0, v, C, RM, 0xf, true);
  return v > o ? v : o;
}
__device__ __forceinline__ unsigned wave_max(unsigned v) {
  v = dppmax<0x111, 0xf>(v); v = dppmax<0x112, 0xf>(v); v = dppmax<0x114, 0xf>(v); v = dppmax<0x118, 0xf>(v);
  v = dppmax<0x142, 0xa>(v); v = dppmax<0x143, 0xc>(v);
  return __builtin_amdgcn_readlane(v, 63);
}
__device__ __forceinline__ s16x8 cat(s16x4 a, s16x4 b) { return __builtin_shufflevector(a, b, 0, 1, 2, 3, 4, 5, 6, 7); }
__device__ __forceinline__ s16x4 trd(LDS char* p) { return __builtin_amdgcn_ds_read_tr16_b64_v4i16((LDS s16x4*)p); }
__device__ __forceinline__ f32x4 mfma16(s16x8 a, s16x8 b, f32x4 c) {
  return __builtin_amdgcn_mfma_f32_16x16x32_bf16(__builtin_bit_cast(bf16x8, a), __builtin_bit_cast(bf16x8, b), c, 0, 0, 0);
}
template <int S> __device__ __forceinline__ void poll(const char* ws, unsigned off, u64 tag) {
  GLB u64* p = (GLB u64*)(ws + off);
#pragma unroll 1
  for (unsigned spins = 0; spins < SPIN_CAP; ++spins) {
    if (wave_all(__hip_atomic_load(p, RLX_AGENT) == tag)) break;
    __builtin_amdgcn_s_sleep(S);
  }
  fence();
}
typedef __attribute__((ext_vector_type(8))) unsigned long long u64x8;
template <int N> __device__ __forceinline__ bool watch(const char* p, u64 tag) {
#pragma unroll 1
  for (unsigned spins = 0; spins < 128; ++spins) {
    u64x8 f[2];
    u64 d = 0;
    if constexpr (N == 1) asm volatile("s_load_dwordx16 %0, %1, 0 glc\n\ts_waitcnt lgkmcnt(0)" : "=&s"(f[0]) : "s"(p) : "memory");
    else asm volatile("s_load_dwordx16 %0, %2, 0 glc\n\ts_load_dwordx16 %1, %2, %3 glc\n\ts_waitcnt lgkmcnt(0)" : "=&s"(f[0]), "=&s"(f[1]) : "s"(p), "n"(YST) : "memory");
#pragma unroll
    for (int i = 0; i < 8 * N; ++i) { d |= f[i >> 3][i & 7] ^ tag; asm("" : "+s"(d)); }
    if (d == 0) return true;
    __builtin_amdgcn_s_sleep(NAP);
  }
  return false;
}
__device__ __forceinline__ void lds_wait(LDS char* p, unsigned v) {
#pragma unroll 1
  for (unsigned spins = 0; spins < SPIN_CAP; ++spins) { if (rfl(*(volatile LDS unsigned*)p) >= v) break; __builtin_amdgcn_s_sleep(1); }
  fence();
}
__device__ __forceinline__ void setflag(char* ws, unsigned off, u64 tag, int lane) {
  if (lane == 0) __hip_atomic_store((GLB u64*)(ws + off), tag, RLX_AGENT);
}
__device__ __forceinline__ f32x2 fma2(f32x2 a, f32x2 b, f32x2 c) { return __builtin_elementwise_fma(a, b, c); }
__device__ __forceinline__ float one(float v) { asm("" : "+v"(v)); return v; }
__device__ __forceinline__ f32x2 dup(float a) { return f32x2{a, a}; }
struct cmulr { f32x2 z, rot; };
__device__ __forceinline__ cmulr mul_by(f32x2 z) { return {z, f32x2{-z[1], z[0]}}; }
__device__ __forceinline__ f32x2 cmul(f32x2 a, cmulr m) { return fma2(dup(a[0]), m.z, dup(a[1]) * m.rot); }
__device__ __forceinline__ f32x2 cpow2(float s2, float th, float p) {
  const float t = th * p, n = __builtin_rintf(t * 0.15915494f);
  const float fr = __builtin_fmaf(t, 0.15915494f, -n) + t * 6.4206382e-9f;
  const float e = __builtin_amdgcn_exp2f(s2 * p);
  return f32x2{e * __builtin_amdgcn_cosf(fr), e * __builtin_amdgcn_sinf(fr)};
}
__device__ __forceinline__ float exp_f(float v) {
  const float t = v * 1.44269504f, e = __builtin_fmaf(v, 1.925963033e-8f, __builtin_fmaf(v, 1.44269504f, -t));
  const float y = __builtin_amdgcn_exp2f(t);
  return __builtin_fmaf(y, e * 0.69314718f, y);
}


struct G1Frag { s16x8 x[4]; s16x4 w[12]; };
struct CvFrag { s16x4 a[16]; s16x8 b[8]; };

__global__ __launch_bounds__(512, 2) void fused_kernel(const u16* x, const u16* lar, const u16* aim, const u16* cre, const u16* cim, const u16* ldt,
                                                       const u16* dsk, const u16* wg, const u16* wu, const u16* wd, u16* out, char* ws) {
  __shared__ __attribute__((aligned(16))) char lds_raw[LDS_BYTES];
  LDS char* lds = (LDS char*)lds_raw;
  int w = rfl(threadIdx.x >> 6);
  const int lane = threadIdx.x & 63, tid = threadIdx.x & 255;
  const int b = blockIdx.x, xcd = b & 7, r = b >> 3;
  if (w == 7) {
    u64 pc; unsigned l0, l1;
    asm volatile("s_getpc_b64 %0\n.Lk_code_begin:\n\ts_mov_b32 %1, .Lk_code_end0-.Lk_code_begin\n\ts_mov_b32 %2, .Lk_code_end1-.Lk_code_begin" : "=s"(pc), "=s"(l0), "=s"(l1));
    touch(DUMMY, lane * 1024, mkr((const void*)pc, max(l0, l1) - 896), (r & 7) * 128);
  }
  const int row0 = (xcd & 1) * 1024 + (xcd >> 1) * 256 + r * 8;
  const unsigned vt = lane < 16 ? (lane >> 3) * (64 * F * 2) + (lane & 7) * 96 : 0x7f000000u;
  const unsigned so_t = ((w & 3) * 128 + (xcd & 1) * 32 + r) * (F * 2) + (xcd >> 1) * 688;
  asm volatile("" ::"s"(row0), "v"(vt), "s"(so_t));
  asm volatile("" : "+s"(x), "+s"(lar), "+s"(aim), "+s"(cre), "+s"(cim), "+s"(ldt), "+s"(dsk), "+s"(wg), "+s"(wu), "+s"(wd), "+s"(out), "+s"(ws));
  const rsrc_t rx = mkr(x, L * D * 2);
  const int w4 = w & 3, mw = w & 1, nw = w4 >> 1;
  const int cp = xcd * 32 + r, ch = cp * 2 + nw;
  u32x4 xr[2];
  u64 tag;
  if (w < 4) {
#pragma unroll
    for (int i = 0; i < 2; ++i)
      asm volatile("buffer_load_dwordx4 %0, %1, %2, %3 offen" : "=v"(xr[i]) : "v"(lane * 16), "s"(rx), "s"((row0 + 2 * w4 + i) * 1024) : "memory");
  } else {
    const unsigned pi = (ch * NM + mw * 64 + lane) * 2;
    asm volatile("global_load_ushort %0, %6, %8\n\tglobal_load_ushort %1, %7, %9\n\tglobal_load_ushort %2, %7, %10\n\tglobal_load_ushort %3, %7, %11\n\t"
                 "global_load_ushort %4, %7, %12\n\tglobal_load_ushort %5, %6, %13"
                 : "=&v"(xr[0][0]), "=&v"(xr[0][1]), "=&v"(xr[0][2]), "=&v"(xr[0][3]), "=&v"(xr[1][0]), "=&v"(xr[1][1])
                 : "v"(ch * 2), "v"(pi), "s"(ldt), "s"(lar), "s"(aim), "s"(cre), "s"(cim), "s"(dsk) : "memory");
  }
  u64 epoch;
  asm volatile("s_load_dwordx2 %0, %1, 0x0" : "=s"(epoch) : "s"(ws) : "memory");
  __builtin_amdgcn_s_barrier();
  touch(DUMMY, vt, mkr(w >> 2 ? wu : wg, D * F * 2), so_t);
  auto get_tag = [&]() INL {
    unsigned lo, hi;
    asm volatile("s_waitcnt lgkmcnt(0)\n\ts_add_u32 %0, %2, 0x7F4A7C15\n\ts_addc_u32 %1, %3, 0x9E3779B9"
                 : "=&s"(lo), "=s"(hi) : "s"((unsigned)epoch), "s"((unsigned)(epoch >> 32)) : "scc", "memory");
    tag = (u64)hi << 32 | lo;
  };
  asm volatile("" : "+s"(w));
  SB();

  const rsrc_t rws = mkr(ws, WS_END), rwd = mkr(wd, F * D * 2);
  const bool gemm = r < 30;
  const int idx = (xcd >> 1) * 30 + r, nt = idx >> 3, mt = (xcd & 1) * 8 + (idx & 7);
  const int n0 = nt < 14 ? nt * 96 : 1280;
  const int m2 = (xcd & 1) * 16 + (xcd >> 1) * 4 + (r >> 3), n2 = r & 7;
  const int j16 = lane & 15, g = lane >> 4;

  const rsrc_t rw = mkr((gemm ? nw : xcd >> 2) ? wu : wg, D * F * 2);
  unsigned vx[2], vw[3], lx = w4 * 4096, lw = (nw ? UOFF : GOFF) + mw * 2 * WBLK;
  asm volatile("" : "+s"(lx), "+s"(lw));
  auto addr_x = [&]() INL {
#pragma unroll
    for (int i = 0; i < 2; ++i) {
      const int row = i * 8 + (lane >> 3), id = (row >> 1) & 7, f = (id & 4) | ((id & 1) << 1) | ((id >> 1) & 1);
      vx[i] = (mt * 128 + w4 * 32 + row) * 1024 + (((lane & 7) ^ f) << 4);
    }
  };
  auto addr_w = [&]() INL {
    const int kr = (lane * 171) >> 11, c = lane - kr * 12;
    vw[0] = (mw * 32 + kr) * (F * 2) + c * 16 + n0 * 2;
    vw[1] = vw[0] + (c >= 8 ? 6 * F * 2 - 128 : 5 * F * 2 + 64);
    vw[2] = vw[0] + (c >= 4 ? 11 * F * 2 - 64 : 10 * F * 2 + 128);
  };
  auto issue = [&](int s, int i) INL {
    const int slot = (s % 3) * SLOT, j = i - 4;
    if (i < 4) dma16(lx, slot + i * 1024, vx[i & 1], rx, s * 128 + (i >> 1) * 16384);
    else dma16(lw, slot + (j / 3) * WBLK + (j % 3) * 1024, vw[j % 3], rw, s * (64 * F * 2) + (j / 3) * 16 * F * 2);
  };
  const unsigned wl = w4 * 8192, wl6 = (nw ? 0 : 2 * SLOT) + LEFT + mw * 4096;
  auto wdloc = [](int pos) INL { return ((pos / 2 + 2) % 3) * SLOT + (pos & 1) * 4096; };

  auto post = [&](int word, unsigned v) INL { fence(); *(volatile LDS unsigned*)(lds + word + w4 * 4) = v; fence(); };
  auto peek = [&](int word) INL { return *(volatile LDS unsigned*)(lds + word + w4 * 4); };
  auto await = [&](int word, unsigned seen, unsigned v) INL { if (__builtin_expect(rfl(seen) < v, 0)) lds_wait(lds + word + w4 * 4, v); fence(); };

  if (__builtin_expect(w < 4, 1)) {
    auto layernorm = [&](auto&& hook) INL {
      get_tag();
      u32x2 stat;
#pragma unroll
      for (int i = 0; i < 2; ++i) {
        f32x2 v[4];
#pragma unroll
        for (int e = 0; e < 4; ++e) asm volatile("v_lshlrev_b32 %0, 16, %2\n\tv_and_b32 %1, 0xffff0000, %2" : "=&v"(v[e][0]), "=v"(v[e][1]) : "v"(xr[i][e]));
        hook(5 * i);
        const float x0 = u2f(rfl(f2u(v[0][0])));
#pragma unroll
        for (int e = 0; e < 4; ++e) v[e] -= dup(x0);
        const f32x2 s1 = (v[0] + v[1]) + (v[2] + v[3]), s2 = fma2(v[0], v[0], v[1] * v[1]) + fma2(v[2], v[2], v[3] * v[3]);
        float t1 = s1[0] + s1[1], t2 = s2[0] + s2[1];
        asm volatile("" : "+v"(t1), "+v"(t2));
        hook(5 * i + 1);
        const float md = wave_sum(t1) * (1.f / D);
        float var = __builtin_fmaf(-md, md, wave_sum(t2) * (1.f / D));
        asm volatile("" : "+v"(var));
        hook(5 * i + 2);
        const float m = rb(x0 + md), sd = rb(__builtin_amdgcn_sqrtf(rb(rb(__builtin_fmaxf(var, 0.f)) + 1e-5f))), rs = __builtin_amdgcn_rcpf(sd);
        float c = (x0 - m) * rs;
        asm volatile("" : "+v"(c));
        hook(5 * i + 3);
        u32x4 o;
#pragma unroll
        for (int e = 0; e < 4; ++e) { const f32x2 n = fma2(v[e], dup(rs), dup(c)); o[e] = pk2(n[0], n[1]); }
        *(LDS u32x4*)(lds + LNB + (2 * w4 + i) * LNROW + lane * 16) = o;
        hook(5 * i + 4);
        stat[i] = pk2(m, sd);
      }
      __builtin_amdgcn_raw_buffer_store_b64(stat, rws, lane ? 0x7f000000u : 0u, WS_ST + (row0 + 2 * w4) * 4, SC1);
      bar();
#pragma unroll
      for (int i = 0; i < 2; ++i) {
        LDS char* p = lds + LNB + (j16 >> 2) * LNROW + (i * 256 + w4 * 64 + g * 16 + (j16 & 3) * 4) * 2;
        st16(__builtin_bit_cast(u32x4, cat(trd(p), trd(p + 4 * LNROW))), rws, tid * 16, WS_XNT + (row0 >> 3) * XNP + i * 4096);
      }
    };
    unsigned ad[3][4], vh[4], vpoll, kh, kh10, a_xt, vflag = 0x7f000000u;
    u32x2 xt[4];
    auto rdxt = [&](int u) INL { xt[u] = *(const LDS u32x2*)(lds + SBASE + CEX - 32 * 1024 + (a_xt ^ (u * 32))); };
    s16x4 wf[11][8];
    auto wd_addr = [&](int i) INL {
      const int q = j16 >> 2, f = ((q >> 1) & 1) + 2 * (g & 1);
#pragma unroll
      for (int u = 0; u < 4; ++u) {
        ad[i][u] = (i == 0 ? wl : i == 1 ? wl + 2 * SLOT : wl6) + (8 * g + q) * 128 + ((u ^ f) << 5) + (j16 & 3) * 8;
        asm volatile("" : "+v"(ad[i][u]));
      }
    };
    auto rdwh = [&](int s, int u, int h) INL {
      const int pos = s % 7;
      LDS char* p = lds + (pos == 6 ? ad[2][u] : pos < 2 ? ad[1][u] + pos * 4096 : ad[0][u] + wdloc(pos));
      wf[s][2 * u + h] = trd(p + h * 512);
    };
    auto rdwd = [&](int s, int u) INL { rdwh(s, u, 0); rdwh(s, u, 1); };
    auto h_addr = [&]() INL {
      wd_addr(0); wd_addr(1); wd_addr(2);
#pragma unroll
      for (int m = 0; m < 4; ++m) { const int mm = (m + w4) & 3; vh[m] = (mm >> 1) * (NU * 1024) + (g >> 1) * 1024 + (32 * (g & 1) + 16 * (mm & 1) + j16) * 16; }
      vpoll = WS_HF + (m2 * 4 + w4) * 1024;
      a_xt = 32 * 1024 + nw * PAIR + mw * 2048 + j16 * 128 + (((g >> 1) ^ (j16 >> 1)) << 4) + (g & 1) * 8;
      asm volatile("" : "+v"(a_xt), "+v"(vh[0]), "+v"(vh[1]), "+v"(vh[2]), "+v"(vh[3]));
      kh = WS_H + m2 * 2 * NU * 1024 + w4 * (11 * 2048);
      kh10 = w4 == 3 ? 0x7f000000u : kh + 10 * 2048;
      asm volatile("" : "+s"(kh), "+s"(kh10), "+s"(vpoll));
    };

    if (gemm) {
      addr_x();
      addr_w();
      asm volatile("" : "+v"(vx[0]), "+v"(vx[1]), "+v"(vw[0]), "+v"(vw[1]), "+v"(vw[2]));
      unsigned ax[4], aw[2];
      {
        const int id = (j16 >> 1) & 7, f = (id & 4) | ((id & 1) << 1) | ((id >> 1) & 1), cg = 2 * (g & 1) + (g >> 1);
#pragma unroll
        for (int s = 0; s < 2; ++s) { ax[s] = (mw * 64 + j16) * 128 + (((4 * s + cg) ^ f) << 4); ax[s + 2] = ax[s] + 2 * SLOT; }
        aw[0] = GOFF + (g & 1) * WBLK + (8 * (g >> 1) + (j16 >> 2)) * 192 + nw * 96 + (j16 & 3) * 8;
        aw[1] = aw[0] + 2 * SLOT;
      }
      unsigned a_st = STO + (mw * 64 + j16) * 8, a_cs = CSO + (nw * 48 + j16) * 16, so_h[3];
#pragma unroll
      for (int u = 0; u < 3; ++u) {
        const int unit = (n0 >> 4) + nw * 3 + u;
        so_h[u] = nt < 14 || unit >= 84 ? WS_H + ((mt * 4 + mw * 2) * NU + unit) * 1024 : 0x7f000000u;
      }
      const unsigned fi = (nt < 14 ? nt * 2 + nw : nw ? 28 : 63) - 7 * (lane >> 3);
      vflag = lane < 32 && (lane & 7) == 0 && fi < 8 ? WS_HF + (mt * 2 + mw) * 4096 + lane * 128 + fi * 8 : 0x7f000000u;
      asm volatile("" : "+v"(a_st), "+v"(a_cs), "+s"(so_h[0]), "+s"(so_h[1]), "+s"(so_h[2]), "+v"(vflag));
      asm volatile("" : "+v"(ax[0]), "+v"(ax[1]), "+v"(ax[2]), "+v"(ax[3]), "+v"(aw[0]), "+v"(aw[1]));
      h_addr();
      SB();
#pragma unroll
      for (int i = 0; i < 10; ++i) issue(0, i);
      waitv<11>();
      layernorm([&](int k) INL { issue(1, k); });
#pragma unroll
      for (int i = 10 - D2F; i < 10; ++i) issue(2, i);
      f32x4 acc[4][6] = {};
      G1Frag fr[2];
      auto half = [&](G1Frag& c, bool domm, G1Frag& n, bool dord, int s, int jj) INL {
#pragma unroll
        for (int t = 0; t < 24; ++t) {
          if (domm) acc[t & 3][t >> 2] = mfma16(cat(c.w[t >> 2 << 1], c.w[(t >> 2 << 1) + 1]), c.x[t & 3], acc[t & 3][t >> 2]);
          SB();
          const int hi = s % 3 == 2, so = s % 3 == 1 ? SLOT : 0;
#pragma unroll
          for (int k = t; k < min(t + 1, 16); ++k) {
            const int u = (k - 4) >> 1;
            if (k < 4) n.x[k] = *(const LDS s16x8*)(lds + so + ax[jj + 2 * hi] + k * 2048);
            else n.w[k - 4] = trd(lds + aw[hi] + so + jj * 2 * WBLK + (u / 3) * 4 * WBLK + (u % 3) * 32 + (k & 1) * 768);
          }
          SB();
        }
      };
#pragma unroll
      for (int s = 0; s < 8; ++s) {
        if (s == 0) waitv<13 + D2F>(); else if (s == 1) waitv<D2F>(); else if (s == 2) waitv<0>();
        bar();
        half(fr[1], s > 0, fr[0], true, s, 0);
        half(fr[0], true, fr[1], true, s, 1);
      }
      bar();
      u32x2 st[4], bm[4]; s16x8 cs[3];
      float kg[4];
#pragma unroll
      for (int t = 0; t < 24; ++t) {
        acc[t & 3][t >> 2] = mfma16(cat(fr[1].w[t >> 2 << 1], fr[1].w[(t >> 2 << 1) + 1]), fr[1].x[t & 3], acc[t & 3][t >> 2]);
        SB();
        if (t < 4) st[t] = *(const LDS u32x2*)(lds + a_st + t * 128);
        else if (t < 7) cs[t - 4] = *(const LDS s16x8*)(lds + a_cs + (t - 4) * 256);
        else if (t < 11) rdxt(t - 7);
        else if (t >= 12 && t < 16) { bm[t - 12] = u32x2{g ? 0 : st[t - 12][1], 0}; asm volatile("" : "+v"(bm[t - 12])); }
        else if (t >= 16 && t < 20) { kg[t - 16] = -1.44269504f * __builtin_amdgcn_rcpf(u2f(st[t - 16][0])); asm volatile("" : "+v"(kg[t - 16])); }
        SB();
      }
      auto mean = [&](int t) INL {
        const int m = (t & 1) + 2 * (t / 12), u = (t >> 2) % 3, up = (t >> 1) & 1;
        const s16x4 c = up ? __builtin_shufflevector(cs[u], cs[u], 4, 5, 6, 7) : __builtin_shufflevector(cs[u], cs[u], 0, 1, 2, 3);
        acc[m][u + 3 * up] = __builtin_amdgcn_mfma_f32_16x16x16bf16_1k(c, __builtin_bit_cast(s16x4, bm[m]), acc[m][u + 3 * up], 0, 0, 0);
      };
#pragma unroll
      for (int t = 0; t < 24; ++t) { mean(t); SB(); }
      unsigned seen;
#pragma unroll
      for (int mp = 0; mp < 2; ++mp) {
#pragma unroll
        for (int u = 0; u < 3; ++u) {
          unsigned o[2][2];
          const int q = mp * 3 + u;
          if (q == B9Q) bar();
          if (q == RD0 || q == RD1) {
            await(LANDED, seen, q == RD0 ? 1 : 2);
#pragma unroll
            for (int k = 0; k < 8; ++k) rdwd((q == RD0 ? 0 : 2) + (k >> 2), k & 3);
            post(USED, q == RD0 ? 1 : 2);
          }
          if (q == RD0 - 1 || q == RD1 - 1) { seen = peek(LANDED); SB(); }
#pragma unroll
          for (int mi = 0; mi < 2; ++mi) {
            const int m = mp * 2 + mi;
            const float sd = u2f(st[m][0]);
#pragma unroll
            for (int e = 0; e < 2; ++e) {
              const f32x2 gg = f32x2{acc[m][u][2 * e], acc[m][u][2 * e + 1]} * dup(kg[m]);
              const f32x2 dn = fma2(f32x2{__builtin_amdgcn_exp2f(gg[0]), __builtin_amdgcn_exp2f(gg[1])}, dup(sd), dup(sd));
              const f32x2 hv = f32x2{acc[m][u + 3][2 * e], acc[m][u + 3][2 * e + 1]} * f32x2{__builtin_amdgcn_rcpf(dn[0]), __builtin_amdgcn_rcpf(dn[1])};
              o[mi][e] = pk2(hv[0], hv[1]);
            }
          }
          const auto r0 = __builtin_amdgcn_permlane16_swap(o[0][0], o[1][0], false, false);
          const auto r1 = __builtin_amdgcn_permlane16_swap(o[0][1], o[1][1], false, false);
          const u32x4 ov = {r0[0], r1[0], r0[1], r1[1]};
          st16(ov, rws, lane * 16 + mp * (NU * 1024), so_h[u]);
          SB();
        }
      }
    } else {
      const int cj = (xcd & 3) * 2 + (r - 30), kg = (tid * 745) >> 14, cc = tid - kg * 22, col = cj * 176 + cc * 8;
      const unsigned vo = (kg < 11 && col < F) ? kg * (F * 2) + col * 2 : 0x7f000000u;
      u32x4 cv[2][8];
      auto ldcs = [&](int sl) INL {
#pragma unroll
        for (int i = 0; i < 8; ++i) cv[sl & 1][i] = __builtin_amdgcn_raw_buffer_load_b128(rw, vo + (sl * 8 + i) * (11 * F * 2), 0, 0);
      };
      ldcs(0);
      waitv<9>();
      layernorm([](int) INL {});
      f32x2 a[4] = {};
#pragma unroll
      for (int sl = 0; sl < 6; ++sl) {
        if (sl == 1) waitv<0>();
        if (sl < 5) ldcs(sl + 1);
        if (sl < 4) bar();
#pragma unroll
        for (int i = 0; i < 8; ++i)
#pragma unroll
          for (int e = 0; e < 4; ++e) { const unsigned q = cv[sl & 1][i][e]; a[e] += f32x2{blo(q), bhi(q)}; }
        asm volatile("" : "+v"(a[0]), "+v"(a[1]), "+v"(a[2]), "+v"(a[3]));
      }
      if (kg < 11) {
        *(LDS f32x4*)(lds + kg * 704 + cc * 32) = f32x4{a[0][0], a[0][1], a[1][0], a[1][1]};
        *(LDS f32x4*)(lds + kg * 704 + cc * 32 + 16) = f32x4{a[2][0], a[2][1], a[3][0], a[3][1]};
      }
      bar();
      if (tid < 176) {
        float s = 0.f;
#pragma unroll
        for (int k = 0; k < 11; ++k) s += *(const LDS float*)(lds + k * 704 + tid * 4);
        __builtin_amdgcn_raw_buffer_store_b64(u32x2{pk2(-s, rb(s) - s), (unsigned)tag}, rws, tid * 8, WS_CS + ((xcd >> 2) * CSP + cj * 176) * 8, SC1);
      }
      h_addr();
      bar(); bar(); bar(); bar();
#pragma unroll
      for (int u = 0; u < 4; ++u) rdxt(u);
      await(LANDED, 0, 2);
#pragma unroll
      for (int k = 0; k < 16; ++k) rdwd(k >> 2, k & 3);
      post(USED, 2);
      bar();
    }

    f32x4 a2[4][4] = {};
#pragma unroll
    for (int u = 0; u < 4; ++u) a2[0][u] = f32x4{blo(xt[u][0]), bhi(xt[u][0]), blo(xt[u][1]), bhi(xt[u][1])};
    unsigned a_p[3], a_y = SBASE + (4 * g + (j16 >> 2)) * 160 + w4 * 32 + (j16 & 3) * 8, a_o[2], v_o = (lane >> 3) * 1024 + (lane & 7) * 16;
#pragma unroll
    for (int m = 1; m < 4; ++m) a_p[m - 1] = wdloc(m - 1) + ((w4 - m) & 3) * 8192 + lane * 16;
    a_o[0] = wdloc(4) + wl + j16 * 144 + g * 8;
    a_o[1] = wdloc(4) + wl + (lane >> 3) * 144 + (lane & 7) * 16;
    asm volatile("" : "+v"(a2[0][0]), "+v"(a2[0][1]), "+v"(a2[0][2]), "+v"(a2[0][3]));
    asm volatile("" : "+v"(a_p[0]), "+v"(a_p[1]), "+v"(a_p[2]), "+v"(a_y), "+v"(a_o[0]), "+v"(a_o[1]), "+v"(v_o));
    waitv<0>();
    __builtin_amdgcn_raw_buffer_store_b64(u32x2{(unsigned)tag, (unsigned)(tag >> 32)}, rws, vflag, 0, SC1);
    __builtin_amdgcn_s_sleep(HSLEEP);
    if (!watch<1>(ws + vpoll, tag)) poll<NAP>(ws, vpoll + (lane & 7) * 8, tag);
    auto fs = [](int n) INL { return n < 32 ? n >> 2 : 8 + (n - 32) % 3; };
    auto fm = [](int n) INL { return ((n < 32 ? n : (n - 32) / 3) + 1) & 3; };
    s16x8 hf[44];
    auto ldh = [&](int n) INL {
      hf[n] = __builtin_bit_cast(s16x8, __builtin_amdgcn_raw_buffer_load_b128(rws, vh[fm(n)] + (fs(n) & 1) * 2048, fs(n) == 10 ? kh10 : kh + (fs(n) >> 1) * 4096, SC1));
    };
#pragma unroll
    for (int n = 0; n < HF; ++n) { ldh(n); SB(); }
    unsigned landed;
    u32x4 dn[2];
    SB();
#pragma unroll
    for (int n = 0; n < 44; ++n) {
      if (n == 2 || n == 22) landed = peek(LANDED);
      if (n == 4 || n == 24) await(LANDED, landed, n == 4 ? 3 : 4);
#pragma unroll
      for (int u = 0; u < 4; ++u) {
        a2[fm(n)][u] = mfma16(cat(wf[fs(n)][2 * u], wf[fs(n)][2 * u + 1]), hf[n], a2[fm(n)][u]);
        SB();
        if (u < 2 && n >= 4 && n < 32) rdwh((n >> 2) + 3, n & 3, u);
        if (u == 3 && n + HF < 44) ldh(n + HF);
        if (n >= 35 && (n - 35) % 3 == 0) *(LDS f32x4*)(lds + wdloc((n - 35) / 3) + wl + u * 1024 + lane * 16) = a2[(n - 35) / 3 + 1][u];
        SB();
      }
      if (n == 41) post(DONE, 1);
      if (n == 42) { dn[0] = *(volatile LDS u32x4*)(lds + DONE); dn[1] = *(volatile LDS u32x4*)(lds + DONE + 16); SB(); }
    }
    auto all4 = [](u32x4 d) INL { return rfl(d[0] & d[1] & d[2] & d[3]); };
#pragma unroll 1
    for (unsigned spins = 0; spins < SPIN_CAP && !all4(dn[0]); ++spins) {
      dn[0] = *(volatile LDS u32x4*)(lds + DONE);
      dn[1] = *(volatile LDS u32x4*)(lds + DONE + 16);
    }
    fence();
    f32x4 pp[4][3];
    u32x2 y[4];
    auto rdy = [&]() INL {
#pragma unroll
      for (int u = 0; u < 4; ++u) y[u] = __builtin_bit_cast(u32x2, trd(lds + a_y + (u >> 1) * PAIR + (u & 1) * 4096));
    };
#pragma unroll
    for (int u = 0; u < 4; ++u)
#pragma unroll
      for (int m = 0; m < 3; ++m) pp[u][m] = *(const LDS f32x4*)(lds + a_p[m] + u * 1024);
    rdy();
    SB();
#pragma unroll
    for (int u = 0; u < 4; ++u) a2[0][u] += (pp[u][0] + pp[u][1]) + pp[u][2];
    asm volatile("" : "+v"(a2[0][0]), "+v"(a2[0][1]), "+v"(a2[0][2]), "+v"(a2[0][3]));
    if (__builtin_expect(!all4(dn[1]), 0)) {
#pragma unroll 1
      for (unsigned spins = 0; spins < SPIN_CAP && !all4(*(volatile LDS u32x4*)(lds + DONE + 16)); ++spins) {}
      fence();
      rdy();
    }
#pragma unroll
    for (int u = 0; u < 4; ++u) {
      const f32x4 s = a2[0][u] + f32x4{blo(y[u][0]), bhi(y[u][0]), blo(y[u][1]), bhi(y[u][1])};
      *(LDS u32x2*)(lds + a_o[0] + u * 32) = u32x2{pk2(s[0], s[1]), pk2(s[2], s[3])};
    }
    fence();
    const u32x4 o0 = *(const LDS u32x4*)(lds + a_o[1]), o1 = *(const LDS u32x4*)(lds + a_o[1] + 8 * 144);
    st16(o0, mkr(out, L * D * 2), v_o, (m2 * 64 + w4 * 16) * 1024 + n2 * 128);
    st16(o1, mkr(out, L * D * 2), v_o, (m2 * 64 + w4 * 16 + 8) * 1024 + n2 * 128);
    asm volatile(".Lk_code_end0:");
  } else {
    const int cl = nw, hh = mw;
    addr_x();
    addr_w();
    asm volatile("" : "+v"(vx[0]), "+v"(vx[1]), "+v"(vw[0]), "+v"(vw[1]), "+v"(vw[2]));
    auto part = [&](int s, int i0, int i1) INL {
      if (__builtin_expect(gemm, 1)) {
#pragma unroll
        for (int i = i0; i < i1; ++i) issue(s, i);
      }
    };
    auto stage = [&](int s) INL { part(s, 0, 10); };
    unsigned vd[4], vcs, pa[4], vxi, vzero, vimg, vxf, vtw[2], vtab[2], vex[2], vyf, vys, vco, vxt[2], vyl, vyw;
#pragma unroll
    for (int i = 0; i < 4; ++i) { const int row = lane >> 3, f = ((row >> 1) & 1) + 2 * (i & 1); vd[i] = (w4 * 352 + i * 8 + row) * 1024 + n2 * 128 + (((lane & 7) ^ (2 * f)) << 4); }
    vcs = tid < 96 ? (n0 + tid) * 8 : (CSP + n0 + min(tid, 191) - 96) * 8;
#pragma unroll
    for (int i = 0; i < 4; ++i) pa[i] = SBASE + cl * PAIR + (j16 & 1 ? TX : TY) + (847 - 256 * i - hh * 64 - ((15 + j16 - 8 * g) >> 1)) * 4;
    vxi = (tid >> 1) * XNP + (tid & 1) * 16 + cp * 32;
    vimg = SBASE + (tid & 1) * PAIR + XIMG + (tid >> 1) * 16;
    vzero = lane * 16 + (hh ? SYNC : SBASE + cl * PAIR + (lane < 2 ? TY + 4096 : TX + 4064));
    vtab[0] = SBASE + cl * PAIR + (255 - (hh ? (lane >> 5 ? 64 : 128) + (lane & 31) : lane)) * 16;
    vtab[1] = vtab[0] - (hh ? 32 : 192) * 16;
    vex[0] = (hh ? SBASE + cl * PAIR + CEX : SLOT + LEFT + cl * 4096) + lane * 16;
    vex[1] = (hh ? SLOT + LEFT + cl * 4096 : SBASE + cl * PAIR + CEX) + lane * 16;
    vyf = lane < 4 ? WS_YF + ((lane & 2) * 2 + (hh ? 1 + (lane & 1) : 3 * (lane & 1))) * (256 * YST) + (ch >> 3) * YST + (ch & 7) * 8
                   : lane == 63 && b == 0 && w4 == 0 ? WS_E : 0x7f000000u;
    vys = ch * 4096 + (hh ? 1 + (g & 1) : 3 * (g & 1)) * 512 + (j16 >> 2) * 128 + (g >> 1) * 64 + (j16 & 3) * 16;
    vco = CSO + (tid < 96 ? tid * 16 : (tid - 96) * 16 + 8);
    vxt[0] = (m2 * 64 + w4 * 16 + (lane >> 3)) * 1024 + n2 * 128 + (((lane & 7) ^ g) << 4);
    vxt[1] = vxt[0] ^ 64;
    vyl = (n2 * 64 + w4 * 16 + (lane >> 3)) * 4096 + m2 * 128 + (lane & 7) * 16;
    vyw = SBASE + cl * PAIR + hh * 4096 + (lane >> 3) * 160 + (lane & 3) * 32 + (lane & 4) * 4;
    asm volatile("" : "+v"(vxt[0]), "+v"(vxt[1]), "+v"(vyl), "+v"(vyw));
    asm volatile("" : "+v"(vco), "+v"(vex[0]), "+v"(vex[1]), "+v"(vyf), "+v"(vys));
    vxf = tid ? 0x7f000000u : WS_XNF + b * 8;
    vtw[0] = lane < 11 ? (b * 43 + w4 * 11 + lane) * 128 : 0x7f000000u;
    vtw[1] = lane < 11 ? (r * 43 + w4 * 11 + lane) * 1024 + ((384u - (unsigned)(u64)wd) & 896u) : 0x7f000000u;
    asm volatile("" : "+v"(vd[0]), "+v"(vd[1]), "+v"(vd[2]), "+v"(vd[3]), "+v"(vcs), "+v"(pa[0]), "+v"(pa[1]), "+v"(pa[2]), "+v"(pa[3]));
    asm volatile("" : "+v"(vxi), "+v"(vimg), "+v"(vzero), "+v"(vxf), "+v"(vtw[0]), "+v"(vtw[1]), "+v"(vtab[0]), "+v"(vtab[1]));
    auto issue_wd = [&](int s, int p) INL {
      const int pos = s % 7;
      dma16(pos == 6 ? wl6 : wl, (pos == 6 ? 0 : wdloc(pos)) + p * 1024, vd[p], rwd, s * 32768);
    };
    __builtin_amdgcn_s_setprio(1);
    waitv<1>();
    get_tag();
    if (w4 < 3) touch(DUMMY, lane << (w4 ? 16 : 18), w4 == 0 ? rws : w4 == 1 ? mkr(out, L * D * 2) : rwd, 4032);
    u32x2 tg = {(unsigned)tag, (unsigned)(tag >> 32)};
    asm volatile("" : "+v"(tg));
    float prm[6];
#pragma unroll
    for (int i = 0; i < 6; ++i) asm volatile("v_lshlrev_b32 %0, 16, %1" : "=v"(prm[i]) : "v"(xr[i >> 2][i & 3]));
    const float p_ldt = prm[0], p_lar = prm[1], p_aim = prm[2], p_cr = prm[3], p_ci = prm[4], skip = one(lane ? 0.f : prm[5]);
    const float dt = rb(exp_f(p_ldt));
    const float s2 = -rb(exp_f(p_lar)) * dt * 1.44269504f, th = p_aim * dt;
    const unsigned eb = min(max(wave_max(max(f2u(p_cr) << 1, f2u(p_ci) << 1)) >> 24, 1u), 253u);
    const float sc = u2f((254u - eb) << 23), ksc = 2.f * dt * u2f(eb << 23);
    f32x2 z1 = cpow2(s2, th, 1.f), z8c = cpow2(s2, -th, 8.f), zb = cpow2(s2, th, 256.f);
    asm volatile("" : "+v"(z1), "+v"(z8c), "+v"(zb));
    const cmulr z256 = mul_by(zb);
    LDS char* pr = lds + SBASE + cl * PAIR;
    LDS char* ks = pr + hh * KSW;
    auto put = [&](int row, f32x2 v) INL { *(LDS unsigned*)(ks + row * KROW + lane * 4) = pkh(v[0], v[1]); };
    auto putu = [&](int reg, int h, f32x2 v) INL { put((reg & 3) + 4 * h + 8 * (reg >> 2), v); };
    f16x8 vf[8];
    {
      f32x2 v[4];
      const cmulr m1 = mul_by(z8c);
      v[0] = f32x2{1.f, 0.f}; v[1] = z8c; v[2] = cmul(z8c, m1); v[3] = cmul(v[2], m1);
      const cmulr m4 = mul_by(cmul(v[3], m1));
#pragma unroll
      for (int k = 0; k < 32; ++k) {
        if (k == 4) v[0] = m4.z; else if (k > 4) v[k & 3] = cmul(v[k & 3], m4);
        put(k, v[k & 3]);
      }
      fence();
#pragma unroll
      for (int s = 0; s < 8; ++s) vf[s] = *(const LDS f16x8*)(ks + (lane & 31) * KROW + s * 32 + (lane >> 5) * 16);
      fence();
    }
    {
      f32x2 e[5];
      const cmulr m1 = mul_by(z1), m2_ = mul_by(cmul(z1, m1));
      e[0] = f32x2{p_cr, p_ci} * dup(sc);
      putu(0, 0, e[0]);
#pragma unroll
      for (int k = 0; k < 4; ++k) { putu(2 * k + 1, 0, cmul(e[k], m1)); e[k + 1] = cmul(e[k], m2_); putu(2 * k + 2, 0, e[k + 1]); }
#pragma unroll
      for (int k = 0; k < 5; ++k) e[k] = cmul(e[k], z256);
      putu(0, 1, e[0]); putu(2, 1, e[1]); putu(3, 1, e[2]); putu(4, 1, e[2]); putu(5, 1, e[2]); putu(6, 1, e[3]); putu(7, 1, e[4]); putu(8, 1, e[4]);
#pragma unroll
      for (int k = 0; k < 6; ++k) {
        const int reg = k < 2 ? 9 + 2 * k : k < 5 ? 5 + 2 * k : 13, h = k < 2 || k == 5;
        e[0] = cmul(e[0], z256); putu(reg, h, e[0]);
        e[4] = cmul(e[4], z256); putu(reg + 1, h, e[4]);
        if (k < 2) { e[2] = cmul(e[2], z256); putu(k ? 1 : 15, 1, e[2]); }
        if (k == 5) touch(DUMMY, vtw[0], rwd, 0);
      }
      fence();
    }
    bar();
    f32x16 kd = {};
    {
      f16x8 uf[8];
#pragma unroll
      for (int s = 0; s < 8; ++s) uf[s] = *(const LDS f16x8*)(ks + (lane & 31) * KROW + s * 32 + (lane >> 5) * 16);
      SB();
#pragma unroll
      for (int s = 0; s < 8; ++s) kd = __builtin_amdgcn_mfma_f32_32x32x16_f16(uf[s], vf[s], kd, 0, 0, 0);
    }
    kd *= ksc;
    const bool h = lane >> 5, odd = lane & 1;
    if (hh) {
      *(LDS f32x4*)(pr + KEX1 + lane * 16) = f32x4{kd[0], kd[1], kd[2], kd[3]};
      *(LDS f32x4*)(pr + KEX1 + 1024 + lane * 16) = f32x4{kd[4], kd[5], kd[6], kd[7]};
      *(LDS f32x4*)(pr + KEX1 + 2048 + lane * 16) = f32x4{kd[8], kd[13], kd[14], kd[15]};
    } else {
      *(LDS f32x4*)(pr + KEX0 + lane * 16) = f32x4{kd[9], kd[10], kd[11], kd[12]};
      *(LDS f32x2*)(pr + KEX0 + 1024 + lane * 8) = f32x2{kd[15], kd[1]};
    }
    __builtin_amdgcn_s_setprio(0);
    bar();
    {
      u32x4 tb[5];
      auto tail = [&](float a, float c, float a3, float a4, float a5, int k) INL {
        const unsigned aa = pk2(a, a), cc = pk2(c, c);
        tb[k] = u32x4{cc, pk2(a5, a4), pk2(a3, a), aa};
        tb[k + 1] = u32x4{cc, pk2(c, a5), pk2(a4, a3), aa};
      };
      if (hh) {
        const f32x4 p = *(const LDS f32x4*)(pr + KEX0 + lane * 16);
        const f32x2 q = *(const LDS f32x2*)(pr + KEX0 + 1024 + lane * 8);
#pragma unroll
        for (int k = 0; k < 2; ++k) {
          const float a = one(kd[9 + 2 * k] + p[2 * k]), c = one(kd[10 + 2 * k] + p[2 * k + 1]), m = one(kd[k ? 1 : 15] + q[k]);
          tail(a, c, h ? m : a, h ? m : odd ? c : a, h ? m : c, 2 * k);
        }
        tb[4][0] = 0;
      } else {
        const f32x4 p0 = *(const LDS f32x4*)(pr + KEX1 + lane * 16), p1 = *(const LDS f32x4*)(pr + KEX1 + 1024 + lane * 16);
        const f32x4 p2 = *(const LDS f32x4*)(pr + KEX1 + 2048 + lane * 16);
        float t[9];
#pragma unroll
        for (int k = 0; k < 9; ++k) t[k] = one(kd[k] + (k < 4 ? p0[k] : k < 8 ? p1[k - 4] : p2[0]));
        t[0] += skip;
        t[1] = h ? t[0] : t[1];
        tb[0] = u32x4{pk2(t[7], t[6]), pk2(t[5], t[4]), pk2(t[3], t[2]), pk2(t[1], t[0])};
        tb[1] = u32x4{pk2(t[8], t[7]), pk2(t[6], t[5]), pk2(t[4], t[3]), pk2(t[2], t[1])};
        const float a = one(kd[13] + p2[1]), c = one(kd[14] + p2[2]);
        tail(a, c, a, odd ? c : a, c, 2);
        tb[4][0] = lane ? 0 : tb[0][3] >> 16;
      }
      tb[4][1] = tb[4][2] = tb[4][3] = 0;
      asm volatile("" : "+v"(tb[0]), "+v"(tb[1]), "+v"(tb[2]), "+v"(tb[3]), "+v"(tb[4]));
#pragma unroll
      for (int k = 0; k < 4; ++k) *(LDS u32x4*)(lds + vtab[k >> 1] + (k & 1 ? TY : TX)) = tb[k];
      if (lane < (hh ? 5 : 36)) *(LDS u32x4*)(lds + vzero) = tb[4];
      part(2, 0, 10 - D2F);
    }
    bar();
    __builtin_amdgcn_raw_buffer_store_b64(tg, rws, vxf, 0, SC1);
    stage(3);
    waitv<10>();
    bar();
    stage(4);
    waitv<10>();
    bar();
    stage(5);
    waitv<10>();
    bar();
    stage(6);
    touch(DUMMY, vtw[1], rwd, 0);
    waitv<11>();
    bar();
    {
      u64 fl;
      asm volatile("buffer_load_dwordx2 %0, %1, %2, %3 offen sc1" : "=v"(fl) : "v"(tid * 8), "s"(rws), "s"(WS_XNF) : "memory");
      stage(7);
      if (gemm) waitv<10>(); else waitv<0>();
      asm volatile("" : "+v"(fl));
      if (__builtin_expect(!wave_all(fl == tag), 0)) poll<4>(ws, WS_XNF + tid * 8, tag);
    }
    bar();
#pragma unroll
    for (int i = 0; i < 2; ++i) dma16(cl * PAIR + hh * 2048, SBASE + CEX + i * 1024, vxt[i], rx, i * 8192);
    unsigned gst; u32x2 csg; u32x4 xi[2];
    asm volatile("buffer_load_dword %0, %1, %2, %3 offen sc1" : "=v"(gst) : "v"((tid & 127) * 4), "s"(rws), "s"(WS_ST + (mt & 15) * 512) : "memory");
    asm volatile("buffer_load_dwordx2 %0, %1, %2, %3 offen sc1" : "=v"(csg) : "v"(vcs), "s"(rws), "s"(WS_CS) : "memory");
#pragma unroll
    for (int i = 0; i < 2; ++i)
      asm volatile("buffer_load_dwordx4 %0, %1, %2, %3 offen sc1" : "=v"(xi[i]) : "v"(vxi), "s"(rws), "s"(WS_XNT + i * (128 * XNP)) : "memory");
#pragma unroll
    for (int i = 0; i < 4; ++i) issue_wd(0, i);
    waitv<10>();
    bar();
    waitv<6>();
    asm volatile("" : "+v"(gst), "+v"(csg));
    if (tid < 128) *(LDS u32x2*)(lds + STO + tid * 8) = u32x2{gst & 0xffff0000u, __builtin_amdgcn_perm(gst, gst, 0x01000100)};
    if (__builtin_expect(gemm && !wave_all(csg[1] == (unsigned)tag), 0)) {
#pragma unroll 1
      for (unsigned spins = 0; spins < SPIN_CAP && !wave_all(csg[1] == (unsigned)tag); ++spins) {
        __builtin_amdgcn_s_sleep(2);
        asm volatile("buffer_load_dwordx2 %0, %1, %2, %3 offen sc1\n\ts_waitcnt vmcnt(0)" : "=&v"(csg) : "v"(vcs), "s"(rws), "s"(WS_CS) : "memory");
      }
    }
    if (tid < 192) *(LDS u32x2*)(lds + vco) = u32x2{csg[0], csg[0]};
#pragma unroll
    for (int i = 0; i < 4; ++i) issue_wd(1, i);
    bar();
#pragma unroll
    for (int i = 8; i < 16; ++i) issue_wd(i >> 2, i & 3);
    waitv<8>();
    post(LANDED, 1);
    asm volatile("" : "+v"(xi[0]), "+v"(xi[1]));
#pragma unroll
    for (int i = 0; i < 2; ++i) *(LDS u32x4*)(lds + vimg + XPAD + i * 2048) = xi[i];
#pragma unroll
    for (int i = 16; i < 28; ++i) issue_wd(i >> 2, i & 3);
    waitv<12>();
    post(LANDED, 2);
    if (NPRE > 12) await(USED, 0, 1);
#pragma unroll
    for (int i = 28; i < 16 + NPRE; ++i) issue_wd(i >> 2, i & 3);
    LDS char* pb = pr + XIMG + XPAD + (16 * j16 + 8 * g - 16) * 2 - hh * 256 - 192;
    CvFrag cf[2];
    auto rdc = [&](CvFrag& f, int rr, int k) INL {
      const int i = k / 3, c = k % 3;
      if (c == 2) f.b[i] = *(const LDS s16x8*)(pb + i * 512 + (3 - rr) * 64);
      else f.a[2 * i + c] = *(const LDS s16x4u*)(lds + pa[i >> 1] + (i & 1 ? 0 : 512) + (3 - rr) * 64 + c * 8);
    };
#pragma unroll
    for (int k = 0; k < 24; ++k) if (k % 3 < 2) rdc(cf[0], 0, k);
    bar();
#pragma unroll
    for (int k = 2; k < 24; k += 3) rdc(cf[0], 0, k);
    __builtin_amdgcn_s_setprio(1);
    unsigned seen = 0;
    f32x4 ca[8] = {};
    auto conv = [&](int rr) INL {
      int n = 0;
#pragma unroll
      for (int u = 0; u < 8; ++u)
#pragma unroll
        for (int t = u; t < 8; ++t) {
          ca[t] = mfma16(cat(cf[rr & 1].a[2 * u], cf[rr & 1].a[2 * u + 1]), cf[rr & 1].b[t - u], ca[t]);
          SB();
          if (n < 32 && n % 4 < 3) rdc(cf[(rr + 1) & 1], rr + 1, n / 4 * 3 + n % 4);
          const int q = rr * 36 + n;
          const int j = 16 + NPRE + (q - Q0) / PACE;
          if (q >= Q0 && (q - Q0) % PACE == 0 && j < 44) {
            if (j == 28 || j == 36) await(USED, seen, j == 28 ? 1 : 2);
            if (j == 27 || j == 35) seen = peek(USED);
            issue_wd(j >> 2, j & 3);
          }
          SB();
          ++n;
        }
    };
    conv(0); conv(1); conv(2);
    {
      unsigned o[4][2];
      auto last = [&](int h) INL {
        auto tile = [](int set, int k) INL { return (k >> 1) * 4 + (set ? 1 + (k & 1) : 3 * (k & 1)); };
        unsigned mate;
        f32x4 in[4];
#pragma unroll
        for (int ph = 0; ph < 2; ++ph) {
          int n = 0;
#pragma unroll
          for (int u = 0; u < 8; ++u)
#pragma unroll
            for (int k = 3; k >= 0; --k) {
              const int t = tile(ph ? h : 1 - h, k);
              if (t < u) continue;
              ca[t] = mfma16(cat(cf[1].a[2 * u], cf[1].a[2 * u + 1]), cf[1].b[t - u], ca[t]);
              SB();
              if (ph && n < 4) *(LDS f32x4*)(lds + vex[0] + n * 1024) = ca[tile(1 - h, n)];
              if (ph && n == 4) post(SYNC, 1);
              if (ph && n == 6) { waitv<8>(); post(LANDED, 3); }
              if (ph && n == 10) mate = *(volatile LDS unsigned*)(lds + SYNC + (w4 ^ 1) * 4);
              SB();
              ++n;
            }
        }
        if (__builtin_expect(!rfl(mate), 0)) lds_wait(lds + SYNC + (w4 ^ 1) * 4, 1);
        fence();
#pragma unroll
        for (int k = 0; k < 4; ++k) in[k] = *(const LDS f32x4*)(lds + vex[1] + k * 1024);
        SB();
#pragma unroll
        for (int k = 0; k < 4; ++k) {
          const f32x4 s = ca[tile(h, k)] + in[k];
          o[k][0] = pk2(s[0], s[1]); o[k][1] = pk2(s[2], s[3]);
        }
      };
      if (hh) last(1); else last(0);
      __builtin_amdgcn_s_setprio(1);
#pragma unroll
      for (int kp = 0; kp < 2; ++kp) {
        const auto r0 = __builtin_amdgcn_permlane16_swap(o[2 * kp][0], o[2 * kp + 1][0], false, false);
        const auto r1 = __builtin_amdgcn_permlane16_swap(o[2 * kp][1], o[2 * kp + 1][1], false, false);
        const u32x4 ov = {r0[0], r1[0], r0[1], r1[1]};
        st16(ov, rws, vys, WS_YT + kp * 2048);
      }
      waitv<2>();
      post(LANDED, 4);
      waitv<0>();
      __builtin_amdgcn_raw_buffer_store_b64(tg, rws, vyf, 0, SC1);
    }
    __builtin_amdgcn_s_sleep(YSLEEP);
    const unsigned yf = WS_YF + ((m2 & -4) * 64 + n2 * 8 + w4 * 2) * YST;
    if (!watch<2>(ws + yf, tag)) poll<NAP>(ws, yf + (j16 >> 3) * YST + (j16 & 7) * 8, tag);
#pragma unroll
    for (int i = 0; i < 2; ++i) xi[i] = __builtin_amdgcn_raw_buffer_load_b128(rws, vyl, WS_YT + i * 32768, SC1);
#pragma unroll
    for (int i = 0; i < 2; ++i) *(LDS u32x4*)(lds + vyw + i * 1280) = xi[i];
    post(DONE + 16, 1);
    waitl0();
    asm volatile(".Lk_code_end1:");
  }
}

}

extern "C" void kernel_launch(void* const* d_in, const int* in_sizes, int n_in, void* d_out, int out_size, void* d_ws, size_t ws_size,
                              hipStream_t stream) {
  fused_kernel<<<256, 512, 0, stream>>>((const u16*)d_in[0], (const u16*)d_in[1], (const u16*)d_in[2], (const u16*)d_in[3], (const u16*)d_in[4],
                                        (const u16*)d_in[5], (const u16*)d_in[6], (const u16*)d_in[8], (const u16*)d_in[9], (const u16*)d_in[10],
                                        (u16*)d_out, (char*)d_ws);
}
```

```cpp
#include <hip/hip_runtime.h>
#include <hip/hip_bf16.h>

namespace {

typedef unsigned short u16;
typedef unsigned long long u64;
typedef __attribute__((ext_vector_type(4))) short s16x4;
typedef s16x4 __attribute__((aligned(4))) s16x4u;
typedef __attribute__((ext_vector_type(8))) short s16x8;
typedef __attribute__((ext_vector_type(8))) __bf16 bf16x8;
typedef __attribute__((ext_vector_type(2))) __bf16 bf16x2;
typedef __attribute__((ext_vector_type(8))) _Float16 f16x8;
typedef __attribute__((ext_vector_type(2))) _Float16 f16x2;
typedef __attribute__((ext_vector_type(2))) float f32x2;
typedef __attribute__((ext_vector_type(4))) float f32x4;
typedef __attribute__((ext_vector_type(16))) float f32x16;
typedef __attribute__((ext_vector_type(4))) unsigned u32x4;
typedef __attribute__((ext_vector_type(2))) unsigned u32x2;
typedef __amdgpu_buffer_rsrc_t rsrc_t;
#define LDS __attribute__((address_space(3)))
#define GLB __attribute__((address_space(1)))
#define INL __attribute__((always_inline))
#define SB() __builtin_amdgcn_sched_barrier(0)
#define RLX_AGENT __ATOMIC_RELAXED, __HIP_MEMORY_SCOPE_AGENT

constexpr int L = 2048, D = 512, NM = 128, F = 1376, NU = F / 16;
constexpr int SC1 = 16;
constexpr int HF = 10;
constexpr int D2F = 6;
constexpr int RD0 = 1, RD1 = 3, B9Q = 5;
constexpr int NPRE = 16, Q0 = 3, PACE = 4;
constexpr int HSLEEP = 8, YSLEEP = 14, NAP = 2;
constexpr int YST = 128;
static_assert(Q0 + (27 - NPRE) * PACE < 108 && RD0 < RD1 && RD1 < B9Q && B9Q < 6, "schedule");
constexpr unsigned SPIN_CAP = 1u << 18;

constexpr int WBLK = 3104, GOFF = 16384, UOFF = GOFF + 4 * WBLK, SLOT = GOFF + 8 * WBLK;
constexpr int LEFT = 32768;
constexpr int LNB = 2 * SLOT, LNROW = 1088;
constexpr int SBASE = 3 * SLOT;
constexpr int KROW = 272, KSW = 32 * KROW, PAIR = 2 * KSW;
constexpr int TX = 0, XIMG = 4160, XPAD = 480, CEX = 8736, TY = 13248, KEX0 = 7168, KEX1 = KSW;
constexpr int STO = SBASE + 2 * PAIR, CSO = STO + 1024, SYNC = CSO + 1536, LANDED = SYNC + 16, USED = SYNC + 32, DONE = SYNC + 48, DUMMY = SYNC + 128;
constexpr int LDS_BYTES = 163840;
static_assert(DUMMY + 256 <= LDS_BYTES && XIMG + XPAD + 4096 <= CEX && CEX + 4096 <= TY && TY + 4128 <= PAIR, "LDS map");

constexpr int WS_E = 0, WS_XNF = 4096, WS_HF = 65536, WS_YF = 196608, WS_ST = 32768, WS_CS = 40960;
constexpr int XNP = 8320, WS_XNT = 1 << 19, WS_YT = 3 << 20, WS_H = 5 << 20, WS_END = WS_H + 64 * NU * 1024;
constexpr int CSP = 1408;

__device__ __forceinline__ float u2f(unsigned v) { return __builtin_bit_cast(float, v); }
__device__ __forceinline__ unsigned f2u(float v) { return __builtin_bit_cast(unsigned, v); }
__device__ __forceinline__ bool wave_all(bool p) { return __builtin_amdgcn_ballot_w64(!p) == 0; }
__device__ __forceinline__ unsigned rfl(unsigned v) { return __builtin_amdgcn_readfirstlane(v); }
__device__ __forceinline__ rsrc_t mkr(const void* p, unsigned n) { return __builtin_amdgcn_make_buffer_rsrc((void*)p, 0, n, 0x00020000); }
__device__ __forceinline__ void dma16(unsigned base, int dst, unsigned voff, rsrc_t r, unsigned soff, int off = 0) {
  asm volatile("s_add_u32 m0, %0, %1\n\ts_nop 0\n\tbuffer_load_dwordx4 %2, %3, %4 offen offset:%5 lds"
               ::"s"(base), "n"(dst - off), "v"(voff), "s"(r), "s"(soff), "n"(off) : "scc", "memory");
}
__device__ __forceinline__ void touch(unsigned m0v, unsigned voff, rsrc_t r, unsigned soff) {
  asm volatile("s_mov_b32 m0, %0\n\ts_nop 0\n\tbuffer_load_dword %1, %2, %3 offen lds" ::"s"(m0v), "v"(voff), "s"(r), "s"(soff) : "memory");
}
__device__ __forceinline__ void st16(u32x4 v, rsrc_t r, unsigned voff, unsigned soff) {
  asm volatile("buffer_store_dwordx4 %0, %1, %2, %3 offen sc1\n\ts_nop 1" ::"v"(v), "v"(voff), "s"(r), "s"(soff) : "memory");
}
template <int W> __device__ __forceinline__ void waitv() { asm volatile("s_waitcnt vmcnt(%0)" ::"n"(W) : "memory"); }
__device__ __forceinline__ void waitl0() { asm volatile("" ::: "memory"); __builtin_amdgcn_s_waitcnt(0xc07f); asm volatile("" ::: "memory"); }
__device__ __forceinline__ void fence() { asm volatile("" ::: "memory"); }
__device__ __forceinline__ void bar() { waitl0(); __builtin_amdgcn_s_barrier(); fence(); }
__device__ __forceinline__ unsigned pk2(float a, float b) { f32x2 p = {a, b}; return __builtin_bit_cast(unsigned, __builtin_convertvector(p, bf16x2)); }
__device__ __forceinline__ unsigned pkh(float a, float b) { f32x2 p = {a, b}; return __builtin_bit_cast(unsigned, __builtin_convertvector(p, f16x2)); }
__device__ __forceinline__ float blo(unsigned v) { return u2f(v << 16); }
__device__ __forceinline__ float bhi(unsigned v) { return u2f(v & 0xffff0000u); }
__device__ __forceinline__ float rb(float a) { return blo(pk2(a, a)); }
template <int C, int RM> __device__ __forceinline__ float dppf(float v) {
  return __builtin_bit_cast(float, __builtin_amdgcn_update_dpp(0, __builtin_bit_cast(int, v), C, RM, 0xf, true));
}
__device__ __forceinline__ float lane63(float v) { return __builtin_bit_cast(float, __builtin_amdgcn_readlane(__builtin_bit_cast(int, v), 63)); }
__device__ __forceinline__ float wave_sum(float v) {
  v += dppf<0x111, 0xf>(v); v += dppf<0x112, 0xf>(v); v += dppf<0x114, 0xf>(v); v += dppf<0x118, 0xf>(v);
  v += dppf<0x142, 0xa>(v); v += dppf<0x143, 0xc>(v);
  return lane63(v);
}
template <int C, int RM> __device__ __forceinline__ unsigned dppmax(unsigned v) {
  const unsigned o = __builtin_amdgcn_update_dpp(0, v, C, RM, 0xf, true);
  return v > o ? v : o;
}
__device__ __forceinline__ unsigned wave_max(unsigned v) {
  v = dppmax<0x111, 0xf>(v); v = dppmax<0x112, 0xf>(v); v = dppmax<0x114, 0xf>(v); v = dppmax<0x118, 0xf>(v);
  v = dppmax<0x142, 0xa>(v); v = dppmax<0x143, 0xc>(v);
  return __builtin_amdgcn_readlane(v, 63);
}
__device__ __forceinline__ s16x8 cat(s16x4 a, s16x4 b) { return __builtin_shufflevector(a, b, 0, 1, 2, 3, 4, 5, 6, 7); }
__device__ __forceinline__ s16x4 trd(LDS char* p) { return __builtin_amdgcn_ds_read_tr16_b64_v4i16((LDS s16x4*)p); }
__device__ __forceinline__ f32x4 mfma16(s16x8 a, s16x8 b, f32x4 c) {
  return __builtin_amdgcn_mfma_f32_16x16x32_bf16(__builtin_bit_cast(bf16x8, a), __builtin_bit_cast(bf16x8, b), c, 0, 0, 0);
}
template <int S> __device__ __forceinline__ void poll(const char* ws, unsigned off, u64 tag) {
  GLB u64* p = (GLB u64*)(ws + off);
#pragma unroll 1
  for (unsigned spins = 0; spins < SPIN_CAP; ++spins) {
    if (wave_all(__hip_atomic_load(p, RLX_AGENT) == tag)) break;
    __builtin_amdgcn_s_sleep(S);
  }
  fence();
}
typedef __attribute__((ext_vector_type(8))) unsigned long long u64x8;
template <int N> __device__ __forceinline__ bool watch(const char* p, u64 tag) {
#pragma unroll 1
  for (unsigned spins = 0; spins < 128; ++spins) {
    u64x8 f[2];
    u64 d = 0;
    if constexpr (N == 1) asm volatile("s_load_dwordx16 %0, %1, 0 glc\n\ts_waitcnt lgkmcnt(0)" : "=&s"(f[0]) : "s"(p) : "memory");
    else asm volatile("s_load_dwordx16 %0, %2, 0 glc\n\ts_load_dwordx16 %1, %2, %3 glc\n\ts_waitcnt lgkmcnt(0)" : "=&s"(f[0]), "=&s"(f[1]) : "s"(p), "n"(YST) : "memory");
#pragma unroll
    for (int i = 0; i < 8 * N; ++i) { d |= f[i >> 3][i & 7] ^ tag; asm("" : "+s"(d)); }
    if (d == 0) return true;
    __builtin_amdgcn_s_sleep(NAP);
  }
  return false;
}
__device__ __forceinline__ void lds_wait(LDS char* p, unsigned v) {
#pragma unroll 1
  for (unsigned spins = 0; spins < SPIN_CAP; ++spins) { if (rfl(*(volatile LDS unsigned*)p) >= v) break; __builtin_amdgcn_s_sleep(1); }
  fence();
}
__device__ __forceinline__ f32x2 fma2(f32x2 a, f32x2 b, f32x2 c) { return __builtin_elementwise_fma(a, b, c); }
__device__ __forceinline__ float one(float v) { asm("" : "+v"(v)); return v; }
__device__ __forceinline__ f32x2 dup(float a) { return f32x2{a, a}; }
struct cmulr { f32x2 z, rot; };
__device__ __forceinline__ cmulr mul_by(f32x2 z) { return {z, f32x2{-z[1], z[0]}}; }
__device__ __forceinline__ f32x2 cmul(f32x2 a, cmulr m) { return fma2(dup(a[0]), m.z, dup(a[1]) * m.rot); }
__device__ __forceinline__ f32x2 cpow2(float s2, float th, float p) {
  const float t = th * p, n = __builtin_rintf(t * 0.15915494f);
  const float fr = __builtin_fmaf(t, 0.15915494f, -n) + t * 6.4206382e-9f;
  const float e = __builtin_amdgcn_exp2f(s2 * p);
  return f32x2{e * __builtin_amdgcn_cosf(fr), e * __builtin_amdgcn_sinf(fr)};
}
__device__ __forceinline__ float exp_f(float v) {
  const float t = v * 1.44269504f, e = __builtin_fmaf(v, 1.925963033e-8f, __builtin_fmaf(v, 1.44269504f, -t));
  const float y = __builtin_amdgcn_exp2f(t);
  return __builtin_fmaf(y, e * 0.69314718f, y);
}


struct G1Frag { s16x8 x[4]; s16x4 w[12]; };
struct CvFrag { s16x4 a[16]; s16x8 b[8]; };

__global__ __launch_bounds__(512, 2) void fused_kernel(const u16* x, const u16* lar, const u16* aim, const u16* cre, const u16* cim, const u16* ldt,
                                                       const u16* dsk, const u16* wg, const u16* wu, const u16* wd, u16* out, char* ws) {
  __shared__ __attribute__((aligned(16))) char lds_raw[LDS_BYTES];
  LDS char* lds = (LDS char*)lds_raw;
  int w = rfl(threadIdx.x >> 6);
  const int lane = threadIdx.x & 63, tid = threadIdx.x & 255;
  const int b = blockIdx.x, xcd = b & 7, r = b >> 3;
  if (w == 7) {
    u64 pc; unsigned l0, l1;
    asm volatile("s_getpc_b64 %0\n.Lk_code_begin:\n\ts_mov_b32 %1, .Lk_code_end0-.Lk_code_begin\n\ts_mov_b32 %2, .Lk_code_end1-.Lk_code_begin" : "=s"(pc), "=s"(l0), "=s"(l1));
    touch(DUMMY, lane * 1024, mkr((const void*)pc, max(l0, l1) - 896), (r & 7) * 128);
  }
  const int row0 = (xcd & 1) * 1024 + (xcd >> 1) * 256 + r * 8;
  const unsigned vt = lane < 16 ? (lane >> 3) * (64 * F * 2) + (lane & 7) * 96 : 0x7f000000u;
  const unsigned so_t = ((w & 3) * 128 + (xcd & 1) * 32 + r) * (F * 2) + (xcd >> 1) * 688;
  asm volatile("" ::"s"(row0), "v"(vt), "s"(so_t));
  asm volatile("" : "+s"(x), "+s"(lar), "+s"(aim), "+s"(cre), "+s"(cim), "+s"(ldt), "+s"(dsk), "+s"(wg), "+s"(wu), "+s"(wd), "+s"(out), "+s"(ws));
  const rsrc_t rx = mkr(x, L * D * 2);
  const int w4 = w & 3, mw = w & 1, nw = w4 >> 1;
  const int cp = xcd * 32 + r, ch = cp * 2 + nw;
  u32x4 xr[2];
  u64 tag;
  if (w < 4) {
#pragma unroll
    for (int i = 0; i < 2; ++i)
      asm volatile("buffer_load_dwordx4 %0, %1, %2, %3 offen" : "=v"(xr[i]) : "v"(lane * 16), "s"(rx), "s"((row0 + 2 * w4 + i) * 1024) : "memory");
  } else {
    const unsigned pi = (ch * NM + mw * 64 + lane) * 2;
    asm volatile("global_load_ushort %0, %6, %8\n\tglobal_load_ushort %1, %7, %9\n\tglobal_load_ushort %2, %7, %10\n\tglobal_load_ushort %3, %7, %11\n\t"
                 "global_load_ushort %4, %7, %12\n\tglobal_load_ushort %5, %6, %13"
                 : "=&v"(xr[0][0]), "=&v"(xr[0][1]), "=&v"(xr[0][2]), "=&v"(xr[0][3]), "=&v"(xr[1][0]), "=&v"(xr[1][1])
                 : "v"(ch * 2), "v"(pi), "s"(ldt), "s"(lar), "s"(aim), "s"(cre), "s"(cim), "s"(dsk) : "memory");
  }
  u64 epoch;
  asm volatile("s_load_dwordx2 %0, %1, 0x0" : "=s"(epoch) : "s"(ws) : "memory");
  __builtin_amdgcn_s_barrier();
  touch(DUMMY, vt, mkr(w >> 2 ? wu : wg, D * F * 2), so_t);
  auto get_tag = [&]() INL {
    unsigned lo, hi;
    asm volatile("s_waitcnt lgkmcnt(0)\n\ts_add_u32 %0, %2, 0x7F4A7C15\n\ts_addc_u32 %1, %3, 0x9E3779B9"
                 : "=&s"(lo), "=s"(hi) : "s"((unsigned)epoch), "s"((unsigned)(epoch >> 32)) : "scc", "memory");
    tag = (u64)hi << 32 | lo;
  };
  asm volatile("" : "+s"(w));
  SB();

  const rsrc_t rws = mkr(ws, WS_END), rwd = mkr(wd, F * D * 2);
  const bool gemm = r < 30;
  const int idx = (xcd >> 1) * 30 + r, nt = idx >> 3, mt = (xcd & 1) * 8 + (idx & 7);
  const int n0 = nt < 14 ? nt * 96 : 1280;
  const int m2 = (xcd & 1) * 16 + (xcd >> 1) * 4 + (r >> 3), n2 = r & 7;
  const int j16 = lane & 15, g = lane >> 4;

  const rsrc_t rw = mkr((gemm ? nw : xcd >> 2) ? wu : wg, D * F * 2);
  unsigned vx[2], vw[3], lx = w4 * 4096, lw = (nw ? UOFF : GOFF) + mw * 2 * WBLK;
  asm volatile("" : "+s"(lx), "+s"(lw));
  auto addr_x = [&]() INL {
#pragma unroll
    for (int i = 0; i < 2; ++i) {
      const int row = i * 8 + (lane >> 3), id = (row >> 1) & 7, f = (id & 4) | ((id & 1) << 1) | ((id >> 1) & 1);
      vx[i] = (mt * 128 + w4 * 32 + row) * 1024 + (((lane & 7) ^ f) << 4);
    }
  };
  auto addr_w = [&]() INL {
    const int kr = (lane * 171) >> 11, c = lane - kr * 12;
    vw[0] = (mw * 32 + kr) * (F * 2) + c * 16 + n0 * 2;
    vw[1] = vw[0] + (c >= 8 ? 6 * F * 2 - 128 : 5 * F * 2 + 64);
    vw[2] = vw[0] + (c >= 4 ? 11 * F * 2 - 64 : 10 * F * 2 + 128);
  };
  auto issue = [&](int s, int i) INL {
    const int slot = (s % 3) * SLOT, j = i - 4;
    if (i < 4) dma16(lx, slot + i * 1024, vx[i & 1], rx, s * 128 + (i >> 1) * 16384);
    else dma16(lw, slot + (j / 3) * WBLK + (j % 3) * 1024, vw[j % 3], rw, s * (64 * F * 2) + (j / 3) * 16 * F * 2);
  };
  const unsigned wl = w4 * 8192, wl6 = (nw ? 0 : 2 * SLOT) + LEFT + mw * 4096;
  auto wdloc = [](int pos) INL { return ((pos / 2 + 2) % 3) * SLOT + (pos & 1) * 4096; };

  auto post = [&](int word, unsigned v) INL { fence(); *(volatile LDS unsigned*)(lds + word + w4 * 4) = v; fence(); };
  auto peek = [&](int word) INL { return *(volatile LDS unsigned*)(lds + word + w4 * 4); };
  auto await = [&](int word, unsigned seen, unsigned v) INL { if (__builtin_expect(rfl(seen) < v, 0)) lds_wait(lds + word + w4 * 4, v); fence(); };

  if (__builtin_expect(w < 4, 1)) {
    auto layernorm = [&](auto&& hook) INL {
      get_tag();
      u32x2 stat;
#pragma unroll
      for (int i = 0; i < 2; ++i) {
        f32x2 v[4];
#pragma unroll
        for (int e = 0; e < 4; ++e) asm volatile("v_lshlrev_b32 %0, 16, %2\n\tv_and_b32 %1, 0xffff0000, %2" : "=&v"(v[e][0]), "=v"(v[e][1]) : "v"(xr[i][e]));
        hook(5 * i);
        const float x0 = u2f(rfl(f2u(v[0][0])));
#pragma unroll
        for (int e = 0; e < 4; ++e) v[e] -= dup(x0);
        const f32x2 s1 = (v[0] + v[1]) + (v[2] + v[3]), s2 = fma2(v[0], v[0], v[1] * v[1]) + fma2(v[2], v[2], v[3] * v[3]);
        float t1 = s1[0] + s1[1], t2 = s2[0] + s2[1];
        asm volatile("" : "+v"(t1), "+v"(t2));
        hook(5 * i + 1);
        const float md = wave_sum(t1) * (1.f / D);
        float var = __builtin_fmaf(-md, md, wave_sum(t2) * (1.f / D));
        asm volatile("" : "+v"(var));
        hook(5 * i + 2);
        const float m = rb(x0 + md), sd = rb(__builtin_amdgcn_sqrtf(rb(rb(__builtin_fmaxf(var, 0.f)) + 1e-5f))), rs = __builtin_amdgcn_rcpf(sd);
        float c = (x0 - m) * rs;
        asm volatile("" : "+v"(c));
        hook(5 * i + 3);
        u32x4 o;
#pragma unroll
        for (int e = 0; e < 4; ++e) { const f32x2 n = fma2(v[e], dup(rs), dup(c)); o[e] = pk2(n[0], n[1]); }
        *(LDS u32x4*)(lds + LNB + (2 * w4 + i) * LNROW + lane * 16) = o;
        hook(5 * i + 4);
        stat[i] = pk2(m, sd);
      }
      __builtin_amdgcn_raw_buffer_store_b64(stat, rws, lane ? 0x7f000000u : 0u, WS_ST + (row0 + 2 * w4) * 4, SC1);
      bar();
#pragma unroll
      for (int i = 0; i < 2; ++i) {
        LDS char* p = lds + LNB + (j16 >> 2) * LNROW + (i * 256 + w4 * 64 + g * 16 + (j16 & 3) * 4) * 2;
        st16(__builtin_bit_cast(u32x4, cat(trd(p), trd(p + 4 * LNROW))), rws, tid * 16, WS_XNT + (row0 >> 3) * XNP + i * 4096);
      }
    };
    unsigned ad[3][4], vh[4], vpoll, kh, kh10, a_xt, vflag = 0x7f000000u;
    u32x2 xt[4];
    auto rdxt = [&](int u) INL { xt[u] = *(const LDS u32x2*)(lds + SBASE + CEX - 32 * 1024 + (a_xt ^ (u * 32))); };
    s16x4 wf[11][8];
    auto wd_addr = [&](int i) INL {
      const int q = j16 >> 2, f = ((q >> 1) & 1) + 2 * (g & 1);
#pragma unroll
      for (int u = 0; u < 4; ++u) {
        ad[i][u] = (i == 0 ? wl : i == 1 ? wl + 2 * SLOT : wl6) + (8 * g + q) * 128 + ((u ^ f) << 5) + (j16 & 3) * 8;
        asm volatile("" : "+v"(ad[i][u]));
      }
    };
    auto rdwh = [&](int s, int u, int h) INL {
      const int pos = s % 7;
      LDS char* p = lds + (pos == 6 ? ad[2][u] : pos < 2 ? ad[1][u] + pos * 4096 : ad[0][u] + wdloc(pos));
      wf[s][2 * u + h] = trd(p + h * 512);
    };
    auto rdwd = [&](int s, int u) INL { rdwh(s, u, 0); rdwh(s, u, 1); };
    auto h_addr = [&]() INL {
      wd_addr(0); wd_addr(1); wd_addr(2);
#pragma unroll
      for (int m = 0; m < 4; ++m) { const int mm = (m + w4) & 3; vh[m] = (mm >> 1) * (NU * 1024) + (g >> 1) * 1024 + (32 * (g & 1) + 16 * (mm & 1) + j16) * 16; }
      vpoll = WS_HF + (m2 * 4 + w4) * 1024;
      a_xt = 32 * 1024 + nw * PAIR + mw * 2048 + j16 * 128 + (((g >> 1) ^ (j16 >> 1)) << 4) + (g & 1) * 8;
      asm volatile("" : "+v"(a_xt), "+v"(vh[0]), "+v"(vh[1]), "+v"(vh[2]), "+v"(vh[3]));
      kh = WS_H + m2 * 2 * NU * 1024 + w4 * (11 * 2048);
      kh10 = w4 == 3 ? 0x7f000000u : kh + 10 * 2048;
      asm volatile("" : "+s"(kh), "+s"(kh10), "+s"(vpoll));
    };

    if (gemm) {
      addr_x();
      addr_w();
      asm volatile("" : "+v"(vx[0]), "+v"(vx[1]), "+v"(vw[0]), "+v"(vw[1]), "+v"(vw[2]));
      unsigned ax[4], aw[2];
      {
        const int id = (j16 >> 1) & 7, f = (id & 4) | ((id & 1) << 1) | ((id >> 1) & 1), cg = 2 * (g & 1) + (g >> 1);
#pragma unroll
        for (int s = 0; s < 2; ++s) { ax[s] = (mw * 64 + j16) * 128 + (((4 * s + cg) ^ f) << 4); ax[s + 2] = ax[s] + 2 * SLOT; }
        aw[0] = GOFF + (g & 1) * WBLK + (8 * (g >> 1) + (j16 >> 2)) * 192 + nw * 96 + (j16 & 3) * 8;
        aw[1] = aw[0] + 2 * SLOT;
      }
      unsigned a_st = STO + (mw * 64 + j16) * 8, a_cs = CSO + (nw * 48 + j16) * 16, so_h[3];
#pragma unroll
      for (int u = 0; u < 3; ++u) {
        const int unit = (n0 >> 4) + nw * 3 + u;
        so_h[u] = nt < 14 || unit >= 84 ? WS_H + ((mt * 4 + mw * 2) * NU + unit) * 1024 : 0x7f000000u;
      }
      const unsigned fi = (nt < 14 ? nt * 2 + nw : nw ? 28 : 63) - 7 * (lane >> 3);
      vflag = lane < 32 && (lane & 7) == 0 && fi < 8 ? WS_HF + (mt * 2 + mw) * 4096 + lane * 128 + fi * 8 : 0x7f000000u;
      asm volatile("" : "+v"(a_st), "+v"(a_cs), "+s"(so_h[0]), "+s"(so_h[1]), "+s"(so_h[2]), "+v"(vflag));
      asm volatile("" : "+v"(ax[0]), "+v"(ax[1]), "+v"(ax[2]), "+v"(ax[3]), "+v"(aw[0]), "+v"(aw[1]));
      h_addr();
      SB();
#pragma unroll
      for (int i = 0; i < 10; ++i) issue(0, i);
      waitv<11>();
      layernorm([&](int k) INL { issue(1, k); });
#pragma unroll
      for (int i = 10 - D2F; i < 10; ++i) issue(2, i);
      f32x4 acc[4][6] = {};
      G1Frag fr[2];
      auto half = [&](G1Frag& c, bool domm, G1Frag& n, bool dord, int s, int jj) INL {
#pragma unroll
        for (int t = 0; t < 24; ++t) {
          if (domm) acc[t & 3][t >> 2] = mfma16(cat(c.w[t >> 2 << 1], c.w[(t >> 2 << 1) + 1]), c.x[t & 3], acc[t & 3][t >> 2]);
          SB();
          const int hi = s % 3 == 2, so = s % 3 == 1 ? SLOT : 0;
#pragma unroll
          for (int k = t; k < min(t + 1, 16); ++k) {
            const int u = (k - 4) >> 1;
            if (k < 4) n.x[k] = *(const LDS s16x8*)(lds + so + ax[jj + 2 * hi] + k * 2048);
            else n.w[k - 4] = trd(lds + aw[hi] + so + jj * 2 * WBLK + (u / 3) * 4 * WBLK + (u % 3) * 32 + (k & 1) * 768);
          }
          SB();
        }
      };
#pragma unroll
      for (int s = 0; s < 8; ++s) {
        if (s == 0) waitv<13 + D2F>(); else if (s == 1) waitv<D2F>(); else if (s == 2) waitv<0>();
        bar();
        half(fr[1], s > 0, fr[0], true, s, 0);
        half(fr[0], true, fr[1], true, s, 1);
      }
      bar();
      u32x2 st[4], bm[4]; s16x8 cs[3];
      float kg[4];
#pragma unroll
      for (int t = 0; t < 24; ++t) {
        acc[t & 3][t >> 2] = mfma16(cat(fr[1].w[t >> 2 << 1], fr[1].w[(t >> 2 << 1) + 1]), fr[1].x[t & 3], acc[t & 3][t >> 2]);
        SB();
        if (t < 4) st[t] = *(const LDS u32x2*)(lds + a_st + t * 128);
        else if (t < 7) cs[t - 4] = *(const LDS s16x8*)(lds + a_cs + (t - 4) * 256);
        else if (t < 11) rdxt(t - 7);
        else if (t >= 12 && t < 16) { bm[t - 12] = u32x2{g ? 0 : st[t - 12][1], 0}; asm volatile("" : "+v"(bm[t - 12])); }
        else if (t >= 16 && t < 20) { kg[t - 16] = -1.44269504f * __builtin_amdgcn_rcpf(u2f(st[t - 16][0])); asm volatile("" : "+v"(kg[t - 16])); }
        SB();
      }
      auto mean = [&](int t) INL {
        const int m = (t & 1) + 2 * (t / 12), u = (t >> 2) % 3, up = (t >> 1) & 1;
        const s16x4 c = up ? __builtin_shufflevector(cs[u], cs[u], 4, 5, 6, 7) : __builtin_shufflevector(cs[u], cs[u], 0, 1, 2, 3);
        acc[m][u + 3 * up] = __builtin_amdgcn_mfma_f32_16x16x16bf16_1k(c, __builtin_bit_cast(s16x4, bm[m]), acc[m][u + 3 * up], 0, 0, 0);
      };
#pragma unroll
      for (int t = 0; t < 24; ++t) { mean(t); SB(); }
      unsigned seen;
#pragma unroll
      for (int mp = 0; mp < 2; ++mp) {
#pragma unroll
        for (int u = 0; u < 3; ++u) {
          unsigned o[2][2];
          const int q = mp * 3 + u;
          if (q == B9Q) bar();
          if (q == RD0 || q == RD1) {
            await(LANDED, seen, q == RD0 ? 1 : 2);
#pragma unroll
            for (int k = 0; k < 8; ++k) rdwd((q == RD0 ? 0 : 2) + (k >> 2), k & 3);
            post(USED, q == RD0 ? 1 : 2);
          }
          if (q == RD0 - 1 || q == RD1 - 1) { seen = peek(LANDED); SB(); }
#pragma unroll
          for (int mi = 0; mi < 2; ++mi) {
            const int m = mp * 2 + mi;
            const float sd = u2f(st[m][0]);
#pragma unroll
            for (int e = 0; e < 2; ++e) {
              const f32x2 gg = f32x2{acc[m][u][2 * e], acc[m][u][2 * e + 1]} * dup(kg[m]);
              const f32x2 dn = fma2(f32x2{__builtin_amdgcn_exp2f(gg[0]), __builtin_amdgcn_exp2f(gg[1])}, dup(sd), dup(sd));
              const f32x2 hv = f32x2{acc[m][u + 3][2 * e], acc[m][u + 3][2 * e + 1]} * f32x2{__builtin_amdgcn_rcpf(dn[0]), __builtin_amdgcn_rcpf(dn[1])};
              o[mi][e] = pk2(hv[0], hv[1]);
            }
          }
          const auto r0 = __builtin_amdgcn_permlane16_swap(o[0][0], o[1][0], false, false);
          const auto r1 = __builtin_amdgcn_permlane16_swap(o[0][1], o[1][1], false, false);
          const u32x4 ov = {r0[0], r1[0], r0[1], r1[1]};
          st16(ov, rws, lane * 16 + mp * (NU * 1024), so_h[u]);
          SB();
        }
      }
    } else {
      const int cj = (xcd & 3) * 2 + (r - 30), kg = (tid * 745) >> 14, cc = tid - kg * 22, col = cj * 176 + cc * 8;
      const unsigned vo = (kg < 11 && col < F) ? kg * (F * 2) + col * 2 : 0x7f000000u;
      u32x4 cv[2][8];
      auto ldcs = [&](int sl) INL {
#pragma unroll
        for (int i = 0; i < 8; ++i) cv[sl & 1][i] = __builtin_amdgcn_raw_buffer_load_b128(rw, vo + (sl * 8 + i) * (11 * F * 2), 0, 0);
      };
      ldcs(0);
      waitv<9>();
      layernorm([](int) INL {});
      f32x2 a[4] = {};
#pragma unroll
      for (int sl = 0; sl < 6; ++sl) {
        if (sl == 1) waitv<0>();
        if (sl < 5) ldcs(sl + 1);
        if (sl < 4) bar();
#pragma unroll
        for (int i = 0; i < 8; ++i)
#pragma unroll
          for (int e = 0; e < 4; ++e) { const unsigned q = cv[sl & 1][i][e]; a[e] += f32x2{blo(q), bhi(q)}; }
        asm volatile("" : "+v"(a[0]), "+v"(a[1]), "+v"(a[2]), "+v"(a[3]));
      }
      if (kg < 11) {
        *(LDS f32x4*)(lds + kg * 704 + cc * 32) = f32x4{a[0][0], a[0][1], a[1][0], a[1][1]};
        *(LDS f32x4*)(lds + kg * 704 + cc * 32 + 16) = f32x4{a[2][0], a[2][1], a[3][0], a[3][1]};
      }
      bar();
      if (tid < 176) {
        float s = 0.f;
#pragma unroll
        for (int k = 0; k < 11; ++k) s += *(const LDS float*)(lds + k * 704 + tid * 4);
        __builtin_amdgcn_raw_buffer_store_b64(u32x2{pk2(-s, rb(s) - s), (unsigned)tag}, rws, tid * 8, WS_CS + ((xcd >> 2) * CSP + cj * 176) * 8, SC1);
      }
      h_addr();
      bar(); bar(); bar(); bar();
#pragma unroll
      for (int u = 0; u < 4; ++u) rdxt(u);
      await(LANDED, 0, 2);
#pragma unroll
      for (int k = 0; k < 16; ++k) rdwd(k >> 2, k & 3);
      post(USED, 2);
      bar();
    }

    f32x4 a2[4][4] = {};
#pragma unroll
    for (int u = 0; u < 4; ++u) a2[0][u] = f32x4{blo(xt[u][0]), bhi(xt[u][0]), blo(xt[u][1]), bhi(xt[u][1])};
    unsigned a_p[3], a_y = SBASE + (4 * g + (j16 >> 2)) * 160 + w4 * 32 + (j16 & 3) * 8, a_o[2], v_o = (lane >> 3) * 1024 + (lane & 7) * 16;
#pragma unroll
    for (int m = 1; m < 4; ++m) a_p[m - 1] = wdloc(m - 1) + ((w4 - m) & 3) * 8192 + lane * 16;
    a_o[0] = wdloc(4) + wl + j16 * 144 + g * 8;
    a_o[1] = wdloc(4) + wl + (lane >> 3) * 144 + (lane & 7) * 16;
    asm volatile("" : "+v"(a2[0][0]), "+v"(a2[0][1]), "+v"(a2[0][2]), "+v"(a2[0][3]));
    asm volatile("" : "+v"(a_p[0]), "+v"(a_p[1]), "+v"(a_p[2]), "+v"(a_y), "+v"(a_o[0]), "+v"(a_o[1]), "+v"(v_o));
    waitv<0>();
    __builtin_amdgcn_raw_buffer_store_b64(u32x2{(unsigned)tag, (unsigned)(tag >> 32)}, rws, vflag, 0, SC1);
    __builtin_amdgcn_s_sleep(HSLEEP);
    if (!watch<1>(ws + vpoll, tag)) poll<NAP>(ws, vpoll + (lane & 7) * 8, tag);
    auto fs = [](int n) INL { return n < 32 ? n >> 2 : 8 + (n - 32) % 3; };
    auto fm = [](int n) INL { return ((n < 32 ? n : (n - 32) / 3) + 1) & 3; };
    s16x8 hf[44];
    auto ldh = [&](int n) INL {
      hf[n] = __builtin_bit_cast(s16x8, __builtin_amdgcn_raw_buffer_load_b128(rws, vh[fm(n)] + (fs(n) & 1) * 2048, fs(n) == 10 ? kh10 : kh + (fs(n) >> 1) * 4096, SC1));
    };
#pragma unroll
    for (int n = 0; n < HF; ++n) { ldh(n); SB(); }
    unsigned landed;
    u32x4 dn[2];
    SB();
#pragma unroll
    for (int n = 0; n < 44; ++n) {
      if (n == 2 || n == 22) landed = peek(LANDED);
      if (n == 4 || n == 24) await(LANDED, landed, n == 4 ? 3 : 4);
#pragma unroll
      for (int u = 0; u < 4; ++u) {
        a2[fm(n)][u] = mfma16(cat(wf[fs(n)][2 * u], wf[fs(n)][2 * u + 1]), hf[n], a2[fm(n)][u]);
        SB();
        if (u < 2 && n >= 4 && n < 32) rdwh((n >> 2) + 3, n & 3, u);
        if (u == 3 && n + HF < 44) ldh(n + HF);
        if (n >= 35 && (n - 35) % 3 == 0) *(LDS f32x4*)(lds + wdloc((n - 35) / 3) + wl + u * 1024 + lane * 16) = a2[(n - 35) / 3 + 1][u];
        SB();
      }
      if (n == 41) post(DONE, 1);
      if (n == 42) { dn[0] = *(volatile LDS u32x4*)(lds + DONE); dn[1] = *(volatile LDS u32x4*)(lds + DONE + 16); SB(); }
    }
    auto all4 = [](u32x4 d) INL { return rfl(d[0] & d[1] & d[2] & d[3]); };
#pragma unroll 1
    for (unsigned spins = 0; spins < SPIN_CAP && !all4(dn[0]); ++spins) {
      dn[0] = *(volatile LDS u32x4*)(lds + DONE);
      dn[1] = *(volatile LDS u32x4*)(lds + DONE + 16);
    }
    fence();
    f32x4 pp[4][3];
    u32x2 y[4];
    auto rdy = [&]() INL {
#pragma unroll
      for (int u = 0; u < 4; ++u) y[u] = __builtin_bit_cast(u32x2, trd(lds + a_y + (u >> 1) * PAIR + (u & 1) * 4096));
    };
#pragma unroll
    for (int u = 0; u < 4; ++u)
#pragma unroll
      for (int m = 0; m < 3; ++m) pp[u][m] = *(const LDS f32x4*)(lds + a_p[m] + u * 1024);
    rdy();
    SB();
#pragma unroll
    for (int u = 0; u < 4; ++u) a2[0][u] += (pp[u][0] + pp[u][1]) + pp[u][2];
    asm volatile("" : "+v"(a2[0][0]), "+v"(a2[0][1]), "+v"(a2[0][2]), "+v"(a2[0][3]));
    if (__builtin_expect(!all4(dn[1]), 0)) {
#pragma unroll 1
      for (unsigned spins = 0; spins < SPIN_CAP && !all4(*(volatile LDS u32x4*)(lds + DONE + 16)); ++spins) {}
      fence();
      rdy();
    }
#pragma unroll
    for (int u = 0; u < 4; ++u) {
      const f32x4 s = a2[0][u] + f32x4{blo(y[u][0]), bhi(y[u][0]), blo(y[u][1]), bhi(y[u][1])};
      *(LDS u32x2*)(lds + a_o[0] + u * 32) = u32x2{pk2(s[0], s[1]), pk2(s[2], s[3])};
    }
    fence();
    const u32x4 o0 = *(const LDS u32x4*)(lds + a_o[1]), o1 = *(const LDS u32x4*)(lds + a_o[1] + 8 * 144);
    st16(o0, mkr(out, L * D * 2), v_o, (m2 * 64 + w4 * 16) * 1024 + n2 * 128);
    st16(o1, mkr(out, L * D * 2), v_o, (m2 * 64 + w4 * 16 + 8) * 1024 + n2 * 128);
    asm volatile(".Lk_code_end0:");
  } else {
    const int cl = nw, hh = mw;
    addr_x();
    addr_w();
    asm volatile("" : "+v"(vx[0]), "+v"(vx[1]), "+v"(vw[0]), "+v"(vw[1]), "+v"(vw[2]));
    auto part = [&](int s, int i0, int i1) INL {
      if (__builtin_expect(gemm, 1)) {
#pragma unroll
        for (int i = i0; i < i1; ++i) issue(s, i);
      }
    };
    auto stage = [&](int s) INL { part(s, 0, 10); };
    unsigned vd[4], vcs, pa[4], vxi, vzero, vimg, vxf, vtw[2], vtab[2], vex[2], vyf, vys, vco, vxt[2], vyl, vyw;
#pragma unroll
    for (int i = 0; i < 4; ++i) { const int row = lane >> 3, f = ((row >> 1) & 1) + 2 * (i & 1); vd[i] = (w4 * 352 + i * 8 + row) * 1024 + n2 * 128 + (((lane & 7) ^ (2 * f)) << 4); }
    vcs = tid < 96 ? (n0 + tid) * 8 : (CSP + n0 + min(tid, 191) - 96) * 8;
#pragma unroll
    for (int i = 0; i < 4; ++i) pa[i] = SBASE + cl * PAIR + (j16 & 1 ? TX : TY) + (847 - 256 * i - hh * 64 - ((15 + j16 - 8 * g) >> 1)) * 4;
    vxi = (tid >> 1) * XNP + (tid & 1) * 16 + cp * 32;
    vimg = SBASE + (tid & 1) * PAIR + XIMG + (tid >> 1) * 16;
    vzero = lane * 16 + (hh ? SYNC : SBASE + cl * PAIR + (lane < 2 ? TY + 4096 : TX + 4064));
    vtab[0] = SBASE + cl * PAIR + (255 - (hh ? (lane >> 5 ? 64 : 128) + (lane & 31) : lane)) * 16;
    vtab[1] = vtab[0] - (hh ? 32 : 192) * 16;
    vex[0] = (hh ? SBASE + cl * PAIR + CEX : SLOT + LEFT + cl * 4096) + lane * 16;
    vex[1] = (hh ? SLOT + LEFT + cl * 4096 : SBASE + cl * PAIR + CEX) + lane * 16;
    vyf = lane < 4 ? WS_YF + ((lane & 2) * 2 + (hh ? 1 + (lane & 1) : 3 * (lane & 1))) * (256 * YST) + (ch >> 3) * YST + (ch & 7) * 8
                   : lane == 63 && b == 0 && w4 == 0 ? WS_E : 0x7f000000u;
    vys = ch * 4096 + (hh ? 1 + (g & 1) : 3 * (g & 1)) * 512 + (j16 >> 2) * 128 + (g >> 1) * 64 + (j16 & 3) * 16;
    vco = CSO + (tid < 96 ? tid * 16 : (tid - 96) * 16 + 8);
    vxt[0] = (m2 * 64 + w4 * 16 + (lane >> 3)) * 1024 + n2 * 128 + (((lane & 7) ^ g) << 4);
    vxt[1] = vxt[0] ^ 64;
    vyl = (n2 * 64 + w4 * 16 + (lane >> 3)) * 4096 + m2 * 128 + (lane & 7) * 16;
    vyw = SBASE + cl * PAIR + hh * 4096 + (lane >> 3) * 160 + (lane & 3) * 32 + (lane & 4) * 4;
    asm volatile("" : "+v"(vxt[0]), "+v"(vxt[1]), "+v"(vyl), "+v"(vyw));
    asm volatile("" : "+v"(vco), "+v"(vex[0]), "+v"(vex[1]), "+v"(vyf), "+v"(vys));
    vxf = tid ? 0x7f000000u : WS_XNF + b * 8;
    vtw[0] = lane < 11 ? (b * 43 + w4 * 11 + lane) * 128 : 0x7f000000u;
    vtw[1] = lane < 11 ? (r * 43 + w4 * 11 + lane) * 1024 + ((384u - (unsigned)(u64)wd) & 896u) : 0x7f000000u;
    asm volatile("" : "+v"(vd[0]), "+v"(vd[1]), "+v"(vd[2]), "+v"(vd[3]), "+v"(vcs), "+v"(pa[0]), "+v"(pa[1]), "+v"(pa[2]), "+v"(pa[3]));
    asm volatile("" : "+v"(vxi), "+v"(vimg), "+v"(vzero), "+v"(vxf), "+v"(vtw[0]), "+v"(vtw[1]), "+v"(vtab[0]), "+v"(vtab[1]));
    auto issue_wd = [&](int s, int p) INL {
      const int pos = s % 7;
      dma16(pos == 6 ? wl6 : wl, (pos == 6 ? 0 : wdloc(pos)) + p * 1024, vd[p], rwd, s * 32768);
    };
    __builtin_amdgcn_s_setprio(1);
    waitv<1>();
    get_tag();
    u32x2 tg = {(unsigned)tag, (unsigned)(tag >> 32)};
    asm volatile("" : "+v"(tg));
    float prm[6];
#pragma unroll
    for (int i = 0; i < 6; ++i) asm volatile("v_lshlrev_b32 %0, 16, %1" : "=v"(prm[i]) : "v"(xr[i >> 2][i & 3]));
    const float p_ldt = prm[0], p_lar = prm[1], p_aim = prm[2], p_cr = prm[3], p_ci = prm[4], skip = one(lane ? 0.f : prm[5]);
    const float dt = rb(exp_f(p_ldt));
    const float s2 = -rb(exp_f(p_lar)) * dt * 1.44269504f, th = p_aim * dt;
    const unsigned eb = min(max(wave_max(max(f2u(p_cr) << 1, f2u(p_ci) << 1)) >> 24, 1u), 253u);
    const float sc = u2f((254u - eb) << 23), ksc = 2.f * dt * u2f(eb << 23);
    f32x2 z1 = cpow2(s2, th, 1.f), z8c = cpow2(s2, -th, 8.f), zb = cpow2(s2, th, 256.f);
    asm volatile("" : "+v"(z1), "+v"(z8c), "+v"(zb));
    const cmulr z256 = mul_by(zb);
    LDS char* pr = lds + SBASE + cl * PAIR;
    LDS char* ks = pr + hh * KSW;
    auto put = [&](int row, f32x2 v) INL { *(LDS unsigned*)(ks + row * KROW + lane * 4) = pkh(v[0], v[1]); };
    auto putu = [&](int reg, int h, f32x2 v) INL { put((reg & 3) + 4 * h + 8 * (reg >> 2), v); };
    f16x8 vf[8];
    {
      f32x2 v[4];
      const cmulr m1 = mul_by(z8c);
      v[0] = f32x2{1.f, 0.f}; v[1] = z8c; v[2] = cmul(z8c, m1); v[3] = cmul(v[2], m1);
      const cmulr m4 = mul_by(cmul(v[3], m1));
#pragma unroll
      for (int k = 0; k < 32; ++k) {
        if (k == 4) v[0] = m4.z; else if (k > 4) v[k & 3] = cmul(v[k & 3], m4);
        put(k, v[k & 3]);
      }
      fence();
#pragma unroll
      for (int s = 0; s < 8; ++s) vf[s] = *(const LDS f16x8*)(ks + (lane & 31) * KROW + s * 32 + (lane >> 5) * 16);
      fence();
    }
    {
      f32x2 e[5];
      const cmulr m1 = mul_by(z1), m2_ = mul_by(cmul(z1, m1));
      e[0] = f32x2{p_cr, p_ci} * dup(sc);
      putu(0, 0, e[0]);
#pragma unroll
      for (int k = 0; k < 4; ++k) { putu(2 * k + 1, 0, cmul(e[k], m1)); e[k + 1] = cmul(e[k], m2_); putu(2 * k + 2, 0, e[k + 1]); }
#pragma unroll
      for (int k = 0; k < 5; ++k) e[k] = cmul(e[k], z256);
      putu(0, 1, e[0]); putu(2, 1, e[1]); putu(3, 1, e[2]); putu(4, 1, e[2]); putu(5, 1, e[2]); putu(6, 1, e[3]); putu(7, 1, e[4]); putu(8, 1, e[4]);
#pragma unroll
      for (int k = 0; k < 6; ++k) {
        const int reg = k < 2 ? 9 + 2 * k : k < 5 ? 5 + 2 * k : 13, h = k < 2 || k == 5;
        e[0] = cmul(e[0], z256); putu(reg, h, e[0]);
        e[4] = cmul(e[4], z256); putu(reg + 1, h, e[4]);
        if (k < 2) { e[2] = cmul(e[2], z256); putu(k ? 1 : 15, 1, e[2]); }
        if (k == 5) touch(DUMMY, vtw[0], rwd, 0);
      }
      fence();
    }
    bar();
    f32x16 kd = {};
    {
      f16x8 uf[8];
#pragma unroll
      for (int s = 0; s < 8; ++s) uf[s] = *(const LDS f16x8*)(ks + (lane & 31) * KROW + s * 32 + (lane >> 5) * 16);
      SB();
#pragma unroll
      for (int s = 0; s < 8; ++s) kd = __builtin_amdgcn_mfma_f32_32x32x16_f16(uf[s], vf[s], kd, 0, 0, 0);
    }
    kd *= ksc;
    const bool h = lane >> 5, odd = lane & 1;
    if (hh) {
      *(LDS f32x4*)(pr + KEX1 + lane * 16) = f32x4{kd[0], kd[1], kd[2], kd[3]};
      *(LDS f32x4*)(pr + KEX1 + 1024 + lane * 16) = f32x4{kd[4], kd[5], kd[6], kd[7]};
      *(LDS f32x4*)(pr + KEX1 + 2048 + lane * 16) = f32x4{kd[8], kd[13], kd[14], kd[15]};
    } else {
      *(LDS f32x4*)(pr + KEX0 + lane * 16) = f32x4{kd[9], kd[10], kd[11], kd[12]};
      *(LDS f32x2*)(pr + KEX0 + 1024 + lane * 8) = f32x2{kd[15], kd[1]};
    }
    __builtin_amdgcn_s_setprio(0);
    bar();
    {
      u32x4 tb[5];
      auto tail = [&](float a, float c, float a3, float a4, float a5, int k) INL {
        const unsigned aa = pk2(a, a), cc = pk2(c, c);
        tb[k] = u32x4{cc, pk2(a5, a4), pk2(a3, a), aa};
        tb[k + 1] = u32x4{cc, pk2(c, a5), pk2(a4, a3), aa};
      };
      if (hh) {
        const f32x4 p = *(const LDS f32x4*)(pr + KEX0 + lane * 16);
        const f32x2 q = *(const LDS f32x2*)(pr + KEX0 + 1024 + lane * 8);
#pragma unroll
        for (int k = 0; k < 2; ++k) {
          const float a = one(kd[9 + 2 * k] + p[2 * k]), c = one(kd[10 + 2 * k] + p[2 * k + 1]), m = one(kd[k ? 1 : 15] + q[k]);
          tail(a, c, h ? m : a, h ? m : odd ? c : a, h ? m : c, 2 * k);
        }
        tb[4][0] = 0;
      } else {
        const f32x4 p0 = *(const LDS f32x4*)(pr + KEX1 + lane * 16), p1 = *(const LDS f32x4*)(pr + KEX1 + 1024 + lane * 16);
        const f32x4 p2 = *(const LDS f32x4*)(pr + KEX1 + 2048 + lane * 16);
        float t[9];
#pragma unroll
        for (int k = 0; k < 9; ++k) t[k] = one(kd[k] + (k < 4 ? p0[k] : k < 8 ? p1[k - 4] : p2[0]));
        t[0] += skip;
        t[1] = h ? t[0] : t[1];
        tb[0] = u32x4{pk2(t[7], t[6]), pk2(t[5], t[4]), pk2(t[3], t[2]), pk2(t[1], t[0])};
        tb[1] = u32x4{pk2(t[8], t[7]), pk2(t[6], t[5]), pk2(t[4], t[3]), pk2(t[2], t[1])};
        const float a = one(kd[13] + p2[1]), c = one(kd[14] + p2[2]);
        tail(a, c, a, odd ? c : a, c, 2);
        tb[4][0] = lane ? 0 : tb[0][3] >> 16;
      }
      tb[4][1] = tb[4][2] = tb[4][3] = 0;
      asm volatile("" : "+v"(tb[0]), "+v"(tb[1]), "+v"(tb[2]), "+v"(tb[3]), "+v"(tb[4]));
#pragma unroll
      for (int k = 0; k < 4; ++k) *(LDS u32x4*)(lds + vtab[k >> 1] + (k & 1 ? TY : TX)) = tb[k];
      if (lane < (hh ? 5 : 36)) *(LDS u32x4*)(lds + vzero) = tb[4];
      part(2, 0, 10 - D2F);
    }
    bar();
    __builtin_amdgcn_raw_buffer_store_b64(tg, rws, vxf, 0, SC1);
    stage(3);
    waitv<10>();
    bar();
    stage(4);
    waitv<10>();
    bar();
    stage(5);
    waitv<10>();
    bar();
    stage(6);
    touch(DUMMY, vtw[1], rwd, 0);
    waitv<11>();
    bar();
    {
      u64 fl;
      asm volatile("buffer_load_dwordx2 %0, %1, %2, %3 offen sc1" : "=v"(fl) : "v"(tid * 8), "s"(rws), "s"(WS_XNF) : "memory");
      stage(7);
      if (gemm) waitv<10>(); else waitv<0>();
      asm volatile("" : "+v"(fl));
      if (__builtin_expect(!wave_all(fl == tag), 0)) poll<4>(ws, WS_XNF + tid * 8, tag);
    }
    bar();
#pragma unroll
    for (int i = 0; i < 2; ++i) dma16(cl * PAIR + hh * 2048, SBASE + CEX + i * 1024, vxt[i], rx, i * 8192);
    unsigned gst; u32x2 csg; u32x4 xi[2];
    asm volatile("buffer_load_dword %0, %1, %2, %3 offen sc1" : "=v"(gst) : "v"((tid & 127) * 4), "s"(rws), "s"(WS_ST + (mt & 15) * 512) : "memory");
    asm volatile("buffer_load_dwordx2 %0, %1, %2, %3 offen sc1" : "=v"(csg) : "v"(vcs), "s"(rws), "s"(WS_CS) : "memory");
#pragma unroll
    for (int i = 0; i < 2; ++i)
      asm volatile("buffer_load_dwordx4 %0, %1, %2, %3 offen sc1" : "=v"(xi[i]) : "v"(vxi), "s"(rws), "s"(WS_XNT + i * (128 * XNP)) : "memory");
#pragma unroll
    for (int i = 0; i < 4; ++i) issue_wd(0, i);
    waitv<10>();
    bar();
    waitv<6>();
    asm volatile("" : "+v"(gst), "+v"(csg));
    if (tid < 128) *(LDS u32x2*)(lds + STO + tid * 8) = u32x2{gst & 0xffff0000u, __builtin_amdgcn_perm(gst, gst, 0x01000100)};
    if (__builtin_expect(gemm && !wave_all(csg[1] == (unsigned)tag), 0)) {
#pragma unroll 1
      for (unsigned spins = 0; spins < SPIN_CAP && !wave_all(csg[1] == (unsigned)tag); ++spins) {
        __builtin_amdgcn_s_sleep(2);
        asm volatile("buffer_load_dwordx2 %0, %1, %2, %3 offen sc1\n\ts_waitcnt vmcnt(0)" : "=&v"(csg) : "v"(vcs), "s"(rws), "s"(WS_CS) : "memory");
      }
    }
    if (tid < 192) *(LDS u32x2*)(lds + vco) = u32x2{csg[0], csg[0]};
#pragma unroll
    for (int i = 0; i < 4; ++i) issue_wd(1, i);
    bar();
#pragma unroll
    for (int i = 8; i < 16; ++i) issue_wd(i >> 2, i & 3);
    waitv<8>();
    post(LANDED, 1);
    asm volatile("" : "+v"(xi[0]), "+v"(xi[1]));
#pragma unroll
    for (int i = 0; i < 2; ++i) *(LDS u32x4*)(lds + vimg + XPAD + i * 2048) = xi[i];
#pragma unroll
    for (int i = 16; i < 28; ++i) issue_wd(i >> 2, i & 3);
    waitv<12>();
    post(LANDED, 2);
    if (NPRE > 12) await(USED, 0, 1);
#pragma unroll
    for (int i = 28; i < 16 + NPRE; ++i) issue_wd(i >> 2, i & 3);
    LDS char* pb = pr + XIMG + XPAD + (16 * j16 + 8 * g - 16) * 2 - hh * 256 - 192;
    CvFrag cf[2];
    auto rdc = [&](CvFrag& f, int rr, int k) INL {
      const int i = k / 3, c = k % 3;
      if (c == 2) f.b[i] = *(const LDS s16x8*)(pb + i * 512 + (3 - rr) * 64);
      else f.a[2 * i + c] = *(const LDS s16x4u*)(lds + pa[i >> 1] + (i & 1 ? 0 : 512) + (3 - rr) * 64 + c * 8);
    };
#pragma unroll
    for (int k = 0; k < 24; ++k) if (k % 3 < 2) rdc(cf[0], 0, k);
    bar();
#pragma unroll
    for (int k = 2; k < 24; k += 3) rdc(cf[0], 0, k);
    __builtin_amdgcn_s_setprio(1);
    unsigned seen = 0;
    f32x4 ca[8] = {};
    auto conv = [&](int rr) INL {
      int n = 0;
#pragma unroll
      for (int u = 0; u < 8; ++u)
#pragma unroll
        for (int t = u; t < 8; ++t) {
          ca[t] = mfma16(cat(cf[rr & 1].a[2 * u], cf[rr & 1].a[2 * u + 1]), cf[rr & 1].b[t - u], ca[t]);
          SB();
          if (n < 32 && n % 4 < 3) rdc(cf[(rr + 1) & 1], rr + 1, n / 4 * 3 + n % 4);
          const int q = rr * 36 + n;
          const int j = 16 + NPRE + (q - Q0) / PACE;
          if (q >= Q0 && (q - Q0) % PACE == 0 && j < 44) {
            if (j == 28 || j == 36) await(USED, seen, j == 28 ? 1 : 2);
            if (j == 27 || j == 35) seen = peek(USED);
            issue_wd(j >> 2, j & 3);
          }
          SB();
          ++n;
        }
    };
    conv(0); conv(1); conv(2);
    {
      unsigned o[4][2];
      auto last = [&](int h) INL {
        auto tile = [](int set, int k) INL { return (k >> 1) * 4 + (set ? 1 + (k & 1) : 3 * (k & 1)); };
        unsigned mate;
        f32x4 in[4];
#pragma unroll
        for (int ph = 0; ph < 2; ++ph) {
          int n = 0;
#pragma unroll
          for (int u = 0; u < 8; ++u)
#pragma unroll
            for (int k = 3; k >= 0; --k) {
              const int t = tile(ph ? h : 1 - h, k);
              if (t < u) continue;
              ca[t] = mfma16(cat(cf[1].a[2 * u], cf[1].a[2 * u + 1]), cf[1].b[t - u], ca[t]);
              SB();
              if (ph && n < 4) *(LDS f32x4*)(lds + vex[0] + n * 1024) = ca[tile(1 - h, n)];
              if (ph && n == 4) post(SYNC, 1);
              if (ph && n == 6) { waitv<8>(); post(LANDED, 3); }
              if (ph && n == 10) mate = *(volatile LDS unsigned*)(lds + SYNC + (w4 ^ 1) * 4);
              SB();
              ++n;
            }
        }
        if (__builtin_expect(!rfl(mate), 0)) lds_wait(lds + SYNC + (w4 ^ 1) * 4, 1);
        fence();
#pragma unroll
        for (int k = 0; k < 4; ++k) in[k] = *(const LDS f32x4*)(lds + vex[1] + k * 1024);
        SB();
#pragma unroll
        for (int k = 0; k < 4; ++k) {
          const f32x4 s = ca[tile(h, k)] + in[k];
          o[k][0] = pk2(s[0], s[1]); o[k][1] = pk2(s[2], s[3]);
        }
      };
      if (hh) last(1); else last(0);
      __builtin_amdgcn_s_setprio(1);
#pragma unroll
      for (int kp = 0; kp < 2; ++kp) {
        const auto r0 = __builtin_amdgcn_permlane16_swap(o[2 * kp][0], o[2 * kp + 1][0], false, false);
        const auto r1 = __builtin_amdgcn_permlane16_swap(o[2 * kp][1], o[2 * kp + 1][1], false, false);
        const u32x4 ov = {r0[0], r1[0], r0[1], r1[1]};
        st16(ov, rws, vys, WS_YT + kp * 2048);
      }
      waitv<2>();
      post(LANDED, 4);
      waitv<0>();
      __builtin_amdgcn_raw_buffer_store_b64(tg, rws, vyf, 0, SC1);
    }
    __builtin_amdgcn_s_sleep(YSLEEP);
    const unsigned yf = WS_YF + ((m2 & -4) * 64 + n2 * 8 + w4 * 2) * YST;
    if (!watch<2>(ws + yf, tag)) poll<NAP>(ws, yf + (j16 >> 3) * YST + (j16 & 7) * 8, tag);
#pragma unroll
    for (int i = 0; i < 2; ++i) xi[i] = __builtin_amdgcn_raw_buffer_load_b128(rws, vyl, WS_YT + i * 32768, SC1);
#pragma unroll
    for (int i = 0; i < 2; ++i) *(LDS u32x4*)(lds + vyw + i * 1280) = xi[i];
    post(DONE + 16, 1);
    waitl0();
    asm volatile(".Lk_code_end1:");
  }
}

}

extern "C" void kernel_launch(void* const* d_in, const int* in_sizes, int n_in, void* d_out, int out_size, void* d_ws, size_t ws_size,
                              hipStream_t stream) {
  fused_kernel<<<256, 512, 0, stream>>>((const u16*)d_in[0], (const u16*)d_in[1], (const u16*)d_in[2], (const u16*)d_in[3], (const u16*)d_in[4],
                                        (const u16*)d_in[5], (const u16*)d_in[6], (const u16*)d_in[8], (const u16*)d_in[9], (const u16*)d_in[10],
                                        (u16*)d_out, (char*)d_ws);
}
```

```cpp
#include <hip/hip_runtime.h>
#include <hip/hip_bf16.h>

namespace {

typedef unsigned short u16;
typedef unsigned long long u64;
typedef __attribute__((ext_vector_type(4))) short s16x4;
typedef s16x4 __attribute__((aligned(4))) s16x4u;
typedef __attribute__((ext_vector_type(8))) short s16x8;
typedef __attribute__((ext_vector_type(8))) __bf16 bf16x8;
typedef __attribute__((ext_vector_type(2))) __bf16 bf16x2;
typedef __attribute__((ext_vector_type(8))) _Float16 f16x8;
typedef __attribute__((ext_vector_type(2))) _Float16 f16x2;
typedef __attribute__((ext_vector_type(2))) float f32x2;
typedef __attribute__((ext_vector_type(4))) float f32x4;
typedef __attribute__((ext_vector_type(16))) float f32x16;
typedef __attribute__((ext_vector_type(4))) unsigned u32x4;
typedef __attribute__((ext_vector_type(2))) unsigned u32x2;
typedef __amdgpu_buffer_rsrc_t rsrc_t;
#define LDS __attribute__((address_space(3)))
#define GLB __attribute__((address_space(1)))
#define INL __attribute__((always_inline))
#define SB() __builtin_amdgcn_sched_barrier(0)
#define RLX_AGENT __ATOMIC_RELAXED, __HIP_MEMORY_SCOPE_AGENT

constexpr int L = 2048, D = 512, NM = 128, F = 1376, NU = F / 16;
constexpr int SC1 = 16;
constexpr int HF = 10;
constexpr int D2F = 6;
constexpr int RD0 = 1, RD1 = 3, B9Q = 5;
constexpr int NPRE = 16, Q0 = 3, PACE = 4;
constexpr int HSLEEP = 8, YSLEEP = 14, NAP = 2;
constexpr int YST = 128;
static_assert(Q0 + (27 - NPRE) * PACE < 108 && RD0 < RD1 && RD1 < B9Q && B9Q < 6, "schedule");
constexpr unsigned SPIN_CAP = 1u << 18, LDS_CAP = 1u << 26;

constexpr int WBLK = 3104, GOFF = 16384, UOFF = GOFF + 4 * WBLK, SLOT = GOFF + 8 * WBLK;
constexpr int LEFT = 32768;
constexpr int LNB = 2 * SLOT, LNROW = 1088;
constexpr int SBASE = 3 * SLOT;
constexpr int KROW = 272, KSW = 32 * KROW, PAIR = 2 * KSW;
constexpr int TX = 0, XIMG = 4160, XPAD = 480, CEX = 8736, TY = 13248, KEX0 = 7168, KEX1 = KSW;
constexpr int STO = SBASE + 2 * PAIR, CSO = STO + 1024, SYNC = CSO + 1536, LANDED = SYNC + 16, USED = SYNC + 32, DONE = SYNC + 48, DUMMY = SYNC + 128;
constexpr int LDS_BYTES = 163840;
static_assert(DUMMY + 256 <= LDS_BYTES && XIMG + XPAD + 4096 <= CEX && CEX + 4096 <= TY && TY + 4128 <= PAIR, "LDS map");

constexpr int WS_E = 0, WS_XNF = 4096, WS_HF = 65536, WS_YF = 196608, WS_ST = 32768, WS_CS = 40960;
constexpr int XNP = 8320, WS_XNT = 1 << 19, WS_YT = 3 << 20, WS_H = 5 << 20, WS_END = WS_H + 64 * NU * 1024;
constexpr int CSP = 1408;

__device__ __forceinline__ float u2f(unsigned v) { return __builtin_bit_cast(float, v); }
__device__ __forceinline__ unsigned f2u(float v) { return __builtin_bit_cast(unsigned, v); }
__device__ __forceinline__ bool wave_all(bool p) { return __builtin_amdgcn_ballot_w64(!p) == 0; }
__device__ __forceinline__ unsigned rfl(unsigned v) { return __builtin_amdgcn_readfirstlane(v); }
__device__ __forceinline__ rsrc_t mkr(const void* p, unsigned n) { return __builtin_amdgcn_make_buffer_rsrc((void*)p, 0, n, 0x00020000); }
__device__ __forceinline__ void dma16(unsigned base, int dst, unsigned voff, rsrc_t r, unsigned soff, int off = 0) {
  asm volatile("s_add_u32 m0, %0, %1\n\ts_nop 0\n\tbuffer_load_dwordx4 %2, %3, %4 offen offset:%5 lds"
               ::"s"(base), "n"(dst - off), "v"(voff), "s"(r), "s"(soff), "n"(off) : "scc", "memory");
}
__device__ __forceinline__ void touch(unsigned m0v, unsigned voff, rsrc_t r, unsigned soff) {
  asm volatile("s_mov_b32 m0, %0\n\ts_nop 0\n\tbuffer_load_dword %1, %2, %3 offen lds" ::"s"(m0v), "v"(voff), "s"(r), "s"(soff) : "memory");
}
__device__ __forceinline__ void st16(u32x4 v, rsrc_t r, unsigned voff, unsigned soff) {
  asm volatile("buffer_store_dwordx4 %0, %1, %2, %3 offen sc1\n\ts_nop 1" ::"v"(v), "v"(voff), "s"(r), "s"(soff) : "memory");
}
template <int W> __device__ __forceinline__ void waitv() { asm volatile("s_waitcnt vmcnt(%0)" ::"n"(W) : "memory"); }
__device__ __forceinline__ void waitl0() { asm volatile("" ::: "memory"); __builtin_amdgcn_s_waitcnt(0xc07f); asm volatile("" ::: "memory"); }
__device__ __forceinline__ void fence() { asm volatile("" ::: "memory"); }
__device__ __forceinline__ void bar() { waitl0(); __builtin_amdgcn_s_barrier(); fence(); }
__device__ __forceinline__ unsigned pk2(float a, float b) { f32x2 p = {a, b}; return __builtin_bit_cast(unsigned, __builtin_convertvector(p, bf16x2)); }
__device__ __forceinline__ unsigned pkh(float a, float b) { f32x2 p = {a, b}; return __builtin_bit_cast(unsigned, __builtin_convertvector(p, f16x2)); }
__device__ __forceinline__ float blo(unsigned v) { return u2f(v << 16); }
__device__ __forceinline__ float bhi(unsigned v) { return u2f(v & 0xffff0000u); }
__device__ __forceinline__ float rb(float a) { return blo(pk2(a, a)); }
template <int C, int RM> __device__ __forceinline__ float dppf(float v) {
  return __builtin_bit_cast(float, __builtin_amdgcn_update_dpp(0, __builtin_bit_cast(int, v), C, RM, 0xf, true));
}
__device__ __forceinline__ float lane63(float v) { return __builtin_bit_cast(float, __builtin_amdgcn_readlane(__builtin_bit_cast(int, v), 63)); }
__device__ __forceinline__ float wave_sum(float v) {
  v += dppf<0x111, 0xf>(v); v += dppf<0x112, 0xf>(v); v += dppf<0x114, 0xf>(v); v += dppf<0x118, 0xf>(v);
  v += dppf<0x142, 0xa>(v); v += dppf<0x143, 0xc>(v);
  return lane63(v);
}
template <int C, int RM> __device__ __forceinline__ unsigned dppmax(unsigned v) {
  const unsigned o = __builtin_amdgcn_update_dpp(0, v, C, RM, 0xf, true);
  return v > o ? v : o;
}
__device__ __forceinline__ unsigned wave_max(unsigned v) {
  v = dppmax<0x111, 0xf>(v); v = dppmax<0x112, 0xf>(v); v = dppmax<0x114, 0xf>(v); v = dppmax<0x118, 0xf>(v);
  v = dppmax<0x142, 0xa>(v); v = dppmax<0x143, 0xc>(v);
  return __builtin_amdgcn_readlane(v, 63);
}
__device__ __forceinline__ s16x8 cat(s16x4 a, s16x4 b) { return __builtin_shufflevector(a, b, 0, 1, 2, 3, 4, 5, 6, 7); }
__device__ __forceinline__ s16x4 trd(LDS char* p) { return __builtin_amdgcn_ds_read_tr16_b64_v4i16((LDS s16x4*)p); }
__device__ __forceinline__ f32x4 mfma16(s16x8 a, s16x8 b, f32x4 c) {
  return __builtin_amdgcn_mfma_f32_16x16x32_bf16(__builtin_bit_cast(bf16x8, a), __builtin_bit_cast(bf16x8, b), c, 0, 0, 0);
}
template <int S> __device__ __forceinline__ void poll(const char* ws, unsigned off, u64 tag) {
  GLB u64* p = (GLB u64*)(ws + off);
#pragma unroll 1
  for (unsigned spins = 0; spins < SPIN_CAP; ++spins) {
    if (wave_all(__hip_atomic_load(p, RLX_AGENT) == tag)) break;
    __builtin_amdgcn_s_sleep(S);
  }
  fence();
}
typedef __attribute__((ext_vector_type(8))) unsigned long long u64x8;
template <int N> __device__ __forceinline__ bool watch(const char* p, u64 tag) {
#pragma unroll 1
  for (unsigned spins = 0; spins < 128; ++spins) {
    u64x8 f[2];
    u64 d = 0;
    if constexpr (N == 1) asm volatile("s_load_dwordx16 %0, %1, 0 glc\n\ts_waitcnt lgkmcnt(0)" : "=&s"(f[0]) : "s"(p) : "memory");
    else asm volatile("s_load_dwordx16 %0, %2, 0 glc\n\ts_load_dwordx16 %1, %2, %3 glc\n\ts_waitcnt lgkmcnt(0)" : "=&s"(f[0]), "=&s"(f[1]) : "s"(p), "n"(YST) : "memory");
#pragma unroll
    for (int i = 0; i < 8 * N; ++i) { d |= f[i >> 3][i & 7] ^ tag; asm("" : "+s"(d)); }
    if (d == 0) return true;
    __builtin_amdgcn_s_sleep(NAP);
  }
  return false;
}
__device__ __forceinline__ void lds_wait(LDS char* p, unsigned v) {
#pragma unroll 1
  for (unsigned spins = 0; spins < LDS_CAP; ++spins) { if (rfl(*(volatile LDS unsigned*)p) >= v) break; __builtin_amdgcn_s_sleep(1); }
  fence();
}
__device__ __forceinline__ f32x2 fma2(f32x2 a, f32x2 b, f32x2 c) { return __builtin_elementwise_fma(a, b, c); }
__device__ __forceinline__ float one(float v) { asm("" : "+v"(v)); return v; }
__device__ __forceinline__ f32x2 dup(float a) { return f32x2{a, a}; }
struct cmulr { f32x2 z, rot; };
__device__ __forceinline__ cmulr mul_by(f32x2 z) { return {z, f32x2{-z[1], z[0]}}; }
__device__ __forceinline__ f32x2 cmul(f32x2 a, cmulr m) { return fma2(dup(a[0]), m.z, dup(a[1]) * m.rot); }
__device__ __forceinline__ f32x2 cpow2(float s2, float th, float p) {
  const float t = th * p, n = __builtin_rintf(t * 0.15915494f);
  const float fr = __builtin_fmaf(t, 0.15915494f, -n) + t * 6.4206382e-9f;
  const float e = __builtin_amdgcn_exp2f(s2 * p);
  return f32x2{e * __builtin_amdgcn_cosf(fr), e * __builtin_amdgcn_sinf(fr)};
}
__device__ __forceinline__ float exp_f(float v) {
  const float t = v * 1.44269504f, e = __builtin_fmaf(v, 1.925963033e-8f, __builtin_fmaf(v, 1.44269504f, -t));
  const float y = __builtin_amdgcn_exp2f(t);
  return __builtin_fmaf(y, e * 0.69314718f, y);
}


struct G1Frag { s16x8 x[4]; s16x4 w[12]; };
struct CvFrag { s16x4 a[16]; s16x8 b[8]; };

__global__ __launch_bounds__(512, 2) void fused_kernel(const u16* x, const u16* lar, const u16* aim, const u16* cre, const u16* cim, const u16* ldt,
                                                       const u16* dsk, const u16* wg, const u16* wu, const u16* wd, u16* out, char* ws) {
  __shared__ __attribute__((aligned(16))) char lds_raw[LDS_BYTES];
  LDS char* lds = (LDS char*)lds_raw;
  int w = rfl(threadIdx.x >> 6);
  const int lane = threadIdx.x & 63, tid = threadIdx.x & 255;
  const int b = blockIdx.x, xcd = b & 7, r = b >> 3;
  if (w == 7) {
    u64 pc; unsigned l0, l1;
    asm volatile("s_getpc_b64 %0\n.Lk_code_begin:\n\ts_mov_b32 %1, .Lk_code_end0-.Lk_code_begin\n\ts_mov_b32 %2, .Lk_code_end1-.Lk_code_begin" : "=s"(pc), "=s"(l0), "=s"(l1));
    touch(DUMMY, lane * 1024, mkr((const void*)pc, max(l0, l1) - 896), (r & 7) * 128);
  }
  const int row0 = (xcd & 1) * 1024 + (xcd >> 1) * 256 + r * 8;
  const unsigned vt = lane < 16 ? (lane >> 3) * (64 * F * 2) + (lane & 7) * 96 : 0x7f000000u;
  const unsigned so_t = ((w & 3) * 128 + (xcd & 1) * 32 + r) * (F * 2) + (xcd >> 1) * 688;
  asm volatile("" ::"s"(row0), "v"(vt), "s"(so_t));
  asm volatile("" : "+s"(x), "+s"(lar), "+s"(aim), "+s"(cre), "+s"(cim), "+s"(ldt), "+s"(dsk), "+s"(wg), "+s"(wu), "+s"(wd), "+s"(out), "+s"(ws));
  const rsrc_t rx = mkr(x, L * D * 2);
  const int w4 = w & 3, mw = w & 1, nw = w4 >> 1;
  const int cp = xcd * 32 + r, ch = cp * 2 + nw;
  u32x4 xr[2];
  u64 tag;
  if (w < 4) {
#pragma unroll
    for (int i = 0; i < 2; ++i)
      asm volatile("buffer_load_dwordx4 %0, %1, %2, %3 offen" : "=v"(xr[i]) : "v"(lane * 16), "s"(rx), "s"((row0 + 2 * w4 + i) * 1024) : "memory");
  } else {
    const unsigned pi = (ch * NM + mw * 64 + lane) * 2;
    asm volatile("global_load_ushort %0, %6, %8\n\tglobal_load_ushort %1, %7, %9\n\tglobal_load_ushort %2, %7, %10\n\tglobal_load_ushort %3, %7, %11\n\t"
                 "global_load_ushort %4, %7, %12\n\tglobal_load_ushort %5, %6, %13"
                 : "=&v"(xr[0][0]), "=&v"(xr[0][1]), "=&v"(xr[0][2]), "=&v"(xr[0][3]), "=&v"(xr[1][0]), "=&v"(xr[1][1])
                 : "v"(ch * 2), "v"(pi), "s"(ldt), "s"(lar), "s"(aim), "s"(cre), "s"(cim), "s"(dsk) : "memory");
  }
  u64 epoch;
  asm volatile("s_load_dwordx2 %0, %1, 0x0" : "=s"(epoch) : "s"(ws) : "memory");
  __builtin_amdgcn_s_barrier();
  touch(DUMMY, vt, mkr(w >> 2 ? wu : wg, D * F * 2), so_t);
  auto get_tag = [&]() INL {
    unsigned lo, hi;
    asm volatile("s_waitcnt lgkmcnt(0)\n\ts_add_u32 %0, %2, 0x7F4A7C15\n\ts_addc_u32 %1, %3, 0x9E3779B9"
                 : "=&s"(lo), "=s"(hi) : "s"((unsigned)epoch), "s"((unsigned)(epoch >> 32)) : "scc", "memory");
    tag = (u64)hi << 32 | lo;
  };
  asm volatile("" : "+s"(w));
  SB();

  const rsrc_t rws = mkr(ws, WS_END), rwd = mkr(wd, F * D * 2);
  const bool gemm = r < 30;
  const int idx = (xcd >> 1) * 30 + r, nt = idx >> 3, mt = (xcd & 1) * 8 + (idx & 7);
  const int n0 = nt < 14 ? nt * 96 : 1280;
  const int m2 = (xcd & 1) * 16 + (xcd >> 1) * 4 + (r >> 3), n2 = r & 7;
  const int j16 = lane & 15, g = lane >> 4;

  const rsrc_t rw = mkr((gemm ? nw : xcd >> 2) ? wu : wg, D * F * 2);
  unsigned vx[2], vw[3], lx = w4 * 4096, lw = (nw ? UOFF : GOFF) + mw * 2 * WBLK;
  asm volatile("" : "+s"(lx), "+s"(lw));
  auto addr_x = [&]() INL {
#pragma unroll
    for (int i = 0; i < 2; ++i) {
      const int row = i * 8 + (lane >> 3), id = (row >> 1) & 7, f = (id & 4) | ((id & 1) << 1) | ((id >> 1) & 1);
      vx[i] = (mt * 128 + w4 * 32 + row) * 1024 + (((lane & 7) ^ f) << 4);
    }
  };
  auto addr_w = [&]() INL {
    const int kr = (lane * 171) >> 11, c = lane - kr * 12;
    vw[0] = (mw * 32 + kr) * (F * 2) + c * 16 + n0 * 2;
    vw[1] = vw[0] + (c >= 8 ? 6 * F * 2 - 128 : 5 * F * 2 + 64);
    vw[2] = vw[0] + (c >= 4 ? 11 * F * 2 - 64 : 10 * F * 2 + 128);
  };
  auto issue = [&](int s, int i) INL {
    const int slot = (s % 3) * SLOT, j = i - 4;
    if (i < 4) dma16(lx, slot + i * 1024, vx[i & 1], rx, s * 128 + (i >> 1) * 16384);
    else dma16(lw, slot + (j / 3) * WBLK + (j % 3) * 1024, vw[j % 3], rw, s * (64 * F * 2) + (j / 3) * 16 * F * 2);
  };
  const unsigned wl = w4 * 8192, wl6 = (nw ? 0 : 2 * SLOT) + LEFT + mw * 4096;
  auto wdloc = [](int pos) INL { return ((pos / 2 + 2) % 3) * SLOT + (pos & 1) * 4096; };

  auto post = [&](int word, unsigned v) INL { fence(); *(volatile LDS unsigned*)(lds + word + w4 * 4) = v; fence(); };
  auto peek = [&](int word) INL { return *(volatile LDS unsigned*)(lds + word + w4 * 4); };
  auto await = [&](int word, unsigned seen, unsigned v) INL { if (__builtin_expect(rfl(seen) < v, 0)) lds_wait(lds + word + w4 * 4, v); fence(); };

  if (__builtin_expect(w < 4, 1)) {
    auto layernorm = [&](auto&& hook) INL {
      get_tag();
      u32x2 stat;
#pragma unroll
      for (int i = 0; i < 2; ++i) {
        f32x2 v[4];
#pragma unroll
        for (int e = 0; e < 4; ++e) asm volatile("v_lshlrev_b32 %0, 16, %2\n\tv_and_b32 %1, 0xffff0000, %2" : "=&v"(v[e][0]), "=v"(v[e][1]) : "v"(xr[i][e]));
        hook(5 * i);
        const float x0 = u2f(rfl(f2u(v[0][0])));
#pragma unroll
        for (int e = 0; e < 4; ++e) v[e] -= dup(x0);
        const f32x2 s1 = (v[0] + v[1]) + (v[2] + v[3]), s2 = fma2(v[0], v[0], v[1] * v[1]) + fma2(v[2], v[2], v[3] * v[3]);
        float t1 = s1[0] + s1[1], t2 = s2[0] + s2[1];
        asm volatile("" : "+v"(t1), "+v"(t2));
        hook(5 * i + 1);
        const float md = wave_sum(t1) * (1.f / D);
        float var = __builtin_fmaf(-md, md, wave_sum(t2) * (1.f / D));
        asm volatile("" : "+v"(var));
        hook(5 * i + 2);
        const float m = rb(x0 + md), sd = rb(__builtin_amdgcn_sqrtf(rb(rb(__builtin_fmaxf(var, 0.f)) + 1e-5f))), rs = __builtin_amdgcn_rcpf(sd);
        float c = (x0 - m) * rs;
        asm volatile("" : "+v"(c));
        hook(5 * i + 3);
        u32x4 o;
#pragma unroll
        for (int e = 0; e < 4; ++e) { const f32x2 n = fma2(v[e], dup(rs), dup(c)); o[e] = pk2(n[0], n[1]); }
        *(LDS u32x4*)(lds + LNB + (2 * w4 + i) * LNROW + lane * 16) = o;
        hook(5 * i + 4);
        stat[i] = pk2(m, sd);
      }
      __builtin_amdgcn_raw_buffer_store_b64(stat, rws, lane ? 0x7f000000u : 0u, WS_ST + (row0 + 2 * w4) * 4, SC1);
      bar();
#pragma unroll
      for (int i = 0; i < 2; ++i) {
        LDS char* p = lds + LNB + (j16 >> 2) * LNROW + (i * 256 + w4 * 64 + g * 16 + (j16 & 3) * 4) * 2;
        st16(__builtin_bit_cast(u32x4, cat(trd(p), trd(p + 4 * LNROW))), rws, tid * 16, WS_XNT + (row0 >> 3) * XNP + i * 4096);
      }
    };
    unsigned ad[3][4], vh[4], vpoll, kh, kh10, a_xt, vflag = 0x7f000000u;
    u32x2 xt[4];
    auto rdxt = [&](int u) INL { xt[u] = *(const LDS u32x2*)(lds + SBASE + CEX - 32 * 1024 + (a_xt ^ (u * 32))); };
    s16x4 wf[11][8];
    auto wd_addr = [&](int i) INL {
      const int q = j16 >> 2, f = ((q >> 1) & 1) + 2 * (g & 1);
#pragma unroll
      for (int u = 0; u < 4; ++u) {
        ad[i][u] = (i == 0 ? wl : i == 1 ? wl + 2 * SLOT : wl6) + (8 * g + q) * 128 + ((u ^ f) << 5) + (j16 & 3) * 8;
        asm volatile("" : "+v"(ad[i][u]));
      }
    };
    auto rdwh = [&](int s, int u, int h) INL {
      const int pos = s % 7;
      LDS char* p = lds + (pos == 6 ? ad[2][u] : pos < 2 ? ad[1][u] + pos * 4096 : ad[0][u] + wdloc(pos));
      wf[s][2 * u + h] = trd(p + h * 512);
    };
    auto rdwd = [&](int s, int u) INL { rdwh(s, u, 0); rdwh(s, u, 1); };
    auto h_addr = [&]() INL {
      wd_addr(0); wd_addr(1); wd_addr(2);
#pragma unroll
      for (int m = 0; m < 4; ++m) { const int mm = (m + w4) & 3; vh[m] = (mm >> 1) * (NU * 1024) + (g >> 1) * 1024 + (32 * (g & 1) + 16 * (mm & 1) + j16) * 16; }
      vpoll = WS_HF + (m2 * 4 + w4) * 1024;
      a_xt = 32 * 1024 + nw * PAIR + mw * 2048 + j16 * 128 + (((g >> 1) ^ (j16 >> 1)) << 4) + (g & 1) * 8;
      asm volatile("" : "+v"(a_xt), "+v"(vh[0]), "+v"(vh[1]), "+v"(vh[2]), "+v"(vh[3]));
      kh = WS_H + m2 * 2 * NU * 1024 + w4 * (11 * 2048);
      kh10 = w4 == 3 ? 0x7f000000u : kh + 10 * 2048;
      asm volatile("" : "+s"(kh), "+s"(kh10), "+s"(vpoll));
    };

    if (gemm) {
      addr_x();
      addr_w();
      asm volatile("" : "+v"(vx[0]), "+v"(vx[1]), "+v"(vw[0]), "+v"(vw[1]), "+v"(vw[2]));
      unsigned ax[4], aw[2];
      {
        const int id = (j16 >> 1) & 7, f = (id & 4) | ((id & 1) << 1) | ((id >> 1) & 1), cg = 2 * (g & 1) + (g >> 1);
#pragma unroll
        for (int s = 0; s < 2; ++s) { ax[s] = (mw * 64 + j16) * 128 + (((4 * s + cg) ^ f) << 4); ax[s + 2] = ax[s] + 2 * SLOT; }
        aw[0] = GOFF + (g & 1) * WBLK + (8 * (g >> 1) + (j16 >> 2)) * 192 + nw * 96 + (j16 & 3) * 8;
        aw[1] = aw[0] + 2 * SLOT;
      }
      unsigned a_st = STO + (mw * 64 + j16) * 8, a_cs = CSO + (nw * 48 + j16) * 16, so_h[3];
#pragma unroll
      for (int u = 0; u < 3; ++u) {
        const int unit = (n0 >> 4) + nw * 3 + u;
        so_h[u] = nt < 14 || unit >= 84 ? WS_H + ((mt * 4 + mw * 2) * NU + unit) * 1024 : 0x7f000000u;
      }
      const unsigned fi = (nt < 14 ? nt * 2 + nw : nw ? 28 : 63) - 7 * (lane >> 3);
      vflag = lane < 32 && (lane & 7) == 0 && fi < 8 ? WS_HF + (mt * 2 + mw) * 4096 + lane * 128 + fi * 8 : 0x7f000000u;
      asm volatile("" : "+v"(a_st), "+v"(a_cs), "+s"(so_h[0]), "+s"(so_h[1]), "+s"(so_h[2]), "+v"(vflag));
      asm volatile("" : "+v"(ax[0]), "+v"(ax[1]), "+v"(ax[2]), "+v"(ax[3]), "+v"(aw[0]), "+v"(aw[1]));
      h_addr();
      SB();
#pragma unroll
      for (int i = 0; i < 10; ++i) issue(0, i);
      waitv<11>();
      layernorm([&](int k) INL { issue(1, k); });
#pragma unroll
      for (int i = 10 - D2F; i < 10; ++i) issue(2, i);
      f32x4 acc[4][6] = {};
      G1Frag fr[2];
      auto half = [&](G1Frag& c, bool domm, G1Frag& n, bool dord, int s, int jj) INL {
#pragma unroll
        for (int t = 0; t < 24; ++t) {
          if (domm) acc[t & 3][t >> 2] = mfma16(cat(c.w[t >> 2 << 1], c.w[(t >> 2 << 1) + 1]), c.x[t & 3], acc[t & 3][t >> 2]);
          SB();
          const int hi = s % 3 == 2, so = s % 3 == 1 ? SLOT : 0;
#pragma unroll
          for (int k = t; k < min(t + 1, 16); ++k) {
            const int u = (k - 4) >> 1;
            if (k < 4) n.x[k] = *(const LDS s16x8*)(lds + so + ax[jj + 2 * hi] + k * 2048);
            else n.w[k - 4] = trd(lds + aw[hi] + so + jj * 2 * WBLK + (u / 3) * 4 * WBLK + (u % 3) * 32 + (k & 1) * 768);
          }
          SB();
        }
      };
#pragma unroll
      for (int s = 0; s < 8; ++s) {
        if (s == 0) waitv<13 + D2F>(); else if (s == 1) waitv<D2F>(); else if (s == 2) waitv<0>();
        bar();
        half(fr[1], s > 0, fr[0], true, s, 0);
        half(fr[0], true, fr[1], true, s, 1);
      }
      bar();
      u32x2 st[4], bm[4]; s16x8 cs[3];
      float kg[4];
#pragma unroll
      for (int t = 0; t < 24; ++t) {
        acc[t & 3][t >> 2] = mfma16(cat(fr[1].w[t >> 2 << 1], fr[1].w[(t >> 2 << 1) + 1]), fr[1].x[t & 3], acc[t & 3][t >> 2]);
        SB();
        if (t < 4) st[t] = *(const LDS u32x2*)(lds + a_st + t * 128);
        else if (t < 7) cs[t - 4] = *(const LDS s16x8*)(lds + a_cs + (t - 4) * 256);
        else if (t < 11) rdxt(t - 7);
        else if (t >= 12 && t < 16) { bm[t - 12] = u32x2{g ? 0 : st[t - 12][1], 0}; asm volatile("" : "+v"(bm[t - 12])); }
        else if (t >= 16 && t < 20) { kg[t - 16] = -1.44269504f * __builtin_amdgcn_rcpf(u2f(st[t - 16][0])); asm volatile("" : "+v"(kg[t - 16])); }
        SB();
      }
      auto mean = [&](int t) INL {
        const int m = (t & 1) + 2 * (t / 12), u = (t >> 2) % 3, up = (t >> 1) & 1;
        const s16x4 c = up ? __builtin_shufflevector(cs[u], cs[u], 4, 5, 6, 7) : __builtin_shufflevector(cs[u], cs[u], 0, 1, 2, 3);
        acc[m][u + 3 * up] = __builtin_amdgcn_mfma_f32_16x16x16bf16_1k(c, __builtin_bit_cast(s16x4, bm[m]), acc[m][u + 3 * up], 0, 0, 0);
      };
#pragma unroll
      for (int t = 0; t < 24; ++t) { mean(t); SB(); }
      unsigned seen;
#pragma unroll
      for (int mp = 0; mp < 2; ++mp) {
#pragma unroll
        for (int u = 0; u < 3; ++u) {
          unsigned o[2][2];
          const int q = mp * 3 + u;
          if (q == B9Q) bar();
          if (q == RD0 || q == RD1) {
            await(LANDED, seen, q == RD0 ? 1 : 2);
#pragma unroll
            for (int k = 0; k < 8; ++k) rdwd((q == RD0 ? 0 : 2) + (k >> 2), k & 3);
            post(USED, q == RD0 ? 1 : 2);
          }
          if (q == RD0 - 1 || q == RD1 - 1) { seen = peek(LANDED); SB(); }
#pragma unroll
          for (int mi = 0; mi < 2; ++mi) {
            const int m = mp * 2 + mi;
            const float sd = u2f(st[m][0]);
#pragma unroll
            for (int e = 0; e < 2; ++e) {
              const f32x2 gg = f32x2{acc[m][u][2 * e], acc[m][u][2 * e + 1]} * dup(kg[m]);
              const f32x2 dn = fma2(f32x2{__builtin_amdgcn_exp2f(gg[0]), __builtin_amdgcn_exp2f(gg[1])}, dup(sd), dup(sd));
              const f32x2 hv = f32x2{acc[m][u + 3][2 * e], acc[m][u + 3][2 * e + 1]} * f32x2{__builtin_amdgcn_rcpf(dn[0]), __builtin_amdgcn_rcpf(dn[1])};
              o[mi][e] = pk2(hv[0], hv[1]);
            }
          }
          const auto r0 = __builtin_amdgcn_permlane16_swap(o[0][0], o[1][0], false, false);
          const auto r1 = __builtin_amdgcn_permlane16_swap(o[0][1], o[1][1], false, false);
          const u32x4 ov = {r0[0], r1[0], r0[1], r1[1]};
          st16(ov, rws, lane * 16 + mp * (NU * 1024), so_h[u]);
          SB();
        }
      }
    } else {
      const int cj = (xcd & 3) * 2 + (r - 30), kg = (tid * 745) >> 14, cc = tid - kg * 22, col = cj * 176 + cc * 8;
      const unsigned vo = (kg < 11 && col < F) ? kg * (F * 2) + col * 2 : 0x7f000000u;
      u32x4 cv[2][8];
      auto ldcs = [&](int sl) INL {
#pragma unroll
        for (int i = 0; i < 8; ++i) cv[sl & 1][i] = __builtin_amdgcn_raw_buffer_load_b128(rw, vo + (sl * 8 + i) * (11 * F * 2), 0, 0);
      };
      ldcs(0);
      waitv<9>();
      layernorm([](int) INL {});
      f32x2 a[4] = {};
#pragma unroll
      for (int sl = 0; sl < 6; ++sl) {
        if (sl == 1) waitv<0>();
        if (sl < 5) ldcs(sl + 1);
        if (sl < 4) bar();
#pragma unroll
        for (int i = 0; i < 8; ++i)
#pragma unroll
          for (int e = 0; e < 4; ++e) { const unsigned q = cv[sl & 1][i][e]; a[e] += f32x2{blo(q), bhi(q)}; }
        asm volatile("" : "+v"(a[0]), "+v"(a[1]), "+v"(a[2]), "+v"(a[3]));
      }
      if (kg < 11) {
        *(LDS f32x4*)(lds + kg * 704 + cc * 32) = f32x4{a[0][0], a[0][1], a[1][0], a[1][1]};
        *(LDS f32x4*)(lds + kg * 704 + cc * 32 + 16) = f32x4{a[2][0], a[2][1], a[3][0], a[3][1]};
      }
      bar();
      if (tid < 176) {
        float s = 0.f;
#pragma unroll
        for (int k = 0; k < 11; ++k) s += *(const LDS float*)(lds + k * 704 + tid * 4);
        __builtin_amdgcn_raw_buffer_store_b64(u32x2{pk2(-s, rb(s) - s), (unsigned)tag}, rws, tid * 8, WS_CS + ((xcd >> 2) * CSP + cj * 176) * 8, SC1);
      }
      h_addr();
      bar(); bar(); bar(); bar();
#pragma unroll
      for (int u = 0; u < 4; ++u) rdxt(u);
      await(LANDED, 0, 2);
#pragma unroll
      for (int k = 0; k < 16; ++k) rdwd(k >> 2, k & 3);
      post(USED, 2);
      bar();
    }

    f32x4 a2[4][4] = {};
#pragma unroll
    for (int u = 0; u < 4; ++u) a2[0][u] = f32x4{blo(xt[u][0]), bhi(xt[u][0]), blo(xt[u][1]), bhi(xt[u][1])};
    unsigned a_p[3], a_y = SBASE + (4 * g + (j16 >> 2)) * 160 + w4 * 32 + (j16 & 3) * 8, a_o[2], v_o = (lane >> 3) * 1024 + (lane & 7) * 16;
#pragma unroll
    for (int m = 1; m < 4; ++m) a_p[m - 1] = wdloc(m - 1) + ((w4 - m) & 3) * 8192 + lane * 16;
    a_o[0] = wdloc(4) + wl + j16 * 144 + g * 8;
    a_o[1] = wdloc(4) + wl + (lane >> 3) * 144 + (lane & 7) * 16;
    asm volatile("" : "+v"(a2[0][0]), "+v"(a2[0][1]), "+v"(a2[0][2]), "+v"(a2[0][3]));
    asm volatile("" : "+v"(a_p[0]), "+v"(a_p[1]), "+v"(a_p[2]), "+v"(a_y), "+v"(a_o[0]), "+v"(a_o[1]), "+v"(v_o));
    waitv<0>();
    __builtin_amdgcn_raw_buffer_store_b64(u32x2{(unsigned)tag, (unsigned)(tag >> 32)}, rws, vflag, 0, SC1);
    __builtin_amdgcn_s_sleep(HSLEEP);
    if (!watch<1>(ws + vpoll, tag)) poll<NAP>(ws, vpoll + (lane & 7) * 8, tag);
    auto fs = [](int n) INL { return n < 32 ? n >> 2 : 8 + (n - 32) % 3; };
    auto fm = [](int n) INL { return ((n < 32 ? n : (n - 32) / 3) + 1) & 3; };
    s16x8 hf[44];
    auto ldh = [&](int n) INL {
      hf[n] = __builtin_bit_cast(s16x8, __builtin_amdgcn_raw_buffer_load_b128(rws, vh[fm(n)] + (fs(n) & 1) * 2048, fs(n) == 10 ? kh10 : kh + (fs(n) >> 1) * 4096, SC1));
    };
#pragma unroll
    for (int n = 0; n < HF; ++n) { ldh(n); SB(); }
    unsigned landed;
    u32x4 dn[2];
    SB();
#pragma unroll
    for (int n = 0; n < 44; ++n) {
      if (n == 2 || n == 22) landed = peek(LANDED);
      if (n == 4 || n == 24) await(LANDED, landed, n == 4 ? 3 : 4);
#pragma unroll
      for (int u = 0; u < 4; ++u) {
        a2[fm(n)][u] = mfma16(cat(wf[fs(n)][2 * u], wf[fs(n)][2 * u + 1]), hf[n], a2[fm(n)][u]);
        SB();
        if (u < 2 && n >= 4 && n < 32) rdwh((n >> 2) + 3, n & 3, u);
        if (u == 3 && n + HF < 44) ldh(n + HF);
        if (n >= 35 && (n - 35) % 3 == 0) *(LDS f32x4*)(lds + wdloc((n - 35) / 3) + wl + u * 1024 + lane * 16) = a2[(n - 35) / 3 + 1][u];
        SB();
      }
      if (n == 41) post(DONE, 1);
      if (n == 42) { dn[0] = *(volatile LDS u32x4*)(lds + DONE); dn[1] = *(volatile LDS u32x4*)(lds + DONE + 16); SB(); }
    }
    auto all4 = [](u32x4 d) INL { return rfl(d[0] & d[1] & d[2] & d[3]); };
#pragma unroll 1
    for (unsigned spins = 0; spins < LDS_CAP && !all4(dn[0]); ++spins) {
      dn[0] = *(volatile LDS u32x4*)(lds + DONE);
      dn[1] = *(volatile LDS u32x4*)(lds + DONE + 16);
    }
    fence();
    f32x4 pp[4][3];
    u32x2 y[4];
    auto rdy = [&]() INL {
#pragma unroll
      for (int u = 0; u < 4; ++u) y[u] = __builtin_bit_cast(u32x2, trd(lds + a_y + (u >> 1) * PAIR + (u & 1) * 4096));
    };
#pragma unroll
    for (int u = 0; u < 4; ++u)
#pragma unroll
      for (int m = 0; m < 3; ++m) pp[u][m] = *(const LDS f32x4*)(lds + a_p[m] + u * 1024);
    rdy();
    SB();
#pragma unroll
    for (int u = 0; u < 4; ++u) a2[0][u] += (pp[u][0] + pp[u][1]) + pp[u][2];
    asm volatile("" : "+v"(a2[0][0]), "+v"(a2[0][1]), "+v"(a2[0][2]), "+v"(a2[0][3]));
    if (__builtin_expect(!all4(dn[1]), 0)) {
#pragma unroll 1
      for (unsigned spins = 0; spins < LDS_CAP && !all4(*(volatile LDS u32x4*)(lds + DONE + 16)); ++spins) {}
      fence();
      rdy();
    }
#pragma unroll
    for (int u = 0; u < 4; ++u) {
      const f32x4 s = a2[0][u] + f32x4{blo(y[u][0]), bhi(y[u][0]), blo(y[u][1]), bhi(y[u][1])};
      *(LDS u32x2*)(lds + a_o[0] + u * 32) = u32x2{pk2(s[0], s[1]), pk2(s[2], s[3])};
    }
    fence();
    const u32x4 o0 = *(const LDS u32x4*)(lds + a_o[1]), o1 = *(const LDS u32x4*)(lds + a_o[1] + 8 * 144);
    st16(o0, mkr(out, L * D * 2), v_o, (m2 * 64 + w4 * 16) * 1024 + n2 * 128);
    st16(o1, mkr(out, L * D * 2), v_o, (m2 * 64 + w4 * 16 + 8) * 1024 + n2 * 128);
    asm volatile(".Lk_code_end0:");
  } else {
    const int cl = nw, hh = mw;
    addr_x();
    addr_w();
    asm volatile("" : "+v"(vx[0]), "+v"(vx[1]), "+v"(vw[0]), "+v"(vw[1]), "+v"(vw[2]));
    auto part = [&](int s, int i0, int i1) INL {
      if (__builtin_expect(gemm, 1)) {
#pragma unroll
        for (int i = i0; i < i1; ++i) issue(s, i);
      }
    };
    auto stage = [&](int s) INL { part(s, 0, 10); };
    unsigned vd[4], vcs, pa[4], vxi, vzero, vimg, vxf, vtw[2], vtab[2], vex[2], vyf, vys, vco, vxt[2], vyl, vyw;
#pragma unroll
    for (int i = 0; i < 4; ++i) { const int row = lane >> 3, f = ((row >> 1) & 1) + 2 * (i & 1); vd[i] = (w4 * 352 + i * 8 + row) * 1024 + n2 * 128 + (((lane & 7) ^ (2 * f)) << 4); }
    vcs = tid < 96 ? (n0 + tid) * 8 : (CSP + n0 + min(tid, 191) - 96) * 8;
#pragma unroll
    for (int i = 0; i < 4; ++i) pa[i] = SBASE + cl * PAIR + (j16 & 1 ? TX : TY) + (847 - 256 * i - hh * 64 - ((15 + j16 - 8 * g) >> 1)) * 4;
    vxi = (tid >> 1) * XNP + (tid & 1) * 16 + cp * 32;
    vimg = SBASE + (tid & 1) * PAIR + XIMG + (tid >> 1) * 16;
    vzero = lane * 16 + (hh ? SYNC : SBASE + cl * PAIR + (lane < 2 ? TY + 4096 : TX + 4064));
    vtab[0] = SBASE + cl * PAIR + (255 - (hh ? (lane >> 5 ? 64 : 128) + (lane & 31) : lane)) * 16;
    vtab[1] = vtab[0] - (hh ? 32 : 192) * 16;
    vex[0] = (hh ? SBASE + cl * PAIR + CEX : SLOT + LEFT + cl * 4096) + lane * 16;
    vex[1] = (hh ? SLOT + LEFT + cl * 4096 : SBASE + cl * PAIR + CEX) + lane * 16;
    vyf = lane < 4 ? WS_YF + ((lane & 2) * 2 + (hh ? 1 + (lane & 1) : 3 * (lane & 1))) * (256 * YST) + (ch >> 3) * YST + (ch & 7) * 8
                   : lane == 63 && b == 0 && w4 == 0 ? WS_E : 0x7f000000u;
    vys = ch * 4096 + (hh ? 1 + (g & 1) : 3 * (g & 1)) * 512 + (j16 >> 2) * 128 + (g >> 1) * 64 + (j16 & 3) * 16;
    vco = CSO + (tid < 96 ? tid * 16 : (tid - 96) * 16 + 8);
    vxt[0] = (m2 * 64 + w4 * 16 + (lane >> 3)) * 1024 + n2 * 128 + (((lane & 7) ^ g) << 4);
    vxt[1] = vxt[0] ^ 64;
    vyl = (n2 * 64 + w4 * 16 + (lane >> 3)) * 4096 + m2 * 128 + (lane & 7) * 16;
    vyw = SBASE + cl * PAIR + hh * 4096 + (lane >> 3) * 160 + (lane & 3) * 32 + (lane & 4) * 4;
    asm volatile("" : "+v"(vxt[0]), "+v"(vxt[1]), "+v"(vyl), "+v"(vyw));
    asm volatile("" : "+v"(vco), "+v"(vex[0]), "+v"(vex[1]), "+v"(vyf), "+v"(vys));
    vxf = tid ? 0x7f000000u : WS_XNF + b * 8;
    vtw[0] = lane < 11 ? (b * 43 + w4 * 11 + lane) * 128 : 0x7f000000u;
    vtw[1] = lane < 11 ? (r * 43 + w4 * 11 + lane) * 1024 + ((384u - (unsigned)(u64)wd) & 896u) : 0x7f000000u;
    asm volatile("" : "+v"(vd[0]), "+v"(vd[1]), "+v"(vd[2]), "+v"(vd[3]), "+v"(vcs), "+v"(pa[0]), "+v"(pa[1]), "+v"(pa[2]), "+v"(pa[3]));
    asm volatile("" : "+v"(vxi), "+v"(vimg), "+v"(vzero), "+v"(vxf), "+v"(vtw[0]), "+v"(vtw[1]), "+v"(vtab[0]), "+v"(vtab[1]));
    auto issue_wd = [&](int s, int p) INL {
      const int pos = s % 7;
      dma16(pos == 6 ? wl6 : wl, (pos == 6 ? 0 : wdloc(pos)) + p * 1024, vd[p], rwd, s * 32768);
    };
    __builtin_amdgcn_s_setprio(1);
    waitv<1>();
    get_tag();
    u32x2 tg = {(unsigned)tag, (unsigned)(tag >> 32)};
    asm volatile("" : "+v"(tg));
    float prm[6];
#pragma unroll
    for (int i = 0; i < 6; ++i) asm volatile("v_lshlrev_b32 %0, 16, %1" : "=v"(prm[i]) : "v"(xr[i >> 2][i & 3]));
    const float p_ldt = prm[0], p_lar = prm[1], p_aim = prm[2], p_cr = prm[3], p_ci = prm[4], skip = one(lane ? 0.f : prm[5]);
    const float dt = rb(exp_f(p_ldt));
    const float s2 = -rb(exp_f(p_lar)) * dt * 1.44269504f, th = p_aim * dt;
    const unsigned eb = min(max(wave_max(max(f2u(p_cr) << 1, f2u(p_ci) << 1)) >> 24, 1u), 253u);
    const float sc = u2f((254u - eb) << 23), ksc = 2.f * dt * u2f(eb << 23);
    f32x2 z1 = cpow2(s2, th, 1.f), z8c = cpow2(s2, -th, 8.f), zb = cpow2(s2, th, 256.f);
    asm volatile("" : "+v"(z1), "+v"(z8c), "+v"(zb));
    const cmulr z256 = mul_by(zb);
    LDS char* pr = lds + SBASE + cl * PAIR;
    LDS char* ks = pr + hh * KSW;
    auto put = [&](int row, f32x2 v) INL { *(LDS unsigned*)(ks + row * KROW + lane * 4) = pkh(v[0], v[1]); };
    auto putu = [&](int reg, int h, f32x2 v) INL { put((reg & 3) + 4 * h + 8 * (reg >> 2), v); };
    f16x8 vf[8];
    {
      f32x2 v[4];
      const cmulr m1 = mul_by(z8c);
      v[0] = f32x2{1.f, 0.f}; v[1] = z8c; v[2] = cmul(z8c, m1); v[3] = cmul(v[2], m1);
      const cmulr m4 = mul_by(cmul(v[3], m1));
#pragma unroll
      for (int k = 0; k < 32; ++k) {
        if (k == 4) v[0] = m4.z; else if (k > 4) v[k & 3] = cmul(v[k & 3], m4);
        put(k, v[k & 3]);
      }
      fence();
#pragma unroll
      for (int s = 0; s < 8; ++s) vf[s] = *(const LDS f16x8*)(ks + (lane & 31) * KROW + s * 32 + (lane >> 5) * 16);
      fence();
    }
    {
      f32x2 e[5];
      const cmulr m1 = mul_by(z1), m2_ = mul_by(cmul(z1, m1));
      e[0] = f32x2{p_cr, p_ci} * dup(sc);
      putu(0, 0, e[0]);
#pragma unroll
      for (int k = 0; k < 4; ++k) { putu(2 * k + 1, 0, cmul(e[k], m1)); e[k + 1] = cmul(e[k], m2_); putu(2 * k + 2, 0, e[k + 1]); }
#pragma unroll
      for (int k = 0; k < 5; ++k) e[k] = cmul(e[k], z256);
      putu(0, 1, e[0]); putu(2, 1, e[1]); putu(3, 1, e[2]); putu(4, 1, e[2]); putu(5, 1, e[2]); putu(6, 1, e[3]); putu(7, 1, e[4]); putu(8, 1, e[4]);
#pragma unroll
      for (int k = 0; k < 6; ++k) {
        const int reg = k < 2 ? 9 + 2 * k : k < 5 ? 5 + 2 * k : 13, h = k < 2 || k == 5;
        e[0] = cmul(e[0], z256); putu(reg, h, e[0]);
        e[4] = cmul(e[4], z256); putu(reg + 1, h, e[4]);
        if (k < 2) { e[2] = cmul(e[2], z256); putu(k ? 1 : 15, 1, e[2]); }
        if (k == 5) touch(DUMMY, vtw[0], rwd, 0);
      }
      fence();
    }
    bar();
    f32x16 kd = {};
    {
      f16x8 uf[8];
#pragma unroll
      for (int s = 0; s < 8; ++s) uf[s] = *(const LDS f16x8*)(ks + (lane & 31) * KROW + s * 32 + (lane >> 5) * 16);
      SB();
#pragma unroll
      for (int s = 0; s < 8; ++s) kd = __builtin_amdgcn_mfma_f32_32x32x16_f16(uf[s], vf[s], kd, 0, 0, 0);
    }
    kd *= ksc;
    const bool h = lane >> 5, odd = lane & 1;
    if (hh) {
      *(LDS f32x4*)(pr + KEX1 + lane * 16) = f32x4{kd[0], kd[1], kd[2], kd[3]};
      *(LDS f32x4*)(pr + KEX1 + 1024 + lane * 16) = f32x4{kd[4], kd[5], kd[6], kd[7]};
      *(LDS f32x4*)(pr + KEX1 + 2048 + lane * 16) = f32x4{kd[8], kd[13], kd[14], kd[15]};
    } else {
      *(LDS f32x4*)(pr + KEX0 + lane * 16) = f32x4{kd[9], kd[10], kd[11], kd[12]};
      *(LDS f32x2*)(pr + KEX0 + 1024 + lane * 8) = f32x2{kd[15], kd[1]};
    }
    __builtin_amdgcn_s_setprio(0);
    bar();
    {
      u32x4 tb[5];
      auto tail = [&](float a, float c, float a3, float a4, float a5, int k) INL {
        const unsigned aa = pk2(a, a), cc = pk2(c, c);
        tb[k] = u32x4{cc, pk2(a5, a4), pk2(a3, a), aa};
        tb[k + 1] = u32x4{cc, pk2(c, a5), pk2(a4, a3), aa};
      };
      if (hh) {
        const f32x4 p = *(const LDS f32x4*)(pr + KEX0 + lane * 16);
        const f32x2 q = *(const LDS f32x2*)(pr + KEX0 + 1024 + lane * 8);
#pragma unroll
        for (int k = 0; k < 2; ++k) {
          const float a = one(kd[9 + 2 * k] + p[2 * k]), c = one(kd[10 + 2 * k] + p[2 * k + 1]), m = one(kd[k ? 1 : 15] + q[k]);
          tail(a, c, h ? m : a, h ? m : odd ? c : a, h ? m : c, 2 * k);
        }
        tb[4][0] = 0;
      } else {
        const f32x4 p0 = *(const LDS f32x4*)(pr + KEX1 + lane * 16), p1 = *(const LDS f32x4*)(pr + KEX1 + 1024 + lane * 16);
        const f32x4 p2 = *(const LDS f32x4*)(pr + KEX1 + 2048 + lane * 16);
        float t[9];
#pragma unroll
        for (int k = 0; k < 9; ++k) t[k] = one(kd[k] + (k < 4 ? p0[k] : k < 8 ? p1[k - 4] : p2[0]));
        t[0] += skip;
        t[1] = h ? t[0] : t[1];
        tb[0] = u32x4{pk2(t[7], t[6]), pk2(t[5], t[4]), pk2(t[3], t[2]), pk2(t[1], t[0])};
        tb[1] = u32x4{pk2(t[8], t[7]), pk2(t[6], t[5]), pk2(t[4], t[3]), pk2(t[2], t[1])};
        const float a = one(kd[13] + p2[1]), c = one(kd[14] + p2[2]);
        tail(a, c, a, odd ? c : a, c, 2);
        tb[4][0] = lane ? 0 : tb[0][3] >> 16;
      }
      tb[4][1] = tb[4][2] = tb[4][3] = 0;
      asm volatile("" : "+v"(tb[0]), "+v"(tb[1]), "+v"(tb[2]), "+v"(tb[3]), "+v"(tb[4]));
#pragma unroll
      for (int k = 0; k < 4; ++k) *(LDS u32x4*)(lds + vtab[k >> 1] + (k & 1 ? TY : TX)) = tb[k];
      if (lane < (hh ? 5 : 36)) *(LDS u32x4*)(lds + vzero) = tb[4];
      part(2, 0, 10 - D2F);
    }
    bar();
    __builtin_amdgcn_raw_buffer_store_b64(tg, rws, vxf, 0, SC1);
    stage(3);
    waitv<10>();
    bar();
    stage(4);
    waitv<10>();
    bar();
    stage(5);
    waitv<10>();
    bar();
    stage(6);
    touch(DUMMY, vtw[1], rwd, 0);
    waitv<11>();
    bar();
    {
      u64 fl;
      asm volatile("buffer_load_dwordx2 %0, %1, %2, %3 offen sc1" : "=v"(fl) : "v"(tid * 8), "s"(rws), "s"(WS_XNF) : "memory");
      stage(7);
      if (gemm) waitv<10>(); else waitv<0>();
      asm volatile("" : "+v"(fl));
      if (__builtin_expect(!wave_all(fl == tag), 0)) poll<4>(ws, WS_XNF + tid * 8, tag);
    }
    bar();
#pragma unroll
    for (int i = 0; i < 2; ++i) dma16(cl * PAIR + hh * 2048, SBASE + CEX + i * 1024, vxt[i], rx, i * 8192);
    unsigned gst; u32x2 csg; u32x4 xi[2];
    asm volatile("buffer_load_dword %0, %1, %2, %3 offen sc1" : "=v"(gst) : "v"((tid & 127) * 4), "s"(rws), "s"(WS_ST + (mt & 15) * 512) : "memory");
    asm volatile("buffer_load_dwordx2 %0, %1, %2, %3 offen sc1" : "=v"(csg) : "v"(vcs), "s"(rws), "s"(WS_CS) : "memory");
#pragma unroll
    for (int i = 0; i < 2; ++i)
      asm volatile("buffer_load_dwordx4 %0, %1, %2, %3 offen sc1" : "=v"(xi[i]) : "v"(vxi), "s"(rws), "s"(WS_XNT + i * (128 * XNP)) : "memory");
#pragma unroll
    for (int i = 0; i < 4; ++i) issue_wd(0, i);
    waitv<10>();
    bar();
    waitv<6>();
    asm volatile("" : "+v"(gst), "+v"(csg));
    if (tid < 128) *(LDS u32x2*)(lds + STO + tid * 8) = u32x2{gst & 0xffff0000u, __builtin_amdgcn_perm(gst, gst, 0x01000100)};
    if (__builtin_expect(gemm && !wave_all(csg[1] == (unsigned)tag), 0)) {
#pragma unroll 1
      for (unsigned spins = 0; spins < SPIN_CAP && !wave_all(csg[1] == (unsigned)tag); ++spins) {
        __builtin_amdgcn_s_sleep(2);
        asm volatile("buffer_load_dwordx2 %0, %1, %2, %3 offen sc1\n\ts_waitcnt vmcnt(0)" : "=&v"(csg) : "v"(vcs), "s"(rws), "s"(WS_CS) : "memory");
      }
    }
    if (tid < 192) *(LDS u32x2*)(lds + vco) = u32x2{csg[0], csg[0]};
#pragma unroll
    for (int i = 0; i < 4; ++i) issue_wd(1, i);
    bar();
#pragma unroll
    for (int i = 8; i < 16; ++i) issue_wd(i >> 2, i & 3);
    waitv<8>();
    post(LANDED, 1);
    asm volatile("" : "+v"(xi[0]), "+v"(xi[1]));
#pragma unroll
    for (int i = 0; i < 2; ++i) *(LDS u32x4*)(lds + vimg + XPAD + i * 2048) = xi[i];
#pragma unroll
    for (int i = 16; i < 28; ++i) issue_wd(i >> 2, i & 3);
    waitv<12>();
    post(LANDED, 2);
    if (NPRE > 12) await(USED, 0, 1);
#pragma unroll
    for (int i = 28; i < 16 + NPRE; ++i) issue_wd(i >> 2, i & 3);
    LDS char* pb = pr + XIMG + XPAD + (16 * j16 + 8 * g - 16) * 2 - hh * 256 - 192;
    CvFrag cf[2];
    auto rdc = [&](CvFrag& f, int rr, int k) INL {
      const int i = k / 3, c = k % 3;
      if (c == 2) f.b[i] = *(const LDS s16x8*)(pb + i * 512 + (3 - rr) * 64);
      else f.a[2 * i + c] = *(const LDS s16x4u*)(lds + pa[i >> 1] + (i & 1 ? 0 : 512) + (3 - rr) * 64 + c * 8);
    };
#pragma unroll
    for (int k = 0; k < 24; ++k) if (k % 3 < 2) rdc(cf[0], 0, k);
    bar();
#pragma unroll
    for (int k = 2; k < 24; k += 3) rdc(cf[0], 0, k);
    __builtin_amdgcn_s_setprio(1);
    unsigned seen = 0;
    f32x4 ca[8] = {};
    auto conv = [&](int rr) INL {
      int n = 0;
#pragma unroll
      for (int u = 0; u < 8; ++u)
#pragma unroll
        for (int t = u; t < 8; ++t) {
          ca[t] = mfma16(cat(cf[rr & 1].a[2 * u], cf[rr & 1].a[2 * u + 1]), cf[rr & 1].b[t - u], ca[t]);
          SB();
          if (n < 32 && n % 4 < 3) rdc(cf[(rr + 1) & 1], rr + 1, n / 4 * 3 + n % 4);
          const int q = rr * 36 + n;
          const int j = 16 + NPRE + (q - Q0) / PACE;
          if (q >= Q0 && (q - Q0) % PACE == 0 && j < 44) {
            if (j == 28 || j == 36) await(USED, seen, j == 28 ? 1 : 2);
            if (j == 27 || j == 35) seen = peek(USED);
            issue_wd(j >> 2, j & 3);
          }
          SB();
          ++n;
        }
    };
    conv(0); conv(1); conv(2);
    {
      unsigned o[4][2];
      auto last = [&](int h) INL {
        auto tile = [](int set, int k) INL { return (k >> 1) * 4 + (set ? 1 + (k & 1) : 3 * (k & 1)); };
        unsigned mate;
        f32x4 in[4];
#pragma unroll
        for (int ph = 0; ph < 2; ++ph) {
          int n = 0;
#pragma unroll
          for (int u = 0; u < 8; ++u)
#pragma unroll
            for (int k = 3; k >= 0; --k) {
              const int t = tile(ph ? h : 1 - h, k);
              if (t < u) continue;
              ca[t] = mfma16(cat(cf[1].a[2 * u], cf[1].a[2 * u + 1]), cf[1].b[t - u], ca[t]);
              SB();
              if (ph && n < 4) *(LDS f32x4*)(lds + vex[0] + n * 1024) = ca[tile(1 - h, n)];
              if (ph && n == 4) post(SYNC, 1);
              if (ph && n == 6) { waitv<8>(); post(LANDED, 3); }
              if (ph && n == 10) mate = *(volatile LDS unsigned*)(lds + SYNC + (w4 ^ 1) * 4);
              SB();
              ++n;
            }
        }
        if (__builtin_expect(!rfl(mate), 0)) lds_wait(lds + SYNC + (w4 ^ 1) * 4, 1);
        fence();
#pragma unroll
        for (int k = 0; k < 4; ++k) in[k] = *(const LDS f32x4*)(lds + vex[1] + k * 1024);
        SB();
#pragma unroll
        for (int k = 0; k < 4; ++k) {
          const f32x4 s = ca[tile(h, k)] + in[k];
          o[k][0] = pk2(s[0], s[1]); o[k][1] = pk2(s[2], s[3]);
        }
      };
      if (hh) last(1); else last(0);
      __builtin_amdgcn_s_setprio(1);
#pragma unroll
      for (int kp = 0; kp < 2; ++kp) {
        const auto r0 = __builtin_amdgcn_permlane16_swap(o[2 * kp][0], o[2 * kp + 1][0], false, false);
        const auto r1 = __builtin_amdgcn_permlane16_swap(o[2 * kp][1], o[2 * kp + 1][1], false, false);
        const u32x4 ov = {r0[0], r1[0], r0[1], r1[1]};
        st16(ov, rws, vys, WS_YT + kp * 2048);
      }
      waitv<2>();
      post(LANDED, 4);
      waitv<0>();
      __builtin_amdgcn_raw_buffer_store_b64(tg, rws, vyf, 0, SC1);
    }
    __builtin_amdgcn_s_sleep(YSLEEP);
    const unsigned yf = WS_YF + ((m2 & -4) * 64 + n2 * 8 + w4 * 2) * YST;
    if (!watch<2>(ws + yf, tag)) poll<NAP>(ws, yf + (j16 >> 3) * YST + (j16 & 7) * 8, tag);
#pragma unroll
    for (int i = 0; i < 2; ++i) xi[i] = __builtin_amdgcn_raw_buffer_load_b128(rws, vyl, WS_YT + i * 32768, SC1);
#pragma unroll
    for (int i = 0; i < 2; ++i) *(LDS u32x4*)(lds + vyw + i * 1280) = xi[i];
    post(DONE + 16, 1);
    waitl0();
    asm volatile(".Lk_code_end1:");
  }
}

}

extern "C" void kernel_launch(void* const* d_in, const int* in_sizes, int n_in, void* d_out, int out_size, void* d_ws, size_t ws_size,
                              hipStream_t stream) {
  fused_kernel<<<256, 512, 0, stream>>>((const u16*)d_in[0], (const u16*)d_in[1], (const u16*)d_in[2], (const u16*)d_in[3], (const u16*)d_in[4],
                                        (const u16*)d_in[5], (const u16*)d_in[6], (const u16*)d_in[8], (const u16*)d_in[9], (const u16*)d_in[10],
                                        (u16*)d_out, (char*)d_ws);
}
```
